```python
import torch

def _softmax(x, axis=-1):
    return torch.softmax(x, dim=axis)
def _gelu(x):
    return torch.nn.functional.gelu(x, approximate="tanh")
def _silu(x):
    return torch.nn.functional.silu(x)
def _sigmoid(x):
    return torch.sigmoid(x)
def _layernorm(x, axis=-1, eps=1e-5):
    m = x.mean(dim=axis, keepdim=True)
    v = x.var(dim=axis, keepdim=True, unbiased=False)
    return (x - m) / torch.sqrt(v + eps)
def _scatter_add(tgt, idx, src, axis=0):
    return tgt.clone().index_add_(axis, idx, src)
def _segment_sum(src, seg, n):
    out = torch.zeros((n,) + src.shape[1:], dtype=src.dtype)
    return out.index_add_(0, seg, src)
def _pad(x, pw):
    flat = tuple(v for pair in reversed(pw) for v in pair)
    return torch.nn.functional.pad(x, flat)

kernel_name = 'gated_mix__linear_attn_block__ssm_conv_s4d__prompt'

def setup_inputs():

    _w = (lambda a: (lambda t: t.to(torch.bfloat16) if t.is_floating_point() else t)(torch.as_tensor(a)))
    def _A_setup():
        B, S, D = 1, 2048, 512
        return {
            "x": _w((lambda *s: torch.randn(*s, dtype=torch.bfloat16) * 0.05)(B, S, D)),
            "wqkv": _w((lambda *s: torch.randn(*s, dtype=torch.bfloat16) * 0.8)(D, 3 * D)),
            "wo": _w((lambda *s: torch.randn(*s, dtype=torch.bfloat16) * 0.05)(D, D)),
        }
    _A_in = _A_setup()

    def _B_setup():
        Bsz, L, D, N = 1, 2048, 512, 128
        return {
            "u": _w((lambda *s: torch.randn(*s, dtype=torch.bfloat16) * 0.05)(Bsz, L, D)),
            "log_A_real": _w((lambda *s: torch.randn(*s, dtype=torch.bfloat16) * 6.4)(D, N) * 0.5),
            "A_imag": _w((lambda *s: torch.randn(*s, dtype=torch.bfloat16) * 0.05)(D, N) * 3.14),
            "C_real": _w((lambda *s: torch.randn(*s, dtype=torch.bfloat16) * 0.2)(D, N) / N ** 0.5),
            "C_imag": _w((lambda *s: torch.randn(*s, dtype=torch.bfloat16) * 0.2)(D, N) / N ** 0.5),
            "log_dt": _w((lambda *s: torch.randn(*s, dtype=torch.bfloat16) * 0.05)(D) * 0.5 - 4.0),
            "D_skip": _w((lambda *s: torch.randn(*s, dtype=torch.bfloat16) * 0.05)(D)),
        }
    _B_in = _B_setup()

    return {
        "A_x": _A_in["x"],
        "A_wqkv": _A_in["wqkv"],
        "A_wo": _A_in["wo"],
        "B_u": _B_in["u"],
        "B_log_A_real": _B_in["log_A_real"],
        "B_A_imag": _B_in["A_imag"],
        "B_C_real": _B_in["C_real"],
        "B_C_imag": _B_in["C_imag"],
        "B_log_dt": _B_in["log_dt"],
        "B_D_skip": _B_in["D_skip"],
        "gate_w": _w((lambda *s: torch.randn(*s, dtype=torch.bfloat16) * 0.05)(512) * 16.0),
    }

def reference(A_x, A_wqkv, A_wo, B_u, B_log_A_real, B_A_imag, B_C_real, B_C_imag, B_log_dt, B_D_skip, gate_w):
    def _A_ref(x, wqkv, wo):
        B, S, D = x.shape
        H = 32; Dh = D // H
        qkv = (lambda x, *d: x.permute(*d))((x @ wqkv).reshape(B, S, 3, H, Dh), 2, 0, 3, 1, 4)
        q, k, v = qkv[0], qkv[1], qkv[2]
        phi = lambda t: torch.where(t > 0, t + 1, torch.exp(t)) + 1e-6
        pq, pk = phi(q), phi(k)
        kv = torch.einsum("bhsd,bhse->bhde", pk, v)
        z = torch.einsum("bhsd->bhd", pk)
        num = torch.einsum("bhqd,bhde->bhqe", pq, kv)
        den = torch.einsum("bhqd,bhd->bhq", pq, z)[..., None] + 1e-6
        o = num / den
        return (lambda x, *d: x.permute(*d))(o, 0, 2, 1, 3).reshape(B, S, D) @ wo

    def _B_ref(u, log_A_real, A_imag, C_real, C_imag, log_dt, D_skip):
        L = u.shape[1]
        dt = torch.exp(log_dt)
        A = -torch.exp(log_A_real) + 1j * A_imag
        C = (C_real + 1j * C_imag) * dt[:, None]
        pos = (lambda x, dt: x.to(dt))(torch.arange(L), log_dt.dtype)
        P = torch.exp((A * dt[:, None])[:, :, None] * pos[None, None, :])
        K = 2.0 * torch.einsum("dn,dnl->dl", C, P).real
        L2 = 2 * L
        u_t = (lambda x, *d: x.permute(*d))(u, 0, 2, 1)
        Kf = (lambda x, *a, **k: torch.fft.rfft(x.float(), *a, **k))(K, L2)
        Uf = (lambda x, *a, **k: torch.fft.rfft(x.float(), *a, **k))(u_t, L2)
        y = (lambda x, *a, **k: torch.fft.irfft(x, *a, **k).to(torch.bfloat16))(Uf * Kf[None, :, :], L2)[:, :, :L]
        y = (lambda x, *d: x.permute(*d))(y, 0, 2, 1)
        return y + u * D_skip[None, None, :]

    x = A_x
    g = _sigmoid((x * gate_w).sum(-1, keepdims=True))
    a = _A_ref(x, A_wqkv, A_wo)
    b = _B_ref(x, B_log_A_real, B_A_imag, B_C_real, B_C_imag, B_log_dt, B_D_skip)
    return g * a + (1 - g) * b
```

```cpp
#include <hip/hip_runtime.h>
#include <hip/hip_bf16.h>
#include <stdint.h>

typedef unsigned short u16;
__device__ __forceinline__ float bf2f(u16 v) { return __uint_as_float(((unsigned)v) << 16); }
__device__ __forceinline__ u16 f2bf(float x) { unsigned u = __float_as_uint(x); u += 0x7fffu + ((u >> 16) & 1u); return (u16)(u >> 16); }
__device__ __forceinline__ float rbf(float x) { return bf2f(f2bf(x)); }

constexpr int S = 2048, D = 512, H = 32, DH = 16, N = 128;

__global__ void k_qkv(const u16* x, const u16* w, u16* qkv) {
  int n = blockIdx.x * 256 + threadIdx.x;
  int s = blockIdx.y;
  float acc = 0.f;
  for (int k = 0; k < D; ++k) acc += bf2f(x[s * D + k]) * bf2f(w[k * 1536 + n]);
  float t = rbf(acc);
  if (n < 1024) {
    float p = t > 0.f ? rbf(t + 1.f) : rbf(expf(t));
    t = rbf(p + 1e-6f);
  }
  qkv[s * 1536 + n] = f2bf(t);
}
__global__ void k_kv(const u16* qkv, float* kv, float* z) {
  int h = blockIdx.x; int d = threadIdx.x >> 4, e = threadIdx.x & 15;
  float acc = 0.f, zz = 0.f;
  for (int s = 0; s < S; ++s) {
    float pk = bf2f(qkv[s * 1536 + 512 + h * 16 + d]);
    float v = bf2f(qkv[s * 1536 + 1024 + h * 16 + e]);
    acc += pk * v; zz += pk;
  }
  kv[h * 256 + d * 16 + e] = rbf(acc);
  if (e == 0) z[h * 16 + d] = rbf(zz);
}
__global__ void k_o(const u16* qkv, const float* kv, const float* z, u16* o) {
  int s = blockIdx.x; int c = threadIdx.x; int h = c >> 4, e = c & 15;
  float num = 0.f, den = 0.f;
  for (int d = 0; d < 16; ++d) {
    float pq = bf2f(qkv[s * 1536 + h * 16 + d]);
    num += pq * kv[h * 256 + d * 16 + e];
    den += pq * z[h * 16 + d];
  }
  num = rbf(num); den = rbf(rbf(den) + 1e-6f);
  o[s * D + c] = f2bf(num / den);
}
__global__ void k_K(const u16* lar, const u16* aim, const u16* cr, const u16* ci, const u16* ldt, float* K) {
  int d = blockIdx.y; int l = blockIdx.x * 256 + threadIdx.x;
  float dt = rbf(expf(bf2f(ldt[d])));
  float pos = rbf((float)l);
  float acc = 0.f;
  for (int n = 0; n < N; ++n) {
    float ar = -rbf(expf(bf2f(lar[d * N + n]))) * dt;
    float ai = bf2f(aim[d * N + n]) * dt;
    float c_r = bf2f(cr[d * N + n]) * dt, c_i = bf2f(ci[d * N + n]) * dt;
    float m = expf(ar * pos);
    float sn, cs; sincosf(ai * pos, &sn, &cs);
    acc += c_r * m * cs - c_i * m * sn;
  }
  K[d * S + l] = 2.f * acc;
}
__global__ void k_conv(const u16* x, const float* K, const u16* dsk, float* b) {
  int d = blockIdx.y; int t = blockIdx.x * 256 + threadIdx.x;
  float acc = 0.f;
  for (int l = 0; l <= t; ++l) acc += K[d * S + l] * bf2f(x[(t - l) * D + d]);
  float y = rbf(acc);
  b[t * D + d] = rbf(y + rbf(bf2f(x[t * D + d]) * bf2f(dsk[d])));
}
__global__ void k_out(const u16* x, const u16* o, const u16* wo, const u16* gw, const float* b, u16* out) {
  int s = blockIdx.x; int c = threadIdx.x;
  float gs = 0.f, a = 0.f;
  for (int k = 0; k < D; ++k) { gs += rbf(bf2f(x[s * D + k]) * bf2f(gw[k])); a += bf2f(o[s * D + k]) * bf2f(wo[k * D + c]); }
  gs = rbf(gs);
  float g = rbf(1.f / (1.f + expf(-gs)));
  a = rbf(a);
  float r = rbf(rbf(g * a) + rbf(rbf(1.f - g) * b[s * D + c]));
  out[s * D + c] = f2bf(r);
}

extern "C" void kernel_launch(void* const* d_in, const int* in_sizes, int n_in,
                              void* d_out, int out_size, void* d_ws, size_t ws_size,
                              hipStream_t stream) {
  const u16* x = (const u16*)d_in[0];
  const u16* wqkv = (const u16*)d_in[1];
  const u16* wo = (const u16*)d_in[2];
  const u16* lar = (const u16*)d_in[4];
  const u16* aim = (const u16*)d_in[5];
  const u16* cr = (const u16*)d_in[6];
  const u16* ci = (const u16*)d_in[7];
  const u16* ldt = (const u16*)d_in[8];
  const u16* dsk = (const u16*)d_in[9];
  const u16* gw = (const u16*)d_in[10];
  char* ws = (char*)d_ws;
  u16* qkv = (u16*)ws;
  float* kv = (float*)(ws + (8 << 20));
  float* z = (float*)(ws + (9 << 20));
  u16* o = (u16*)(ws + (10 << 20));
  float* K = (float*)(ws + (16 << 20));
  float* b = (float*)(ws + (24 << 20));
  k_qkv<<<dim3(6, S), 256, 0, stream>>>(x, wqkv, qkv);
  k_kv<<<H, 256, 0, stream>>>(qkv, kv, z);
  k_o<<<S, 512, 0, stream>>>(qkv, kv, z, o);
  k_K<<<dim3(8, D), 256, 0, stream>>>(lar, aim, cr, ci, ldt, K);
  k_conv<<<dim3(8, D), 256, 0, stream>>>(x, K, dsk, b);
  k_out<<<S, 512, 0, stream>>>(x, o, wo, gw, b, (u16*)d_out);
}
```

```cpp
#include <hip/hip_runtime.h>
#include <stdint.h>

typedef unsigned short u16;
typedef unsigned long long u64;
typedef __attribute__((ext_vector_type(8))) __bf16 bf16x8;
typedef __attribute__((ext_vector_type(2))) __bf16 bf16x2;
typedef __attribute__((ext_vector_type(8))) _Float16 f16x8;
typedef __attribute__((ext_vector_type(2))) _Float16 f16x2;
typedef __attribute__((ext_vector_type(4))) short s16x4;
typedef __attribute__((ext_vector_type(16))) float f32x16;
typedef __attribute__((ext_vector_type(4))) float f32x4;
typedef __attribute__((ext_vector_type(2))) float f32x2;
typedef __attribute__((ext_vector_type(4))) unsigned u32x4;
typedef __attribute__((ext_vector_type(2))) unsigned u32x2;
#define LAS __attribute__((address_space(3)))
#define CAS __attribute__((address_space(4)))
#define DI __device__ __forceinline__

constexpr unsigned WS_PQF = 0;
constexpr unsigned WS_BT  = 2u << 20;
constexpr unsigned WS_KVP = 4u << 20;
constexpr unsigned WS_KVZ = 5u << 20;
constexpr unsigned WS_G   = (5u << 20) + 65536;
constexpr unsigned WS_USED = 6u << 20;

constexpr float LOG2E = 1.4426950408889634f;

#define STAMP_DECL
#define STAMP(i)
#define STAMP_FLUSH
#define SB __builtin_amdgcn_sched_barrier(0)
#define PIN(x) asm volatile("" : "+v"(x))
#define PINS(x) asm volatile("" : "+s"(x))

typedef LAS char* lptr;
DI float bf2f(unsigned v) { return __uint_as_float(v << 16); }
DI float bfhi(unsigned v) { return __uint_as_float(v & 0xffff0000u); }
DI unsigned pk_bf16(float a, float b) { f32x2 v = {a, b}; return __builtin_bit_cast(unsigned, __builtin_convertvector(v, bf16x2)); }
DI float rbf(float a) { return __uint_as_float(pk_bf16(a, a) << 16); }
DI f16x2 pk_f16(f32x2 v) { return __builtin_convertvector(v, f16x2); }
DI f32x2 cmul(f32x2 a, f32x2 b) { f32x2 t = a.xx * b; f32x2 bs = {-b.y, b.x}; return __builtin_elementwise_fma(a.yy, bs, t); }
DI void cmul2(f32x2& r0, f32x2 a0, f32x2 b0, f32x2& r1, f32x2 a1, f32x2 b1) {
  f32x2 t0, t1;
  asm("v_pk_mul_f32 %0, %2, %3 op_sel_hi:[0,1]\n\tv_pk_mul_f32 %1, %4, %5 op_sel_hi:[0,1]\n\t"
      "v_pk_fma_f32 %0, %2, %3, %0 op_sel:[1,1,0] op_sel_hi:[1,0,1] neg_lo:[0,1,0]\n\t"
      "v_pk_fma_f32 %1, %4, %5, %1 op_sel:[1,1,0] op_sel_hi:[1,0,1] neg_lo:[0,1,0]"
      : "=&v"(t0), "=&v"(t1) : "v"(a0), "v"(b0), "v"(a1), "v"(b1));
  r0 = t0; r1 = t1;
}
DI f16x2 pk_f16c(f32x2 v) { const f32x2 c = {v.x, -v.y}; return __builtin_convertvector(c, f16x2); }
DI f16x2 cmulh(f16x2 a, f16x2 b) { f16x2 t = a.xx * b; f16x2 bs = {-b.y, b.x}; return __builtin_elementwise_fma(a.yy, bs, t); }
DI u32x4 mk_rsrc(const void* p, unsigned bytes) { const u64 a = (u64)p; return u32x4{(unsigned)a, (unsigned)(a >> 32), bytes, 0x00020000u}; }
DI void dma16(u32x4 rs, unsigned voff, unsigned soff, lptr l) {
  asm volatile("s_mov_b32 m0, %3\n\ts_nop 0\n\tbuffer_load_dwordx4 %0, %1, %2 offen lds"
               :: "v"(voff), "s"(rs), "s"(soff), "s"((unsigned)(size_t)l) : "memory");
}
DI int sig(int w, int n) { return 32 * ((n >> 3) & 1) + ((n >> 2) & 1) + 16 * w + 8 * (n >> 4) + 2 * (n & 3); }
DI bf16x8 lds_b128(lptr p) { return *(const LAS bf16x8*)p; }
DI s16x4 lds_tr(lptr p) { return __builtin_amdgcn_ds_read_tr16_b64_v4i16((LAS s16x4*)p); }
DI bf16x8 lds_tr2(lptr p0, lptr p1) { s16x4 a = lds_tr(p0), b = lds_tr(p1); return __builtin_bit_cast(bf16x8, __builtin_shufflevector(a, b, 0, 1, 2, 3, 4, 5, 6, 7)); }
DI f32x16 mfma32(bf16x8 a, bf16x8 b, f32x16 c) { return __builtin_amdgcn_mfma_f32_32x32x16_bf16(a, b, c, 0, 0, 0); }
DI f32x4 mfma16(bf16x8 a, bf16x8 b, f32x4 c) { return __builtin_amdgcn_mfma_f32_16x16x32_bf16(a, b, c, 0, 0, 0); }
DI f32x4 mfma16h(f16x8 a, f16x8 b, f32x4 c) { return __builtin_amdgcn_mfma_f32_16x16x32_f16(a, b, c, 0, 0, 0); }
DI void bar() { asm volatile("" ::: "memory"); __builtin_amdgcn_s_barrier(); asm volatile("" ::: "memory"); }
DI void lgkm0() { asm volatile("s_waitcnt lgkmcnt(0)" ::: "memory"); }
#define WAIT_VM(n) asm volatile("s_waitcnt vmcnt(" #n ")" ::: "memory")
DI float phi(float tp) {
  const float e = __builtin_amdgcn_fmed3f(__builtin_amdgcn_exp2f(tp * LOG2E), 0.f, 1.f);
  const int ti = __float_as_int(tp);
  return __int_as_float(ti > 0x358637bd ? ti : 0x358637bd) + e;
}
DI bf16x8 pack8(const f32x16& a, int o) {
  u32x4 p = {pk_bf16(a[o], a[o + 1]), pk_bf16(a[o + 2], a[o + 3]), pk_bf16(a[o + 4], a[o + 5]), pk_bf16(a[o + 6], a[o + 7])};
  return __builtin_bit_cast(bf16x8, p);
}
template <int CTRL, int RM = 0xf> DI int dppi(int v) { return __builtin_amdgcn_update_dpp(0, v, CTRL, RM, 0xf, true); }
template <int CTRL, int RM = 0xf> DI float dppf(float v) { return __int_as_float(dppi<CTRL, RM>(__float_as_int(v))); }
DI unsigned umax(unsigned a, unsigned b) { return a > b ? a : b; }
DI unsigned wave_umax(unsigned v) {
  v = umax(v, dppi<0x111>(v)); v = umax(v, dppi<0x112>(v)); v = umax(v, dppi<0x114>(v)); v = umax(v, dppi<0x118>(v));
  v = umax(v, dppi<0x142, 0xa>(v)); v = umax(v, dppi<0x143, 0xc>(v));
  return __builtin_amdgcn_readlane(v, 63);
}
DI float wave_sum(float v) {
  v += dppf<0x111>(v); v += dppf<0x112>(v); v += dppf<0x114>(v); v += dppf<0x118>(v);
  v += dppf<0x142, 0xa>(v); v += dppf<0x143, 0xc>(v);
  return __int_as_float(__builtin_amdgcn_readlane(__float_as_int(v), 63));
}
typedef __amdgpu_buffer_rsrc_t rsrc_t;
DI rsrc_t make_rsrc(void* p, unsigned bytes) { return __builtin_amdgcn_make_buffer_rsrc(p, 0, bytes, 0x00020000); }
constexpr int WT = 16;
DI void st128(rsrc_t r, unsigned off, u32x4 v) { __builtin_amdgcn_raw_buffer_store_b128(v, r, off, 0, WT); }
DI void st64(rsrc_t r, unsigned off, u32x2 v) { __builtin_amdgcn_raw_buffer_store_b64(v, r, off, 0, WT); }
DI void st32(rsrc_t r, unsigned off, unsigned v) { __builtin_amdgcn_raw_buffer_store_b32(v, r, off, 0, WT); }

constexpr int SLOT = 32768, NSLOT = 4;
constexpr int GA_EX = NSLOT * SLOT;
constexpr int GA_GW = GA_EX + 8 * 544 * 4;

DI void gemm_role(lptr lds, const u16* __restrict__ x, const u16* __restrict__ w, const u16* __restrict__ gw, char* ws,
                  int rb, int cg, int rot, u64* stamps) {
  STAMP_DECL;
  const int tid = threadIdx.x, lane = tid & 63;
  const int wv = __builtin_amdgcn_readfirstlane(tid >> 6), rg = wv & 3, hp = wv >> 2;
  const u32x4 xr = mk_rsrc(x, 2048 * 1024), wr_ = mk_rsrc(w, 512 * 3072);
  const unsigned xrow = tid >> 3, xo = rb * 131072 + xrow * 1024 + (((tid & 7) ^ ((xrow >> 1) & 7)) << 4);
  const unsigned wrow = tid >> 4, wp = (tid & 15) ^ ((wrow & 3) << 2);
  const unsigned wo_ = cg * 128 + wrow * 3072 + 1024 + (wp >> 3) * 1024 + ((wp & 7) << 4);
  const unsigned qo = cg * 128 + xrow * 3072 + (((tid & 7) ^ (((xrow >> 1) & 1) << 2)) << 4);
  const lptr lw = lds + wv * 1024;
  auto issue = [&](int c, int i) {
    const lptr slot = lw + (c % NSLOT) * SLOT;
    if (c < 8) {
      const unsigned kc = (c + rot) & 7;
      if (i == 0) dma16(xr, xo, kc * 128, slot);
      if (i == 1) dma16(wr_, wo_, kc * (64 * 3072), slot + 16384);
      if (i == 2) dma16(xr, xo, kc * 128 + 65536, slot + 8192);
      if (i == 3) dma16(wr_, wo_, kc * (64 * 3072) + 32 * 3072, slot + 24576);
    } else {
      dma16(wr_, qo, (((c - 8) * 4 + i + rot) & 7) * (64 * 3072), slot + i * 8192);
    }
  };
  STAMP(0);
  const bool do_g = (wv == cg) && (cg < 4);
  if (do_g) dma16(mk_rsrc(gw, 1024), lane * 16, 0, lds + GA_GW);
#pragma unroll
  for (int c = 0; c < 3; ++c) {
#pragma unroll
    for (int i = 0; i < 4; ++i) issue(c, i);
    if (c < 2) bar();
  }
  const int h = lane >> 5, row = (rg >> 1) * 64 + sig(rg & 1, lane & 31);
  const int gi = lane >> 4, blk = gi & 1, q = (lane & 15) >> 2, p4 = lane & 3;
  lptr ap[4], bp[2];
#pragma unroll
  for (int s = 0; s < 4; ++s) ap[s] = lds + row * 128 + (((2 * s + h) ^ ((row >> 1) & 7)) << 4);
#pragma unroll
  for (int ct = 0; ct < 2; ++ct) bp[ct] = lds + 16384 + (8 * h + q) * 256 + (((4 * (2 * ct + hp) + 2 * blk + (p4 >> 1)) ^ (q << 2)) << 4) + (p4 & 1) * 8;
  f32x16 ak, av, ag = {};
#pragma unroll
  for (int i = 0; i < 16; ++i) { ak[i] = 1e-6f; av[i] = 0.f; }
  bf16x8 xf[32], Bf[2][2];
  STAMP(1);
#pragma unroll
  for (int n = 0; n <= 32; ++n) {
    const int c = n >> 2, s = n & 3, nb = n & 1, so = (c & 1) * SLOT;
    if (s == 0 && n < 32) {
      if (c > 0) {
        lgkm0();
        if ((c & 1) == 0) {
          const int dlt = (c == 4) ? -2 * SLOT : 2 * SLOT;
#pragma unroll
          for (int i = 0; i < 4; ++i) { ap[i] += dlt; PIN(ap[i]); }
#pragma unroll
          for (int i = 0; i < 2; ++i) { bp[i] += dlt; PIN(bp[i]); }
        }
      }
      WAIT_VM(8);
      bar();
      if (c == 0) STAMP(2);
      SB;
    }
    if (n > 0) { ak = mfma32(xf[n - 1], Bf[nb ^ 1][0], ak); SB; }
    if (n < 32) { xf[n] = lds_b128(ap[s] + so); Bf[nb][0] = lds_tr2(bp[0] + so + s * 4096, bp[0] + so + s * 4096 + 1024); SB; }
    if (n > 0) { av = mfma32(xf[n - 1], Bf[nb ^ 1][1], av); SB; }
    if (n < 32) { Bf[nb][1] = lds_tr2(bp[1] + so + s * 4096, bp[1] + so + s * 4096 + 1024); SB; }
    if (c + 3 < 10) { issue(c + 3, s); SB; }
    if (n > 0 && s == 0 && do_g) {
      const lptr gp = lds + GA_GW + ((c - 1 + rot) & 7) * 128 + h * 16;
#pragma unroll
      for (int i = 0; i < 4; ++i) ag = mfma32(lds_b128(gp + i * 32), xf[n - 4 + i], ag);
      SB;
    }
  }
  STAMP(3);
  const rsrc_t wsr = make_rsrc(ws, WS_USED);
  {
    bf16x8 ones;
#pragma unroll
    for (int i = 0; i < 8; ++i) ones[i] = (__bf16)1.0f;
    const int dp = lane & 31, d = lane & 15;
    const int pos = ((d >> 2) & 1) * 8 + (d & 3) + 4 * (d >> 3);
    const bool lo = dp < 16;
#pragma unroll
    for (int i = 0; i < 16; ++i) ak[i] = phi(ak[i]);
    f32x16 kv = {}, z = {};
#pragma unroll
    for (int s = 0; s < 2; ++s) {
      const bf16x8 pk = pack8(ak, 8 * s), vv = pack8(av, 8 * s);
      kv = mfma32(vv, pk, kv);
      z = mfma32(ones, pk, z);
    }
    LAS float* e0 = (LAS float*)(lds + GA_EX) + (rg * 4 + 2 * hp + (dp >> 4)) * 272 + pos + 64 * h;
#pragma unroll
    for (int i = 0; i < 8; ++i) {
      float a = kv[i], b = kv[8 + i];
      PIN(a); PIN(b);
      e0[((i & 3) + 8 * (i >> 2)) * 16] = lo ? a : b;
    }
    if (h == 0) e0[256] = z[0];
  }
  STAMP(4);
  f32x16 aq;
#pragma unroll
  for (int i = 0; i < 16; ++i) aq[i] = 1e-6f;
  const lptr qp = lds + (8 * h + q) * 128 + (((4 * hp + 2 * blk + (p4 >> 1)) ^ ((q >> 1) << 2)) << 4) + (p4 & 1) * 8;
  bf16x8 Aq[4];
  constexpr int PD = 3;
#pragma unroll
  for (int n = 0; n < 32 + PD; ++n) {
    if (n == 0) { lgkm0(); WAIT_VM(4); bar(); SB; }
    if (n == 16) { lgkm0(); WAIT_VM(1); bar(); SB; }
    if (n >= PD) { aq = mfma32(Aq[(n - PD) & 3], xf[n - PD], aq); SB; }
    if (n < 32) { Aq[n & 3] = lds_tr2(qp + n * 2048, qp + n * 2048 + 512); SB; }
    if (n == PD) {
      const LAS f32x4* ex = (const LAS f32x4*)(lds + GA_EX) + (tid < 272 ? tid : 0);
      const f32x4 sum = (ex[0] + ex[272]) + (ex[544] + ex[816]);
      st128(wsr, tid < 272 ? WS_KVP + ((rb * 32 + cg * 4) * 68 + tid) * 16 : 0xfffffff0u, __builtin_bit_cast(u32x4, sum));
      SB;
    }
  }
  STAMP(5);
  {
    const unsigned o = WS_PQF + (((rb * 4 + rg) * 32 + cg * 4 + 2 * hp) * 64 + lane) * 16;
#pragma unroll
    for (int i = 0; i < 16; ++i) aq[i] = phi(aq[i]);
    st128(wsr, o, __builtin_bit_cast(u32x4, pack8(aq, 0)));
    st128(wsr, o + 1024, __builtin_bit_cast(u32x4, pack8(aq, 8)));
    if (do_g && h == 0) {
      const float g = __builtin_amdgcn_rcpf(1.f + __builtin_amdgcn_exp2f(-ag[0] * LOG2E));
      st32(wsr, WS_G + (rb * 128 + row) * 4, __float_as_uint(g));
    }
  }
  STAMP(6);
  STAMP_FLUSH;
}

constexpr int SC_ROW = 2064;
constexpr int KC_CP = 4224, KC_CH = 2 * KC_CP;
constexpr int EX_BASE = 4 * KC_CH;
constexpr int U_BASE = 57 * SC_ROW, U_PAD = 224, U_CP = 4576, U_CH = 2 * U_CP;
constexpr int T_BASE = U_BASE + 4 * U_CH, T_CH = 1536;
static_assert(T_BASE + 4 * T_CH <= 163840 && U_BASE % 16 == 0, "lds");

DI f32x2 cexp_p(float l2, float rv) {
  const float m = __builtin_amdgcn_exp2f(l2), f = __builtin_amdgcn_fractf(rv);
  const f32x2 r = {m * __builtin_amdgcn_cosf(f), m * __builtin_amdgcn_sinf(f)};
  return r;
}
DI bf16x8 lds_2b64(lptr p) {
  const u32x2 a = *(const volatile LAS u32x2*)p, b = *(const volatile LAS u32x2*)(p + 8);
  const u32x4 v = {a.x, a.y, b.x, b.y};
  return __builtin_bit_cast(bf16x8, v);
}
DI unsigned perm(unsigned hi, unsigned lo, unsigned sel) { return __builtin_amdgcn_perm(hi, lo, sel); }
constexpr unsigned LL = 0x01000100, LH = 0x03020302, HL = 0x05040504, SWL = 0x01000302, SWH = 0x05040706, HL_LH = 0x03020504;
template <int E0, int E1, int E2, int E3>
DI void put4(unsigned base, f16x2 a, f16x2 b, f16x2 c, f16x2 d) {
  constexpr int O = E0 < 28 ? 0 : 28;
  static_assert((E1 < 28) == (E0 < 28) && (E2 < 28) == (E0 < 28) && (E3 < 28) == (E0 < 28), "one base");
  asm volatile("s_mov_b32 m0, %4\n\ts_nop 0\n\tds_write_addtid_b32 %0 offset:%5\n\tds_write_addtid_b32 %1 offset:%6\n\t"
               "ds_write_addtid_b32 %2 offset:%7\n\tds_write_addtid_b32 %3 offset:%8"
               :: "v"(a), "v"(b), "v"(c), "v"(d), "s"(base + O * SC_ROW), "i"((E0 - O) * SC_ROW), "i"((E1 - O) * SC_ROW),
                  "i"((E2 - O) * SC_ROW), "i"((E3 - O) * SC_ROW) : "memory");
}

DI void conv_role(lptr lds, const u16* __restrict__ x, const u16* __restrict__ lar, const u16* __restrict__ aim,
                  const u16* __restrict__ cre, const u16* __restrict__ cim, const u16* __restrict__ ldt,
                  const u16* __restrict__ dsk, char* ws, int ch0, int jrot, u64* stamps) {
  STAMP_DECL;
  const int tid = threadIdx.x, lane = tid & 63;
  const int wv = __builtin_amdgcn_readfirstlane(tid >> 6), cl = wv & 3, half = wv >> 2;
  const int d = ch0 + cl;
  STAMP(0);
  const unsigned r_lar = ((const unsigned*)lar)[d * 64 + lane], r_aim = ((const unsigned*)aim)[d * 64 + lane];
  const unsigned r_cr = ((const unsigned*)cre)[d * 64 + lane], r_ci = ((const unsigned*)cim)[d * 64 + lane];
  const unsigned s_ldt = ((const CAS unsigned*)ldt)[d >> 1], s_dsk = ((const CAS unsigned*)dsk)[d >> 1];
  bar();
  const int tt = (tid + jrot * 64) & 511;
  const char* xg = (const char*)x + ch0 * 2;
  const unsigned xoff = tt * 4096;
  u32x2 ur[4], up = {0u, 0u};
  ur[0] = *(const u32x2*)(xg + xoff); ur[1] = *(const u32x2*)(xg + xoff + 1024);
  if (lane == 0 && tt > 0) up = *(const u32x2*)(xg + xoff - 1024);
  {
    const u32x4 zero = {0, 0, 0, 0};
    if (lane < 28) *(LAS u32x4*)(lds + U_BASE + cl * U_CH + half * U_CP + lane * 16) = zero;
  }
  const int j = lane & 15, g = lane >> 4;
  const unsigned scb = (unsigned)(size_t)lds + cl * 512 + half * 256;
  const lptr fb = lds + cl * 512 + half * 256 + j * SC_ROW + g * 16;
  unsigned selA = lane < 32 ? LL : lane == 32 ? HL : SWH, selB = lane < 32 ? HL_LH : SWH, selC = lane < 32 ? LL : SWH, selE = lane < 32 ? LL : SWL;
  if (half == 0) { const bool odd = !(lane & 1); selA = odd ? LH : LL; selB = odd ? HL : LH; selC = odd ? HL : SWL; }
  PIN(selA); PIN(selB); PIN(selC); PIN(selE);
  const unsigned shl = half ? 0 : 16;
  const float f_ldt = (d & 1) ? bfhi(s_ldt) : bf2f(s_ldt), f_dsk = (d & 1) ? bfhi(s_dsk) : bf2f(s_dsk);
  const float dt = __int_as_float(__builtin_amdgcn_readfirstlane(__float_as_int(rbf(__expf(f_ldt)))));
  SB;
  STAMP(1);
  const float l2 = -rbf(__builtin_amdgcn_exp2f(bfhi(r_lar << shl) * LOG2E)) * dt * LOG2E;
  const float rv = bfhi(r_aim << shl) * dt * 0.15915494309189535f;
  unsigned cm;
  {
    typedef __attribute__((ext_vector_type(2))) unsigned short u16x2;
    const u16x2 m2 = __builtin_elementwise_max(__builtin_bit_cast(u16x2, r_cr & 0x7fff7fffu), __builtin_bit_cast(u16x2, r_ci & 0x7fff7fffu));
    const unsigned m = __builtin_bit_cast(unsigned, m2);
    cm = umax(wave_umax(umax(m << 16, m)), 0x0da24260u);
  }
  const float cmax = __uint_as_float(cm), cinv = __builtin_amdgcn_rcpf(cmax);
  f32x4 kt[3];
  {
    f16x8 rf[4], t0[4];
    const f16x2 one = {(_Float16)1.f, (_Float16)0.f};
    const f32x2 E1 = cexp_p(l2, rv), E8 = cexp_p(l2 * 8.f, rv * 8.f), E128 = cexp_p(l2 * 128.f, rv * 128.f);
    f32x2 E2, E3, E4, E16, E24, E32, E64, E96, E256, E384, E512, H1, H2, H3, H4, dummy;
    const f32x2 H0 = {bfhi(r_cr << shl) * cinv, bfhi(r_ci << shl) * cinv};
    cmul2(E16, E8, E8, E256, E128, E128);
    cmul2(E32, E16, E16, E512, E256, E256);
    cmul2(E24, E16, E8, E2, E1, E1);
    cmul2(E64, E32, E32, H1, H0, E512);
    cmul2(E3, E2, E1, E4, E2, E2);
    cmul2(E96, E64, E32, H2, H1, E512);
    cmul2(E384, E256, E128, H3, H2, E512);
    ur[2] = *(const u32x2*)(xg + xoff + 2048); ur[3] = *(const u32x2*)(xg + xoff + 3072);
    {
      const f16x2 P1 = pk_f16c(E8), P2 = pk_f16c(E16), P3 = pk_f16c(E24), Q1 = pk_f16c(E32), Q2 = pk_f16c(E64), Q3 = pk_f16c(E96);
      put4<0, 1, 2, 3>(scb, one, P1, P2, P3);
      put4<4, 5, 6, 7>(scb, Q1, cmulh(Q1, P1), cmulh(Q1, P2), cmulh(Q1, P3));
      put4<8, 9, 10, 11>(scb, Q2, cmulh(Q2, P1), cmulh(Q2, P2), cmulh(Q2, P3));
      put4<12, 13, 14, 15>(scb, Q3, cmulh(Q3, P1), cmulh(Q3, P2), cmulh(Q3, P3));
    }
    H4 = cmul(H3, E512); (void)dummy;
    const f16x2 e1 = pk_f16(E1), e2 = pk_f16(E2), e3 = pk_f16(E3), e4 = pk_f16(E4);
    const f16x2 e5 = cmulh(e4, e1), e6 = cmulh(e4, e2), e7 = cmulh(e4, e3);
    const f16x2 S1 = pk_f16(E128), S2 = pk_f16(E256), S3 = pk_f16(E384);
    const f16x2 h0 = pk_f16(H0), h1 = pk_f16(H1), h2 = pk_f16(H2), h3 = pk_f16(H3);
    {
      const f16x2 G1 = cmulh(h0, S1);
      put4<16, 17, 18, 19>(scb, h0, cmulh(h0, e1), cmulh(h0, e2), cmulh(h0, e3));
      put4<20, 21, 22, 23>(scb, cmulh(h0, e4), cmulh(h0, e5), cmulh(h0, e6), cmulh(h0, e7));
      put4<24, 25, 26, 27>(scb, G1, cmulh(G1, e1), cmulh(G1, e2), cmulh(G1, e3));
      put4<28, 29, 30, 31>(scb, cmulh(G1, e4), cmulh(G1, e5), cmulh(G1, e6), cmulh(G1, e7));
    }
#pragma unroll
    for (int s = 0; s < 4; ++s) { rf[s] = *(const LAS f16x8*)(fb + s * 64); t0[s] = *(const LAS f16x8*)(fb + 16 * SC_ROW + s * 64); }
    SB;
    {
      const f16x2 G2 = cmulh(h0, S2), G3 = cmulh(h0, S3), G5 = cmulh(h1, S1), G6 = cmulh(h1, S2), G7 = cmulh(h1, S3);
      put4<32, 33, 34, 35>(scb, G2, cmulh(G2, e2), cmulh(G2, e4), cmulh(G2, e6));
      kt[0] = mfma16h(t0[0], rf[0], f32x4{0.f, 0.f, 0.f, 0.f});
      kt[0] = mfma16h(t0[1], rf[1], kt[0]);
      put4<36, 37, 38, 39>(scb, G3, cmulh(G3, e2), cmulh(G3, e4), cmulh(G3, e6));
      kt[0] = mfma16h(t0[2], rf[2], kt[0]);
      put4<40, 41, 42, 43>(scb, h1, cmulh(h1, e4), G5, cmulh(G5, e4));
      kt[0] = mfma16h(t0[3], rf[3], kt[0]);
      put4<44, 45, 46, 47>(scb, G6, cmulh(G6, e4), G7, cmulh(G7, e4));
    }
#pragma unroll
    for (int s = 0; s < 4; ++s) t0[s] = *(const LAS f16x8*)(fb + 32 * SC_ROW + s * 64);
    SB;
    put4<48, 49, 50, 51>(scb, h2, cmulh(h2, S1), cmulh(h2, S2), cmulh(h2, S3));
    put4<52, 53, 54, 55>(scb, h3, cmulh(h3, S1), cmulh(h3, S2), cmulh(h3, S3));
    *(LAS f16x2*)(lds + cl * 512 + half * 256 + lane * 4 + 56 * SC_ROW) = pk_f16(H4);
    f16x8 t2[4];
#pragma unroll
    for (int s = 0; s < 4; ++s) t2[s] = *(const LAS f16x8*)(fb + 48 * SC_ROW + s * 64);
    SB;
    kt[1] = f32x4{0.f, 0.f, 0.f, 0.f}; kt[2] = kt[1];
#pragma unroll
    for (int s = 0; s < 4; ++s) kt[1] = mfma16h(t0[s], rf[s], kt[1]);
#pragma unroll
    for (int s = 0; s < 4; ++s) kt[2] = mfma16h(t2[s], rf[s], kt[2]);
  }
  STAMP(2);
  {
    const u32x2 pv = {(unsigned)__builtin_amdgcn_update_dpp((int)up.x, (int)ur[3].x, 0x138, 0xf, 0xf, false),
                      (unsigned)__builtin_amdgcn_update_dpp((int)up.y, (int)ur[3].y, 0x138, 0xf, 0xf, false)};
    const lptr ub = lds + U_BASE + (U_PAD + 4 * tt) * 2;
#pragma unroll
    for (int c = 0; c < 4; ++c) {
      const unsigned sel = (c & 1) ? 0x07060302 : 0x05040100;
      const unsigned p = pv[c >> 1], r0 = ur[0][c >> 1], r1 = ur[1][c >> 1], r2 = ur[2][c >> 1], r3 = ur[3][c >> 1];
      *(LAS u32x2*)(ub + c * U_CH) = u32x2{perm(r1, r0, sel), perm(r3, r2, sel)};
      *(LAS u32x2*)(ub + c * U_CH + U_CP) = u32x2{perm(r0, p, sel), perm(r2, r1, sel)};
    }
  }
  const lptr Tb = lds + T_BASE + cl * T_CH;
  {
    LAS f32x4* xw = (LAS f32x4*)(lds + cl * 512 + half * 256 + g * SC_ROW) + j;
    const LAS f32x4* xr = (const LAS f32x4*)(lds + cl * 512 + (half ^ 1) * 256 + g * SC_ROW) + j;
    if (half) xw[0] = kt[0]; else { xw[0] = kt[1]; xw[4 * SC_ROW / 16] = kt[2]; }
    const float ks = 2.f * dt * cmax;
    const lptr p1 = half ? Tb + (256 + 64 * g + (g < 2 ? 4 : 2) * j) * 2 : Tb + (128 * (g >> 1) + 4 * (g & 1) + 8 * j) * 2;
    lptr p1b = p1 + (g < 2 ? 4 : 64);
    PIN(p1b);
    lgkm0();
    STAMP(3);
    bar();
    if (half == 0) {
      f32x4 a0 = (kt[0] + xr[0]) * ks;
      if (lane == 0) a0[0] += f_dsk;
      *(LAS u32x2*)p1 = u32x2{pk_bf16(a0[0], a0[1]), pk_bf16(a0[2], a0[3])};
    } else {
      const f32x4 a1 = (kt[1] + xr[0]) * ks, a2 = (kt[2] + xr[4 * SC_ROW / 16]) * ks;
      *(LAS unsigned*)p1 = pk_bf16(a1[0], a1[1]);
      *(LAS unsigned*)p1b = pk_bf16(a1[2], a1[3]);
      const unsigned q0 = pk_bf16(a2[0], a2[1]), q1 = pk_bf16(a2[2], a2[3]);
      LAS u16* p2 = (LAS u16*)Tb + 512 + 64 * g + j;
      p2[0] = (u16)q0; p2[16] = (u16)(q0 >> 16); p2[32] = (u16)q1; p2[48] = (u16)(q1 >> 16);
    }
  }
  lgkm0();
  STAMP(4);
  bar();
  {
    const lptr kc = lds + cl * KC_CH + lane * 16;
    auto emit = [&](int Y0, unsigned P0, unsigned P1, unsigned P2, unsigned P3, unsigned P4) {
      *(LAS u32x4*)(kc + Y0 * 16) = u32x4{P0, P1, P2, P3};
      *(LAS u32x4*)(kc + KC_CP + Y0 * 16) = u32x4{P1, P2, P3, P4};
    };
    if (half == 0) {
      const LAS unsigned* p = (const LAS unsigned*)(Tb + ((575 - lane) >> 1) * 4);
      const unsigned D0 = p[32], D1 = p[33], F0 = p[0], F1 = p[1], Z = *(const LAS unsigned*)Tb;
      SB;
      const unsigned e1 = perm(D1, D0, selB), e0 = perm(D1, D0, selA), f1 = perm(F1, F0, selB), f0 = perm(F1, F0, selA);
      emit(0, e1, e1, perm(D1, D0, selC), e0, e0);
      emit(64, f1, f1, perm(F1, F0, selC), f0, f0);
      if (lane < 4) emit(256, lane == 0 ? perm(0, Z, SWL) : 0u, 0u, 0u, 0u, 0u);
    } else {
      const LAS unsigned* p = (const LAS unsigned*)(Tb + (255 - lane) * 4);
      const LAS unsigned* r = (const LAS unsigned*)(Tb + (lane < 32 ? 760 - 8 * lane : 1008 - 16 * lane));
      const unsigned F0 = p[0], F1 = p[1], D0 = r[0], D1 = r[1], D2 = r[2], D3 = r[3], D4 = r[4];
      SB;
      const unsigned e2 = perm(F1, F0, HL);
      emit(128, e2, e2, perm(F1, F0, LH), perm(F1, F0, SWL), perm(F1, F0, LL));
      emit(192, perm(D4, D2, selA), perm(lane < 32 ? D2 : D3, D1, selB), perm(D2, D1, selC), perm(D1, D0, selB), perm(D1, D0, selE));
    }
  }
  lgkm0();
  STAMP(5);
  bar();
  const int a_ = j;
  const lptr ab = lds + cl * KC_CH + ((a_ & 1) ? KC_CP + 4088 : 4096) - 8 * (a_ >> 1) + 16 * g - 3584 - 192 - half * 256;
  const lptr bb = lds + U_BASE + cl * U_CH + ((a_ & 1) ? 0 : U_CP) + (U_PAD + 32 * (a_ >> 1) + 8 * g) * 2 - 192 - half * 256;
  f32x4 acc[8];
#pragma unroll
  for (int i = 0; i < 8; ++i) acc[i] = f32x4{0.f, 0.f, 0.f, 0.f};
  LAS f32x4* exw = (LAS f32x4*)(lds + EX_BASE + wv * 4096) + lane;
  f32x4 keep[4];
  {
    bf16x8 A[2][8], B[2][8];
#pragma unroll
    for (int r = 0; r < 4; ++r) {
      const int nb = r & 1;
#pragma unroll
      for (int mu = 0; mu < 8; ++mu) {
        B[nb][mu] = lds_b128(bb + 512 * mu + 64 * (3 - r));
        A[nb][mu] = lds_2b64(ab + 512 * (7 - mu) + 64 * (3 - r));
        SB;
        if (r > 0) {
#pragma unroll
          for (int n = 0; n < 8; ++n) if (mu + n < 8) acc[mu + n] = mfma16(A[nb ^ 1][mu], B[nb ^ 1][n], acc[mu + n]);
          SB;
        }
      }
    }
    auto tiles = [&](int t0) {
#pragma unroll
      for (int mu = 0; mu < 8; ++mu)
#pragma unroll
        for (int n = 0; n < 8; ++n) if (mu + n >= t0 && mu + n < t0 + 4) acc[mu + n] = mfma16(A[1][mu], B[1][n], acc[mu + n]);
    };
    if (half) {
      tiles(0); SB; tiles(4);
#pragma unroll
      for (int i = 0; i < 4; ++i) { exw[64 * i] = acc[i]; keep[i] = acc[4 + i]; }
    } else {
      tiles(4); SB; tiles(0);
#pragma unroll
      for (int i = 0; i < 4; ++i) { exw[64 * i] = acc[4 + i]; keep[i] = acc[i]; }
    }
  }
  STAMP(6);
  {
    const LAS f32x4* exr = (const LAS f32x4*)(lds + EX_BASE + (wv ^ 4) * 4096) + lane;
    const rsrc_t wsr = make_rsrc(ws, WS_USED);
    const unsigned bto = WS_BT + d * 4096 + half * 2048 + (a_ >> 2) * 128 + g * 32 + (a_ & 3) * 8;
    lgkm0();
    STAMP(7);
    bar();
#pragma unroll
    for (int i = 0; i < 4; ++i) {
      const f32x4 v = keep[i] + exr[64 * i];
      st64(wsr, bto + 512 * i, u32x2{pk_bf16(v[0], v[1]), pk_bf16(v[2], v[3])});
    }
  }
  STAMP(8);
  STAMP_FLUSH;
}

__global__ void __launch_bounds__(512, 2) k1(const u16* __restrict__ x, const u16* __restrict__ wqkv, const u16* __restrict__ gw,
                                             const u16* __restrict__ lar, const u16* __restrict__ aim, const u16* __restrict__ cre,
                                             const u16* __restrict__ cim, const u16* __restrict__ ldt, const u16* __restrict__ dsk,
                                             char* ws
) {
  u64* stamps = nullptr;
  extern __shared__ __attribute__((aligned(16))) char lds_[];
  const lptr lds = (lptr)lds_;
  const int b = blockIdx.x, xcd = b & 7, idx = b >> 3;
  if (idx < 16) {
    conv_role(lds, x, lar, aim, cre, cim, ldt, dsk, ws, xcd * 64 + idx * 4, idx, stamps);
  } else {
    const int rl = idx & 3, cl = (idx >> 2) & 3;
    gemm_role(lds, x, wqkv, gw, ws, (xcd >> 1) * 4 + rl, (xcd & 1) * 4 + cl, 2 * ((rl + cl) & 3), stamps);
  }
}

__global__ void __launch_bounds__(256) kr(char* ws) {
  const int i = blockIdx.x * 256 + threadIdx.x;
  if (i < 4352) {
    const f32x2* p = (const f32x2*)(ws + WS_KVP) + i;
    f32x2 s = {0.f, 0.f};
#pragma unroll
    for (int r = 0; r < 16; ++r) s += p[r * 4352];
    ((unsigned*)(ws + WS_KVZ))[i] = pk_bf16(s.x, s.y);
  }
}

constexpr int L_PQ = 0, L_WO = 65536, L_KVZ = 131072, L_BT = L_KVZ + 17408, K2_LDS = L_BT + 8192;

__global__ void __launch_bounds__(512, 2) k2(const u16* __restrict__ wo, const char* __restrict__ ws, u16* __restrict__ out
) {
  STAMP_DECL;
  extern __shared__ __attribute__((aligned(16))) char lds_[];
  const lptr lds = (lptr)lds_;
  const int b = blockIdx.x, xcd = b & 7, idx = b >> 3;
  const int rl = idx & 7, cl = idx >> 3, rt = (xcd >> 1) * 8 + rl, ct = (xcd & 1) * 4 + cl, rot = (rl + 2 * cl) & 7;
  const int tid = threadIdx.x, lane = tid & 63;
  const int wv = __builtin_amdgcn_readfirstlane(tid >> 6), wr = wv & 1, wc = (wv >> 1) & 1, par = wv >> 2;
  const unsigned r8 = tid >> 3, sp = ((tid & 7) ^ (((r8 >> 1) & 1) << 2)) << 4;
  const lptr lw = lds + wv * 1024;
  STAMP(0);
  const int h = lane >> 5, t = lane & 31;
  float gate;
  const int trow = rt * 64 + sig(wr, t);
  asm volatile("global_load_dword %0, %1, %2" : "=v"(gate) : "v"(trow * 4), "s"(ws + WS_G));
  const u32x4 wsr = mk_rsrc(ws, WS_USED), wor = mk_rsrc(wo, 512 * 1024);
  dma16(wsr, tid * 16, WS_KVZ, lw + L_KVZ);
  dma16(wsr, tid * 16, WS_KVZ + 8192, lw + L_KVZ + 8192);
  if (wv == 0) dma16(wsr, tid * 16, WS_KVZ + 16384, lw + L_KVZ + 16384);
  const unsigned pqo = WS_PQF + rt * 65536 + (tid >> 8) * 32768 + (tid & 255) * 16, woo = ct * 128 + r8 * 1024 + sp;
  const lptr lpq = lds + L_PQ + (wv >> 2) * 32768 + (wv & 3) * 1024;
  auto issue = [&](int gidx) {
    const int gr = (gidx + rot) & 7;
    dma16(wsr, pqo, gr * 4096, lpq + gr * 4096);
    dma16(wor, woo, gr * 65536, lw + L_WO + gr * 8192);
  };
  issue(0); issue(1);
  bar();
  issue(2); issue(3); issue(4);
  const int gi = lane >> 4, blk = gi & 1, q = (lane & 15) >> 2, p4 = lane & 3;
  const lptr kvb = lds + L_KVZ + (t < 16 ? t : 16) * 32 + h * 16 + par * 1088;
  const lptr pqb = lds + L_PQ + wr * 32768 + lane * 16 + par * 2048;
  const lptr wob = lds + L_WO + (4 * h + q) * 128 + (((4 * wc + 2 * blk + (p4 >> 1)) ^ ((q >> 1) << 2)) << 4) + (p4 & 1) * 8 + par * 4096;
  f32x16 acc = {};
  f32x16 nd[2];
  bf16x8 wf[2][2], kf[2], pf[2];
#pragma unroll
  for (int gidx = 0; gidx <= 8; ++gidx) {
    lptr wp;
    if (gidx < 8) {
      const int hd0 = ((gidx + rot) & 7) * 4;
      const lptr kp = kvb + hd0 * 544, pp = pqb + hd0 * 1024;
      wp = wob + hd0 * 2048;
      if (gidx <= 3) WAIT_VM(8); else if (gidx == 4) WAIT_VM(7); else if (gidx == 5) WAIT_VM(5); else if (gidx == 6) WAIT_VM(3); else WAIT_VM(1);
      bar();
      if (gidx == 0) STAMP(1);
      SB;
#pragma unroll
      for (int i = 0; i < 2; ++i) { kf[i] = lds_b128(kp + i * 544); pf[i] = lds_b128(pp + i * 1024); }
      SB;
      if (gidx + 5 < 8) issue(gidx + 5);
      if (gidx == 3) dma16(wsr, r8 * 4096 + sp, WS_BT + ct * 64 * 4096 + rt * 128, lw + L_BT);
      SB;
    }
#pragma unroll
    for (int i = 0; i < 2; ++i) {
      if (gidx > 0) {
        PIN(nd[i]);
        const float r = __builtin_amdgcn_rcpf(nd[i][8] + 1e-6f);
#pragma unroll
        for (int e = 0; e < 8; ++e) nd[i][e] *= r;
        acc = mfma32(wf[(gidx - 1) & 1][i], pack8(nd[i], 0), acc);
      }
      if (gidx < 8) {
        const f32x16 zero = {};
        nd[i] = mfma32(kf[i], pf[i], zero);
        wf[gidx & 1][i] = lds_tr2(wp + i * 2048, wp + i * 2048 + 1024);
      }
      SB;
    }
  }
  STAMP(2);
  lgkm0();
  WAIT_VM(0);
  bar();
  {
    LAS f32x4* xw = (LAS f32x4*)(lds + L_PQ + wv * 2048) + lane;
    const LAS f32x4* xr = (const LAS f32x4*)(lds + L_PQ + (wv ^ 4) * 2048) + lane;
    f32x4 keep[2];
#pragma unroll
    for (int G = 0; G < 2; ++G) {
      const f32x4 lo = {acc[4 * G], acc[4 * G + 1], acc[4 * G + 2], acc[4 * G + 3]};
      const f32x4 hi = {acc[8 + 4 * G], acc[9 + 4 * G], acc[10 + 4 * G], acc[11 + 4 * G]};
      if (par) { xw[64 * G] = lo; keep[G] = hi; } else { xw[64 * G] = hi; keep[G] = lo; }
    }
    const lptr bb = lds + L_BT + (wc * 32 + par * 16 + 4 * h + q) * 128 + (((4 * wr + 2 * blk + (p4 >> 1)) ^ ((q >> 1) << 2)) << 4) + (p4 & 1) * 8;
    const u32x2 bv0 = __builtin_bit_cast(u32x2, lds_tr(bb)), bv1 = __builtin_bit_cast(u32x2, lds_tr(bb + 1024));
    lgkm0();
    bar();
    PIN(gate);
    u32x2 o[2];
#pragma unroll
    for (int G = 0; G < 2; ++G) {
      const f32x4 a = keep[G] + xr[64 * G];
      const unsigned b0 = G ? bv1.x : bv0.x, b1 = G ? bv1.y : bv0.y;
      const float bf[4] = {bf2f(b0), bfhi(b0), bf2f(b1), bfhi(b1)};
      float r[4];
#pragma unroll
      for (int e = 0; e < 4; ++e) r[e] = bf[e] + gate * (a[e] - bf[e]);
      o[G] = u32x2{pk_bf16(r[0], r[1]), pk_bf16(r[2], r[3])};
    }
    char* dst = (char*)out + (unsigned)(trow * 1024 + ct * 128 + wc * 64 + par * 32 + h * 16);
    auto sx = __builtin_amdgcn_permlane32_swap(o[0].x, o[1].x, false, false);
    auto sy = __builtin_amdgcn_permlane32_swap(o[0].y, o[1].y, false, false);
    const u32x4 v = {sx[0], sy[0], sx[1], sy[1]};
    *(u32x4*)dst = v;
  }
  STAMP(3);
  STAMP_FLUSH;
}

#define STARG(o)

extern "C" void kernel_launch(void* const* d_in, const int* in_sizes, int n_in, void* d_out, int out_size, void* d_ws,
                              size_t ws_size, hipStream_t stream) {
  const u16* x = (const u16*)d_in[0];
  const u16* wqkv = (const u16*)d_in[1];
  const u16* wo = (const u16*)d_in[2];
  const u16* lar = (const u16*)d_in[4];
  const u16* aim = (const u16*)d_in[5];
  const u16* cre = (const u16*)d_in[6];
  const u16* cim = (const u16*)d_in[7];
  const u16* ldt = (const u16*)d_in[8];
  const u16* dsk = (const u16*)d_in[9];
  const u16* gw = (const u16*)d_in[10];
  char* ws = (char*)d_ws;
  (void)hipFuncSetAttribute((const void*)k1, hipFuncAttributeMaxDynamicSharedMemorySize, 163840);
  (void)hipFuncSetAttribute((const void*)k2, hipFuncAttributeMaxDynamicSharedMemorySize, K2_LDS);
  k1<<<256, 512, 163840, stream>>>(x, wqkv, gw, lar, aim, cre, cim, ldt, dsk, ws STARG(0));
  kr<<<17, 256, 0, stream>>>(ws);
  k2<<<256, 512, K2_LDS, stream>>>(wo, ws, (u16*)d_out STARG(32768));
}
```

```cpp
#include <hip/hip_runtime.h>
#include <stdint.h>

typedef unsigned short u16;
typedef unsigned long long u64;
typedef __attribute__((ext_vector_type(8))) __bf16 bf16x8;
typedef __attribute__((ext_vector_type(2))) __bf16 bf16x2;
typedef __attribute__((ext_vector_type(8))) _Float16 f16x8;
typedef __attribute__((ext_vector_type(2))) _Float16 f16x2;
typedef __attribute__((ext_vector_type(4))) short s16x4;
typedef __attribute__((ext_vector_type(16))) float f32x16;
typedef __attribute__((ext_vector_type(4))) float f32x4;
typedef __attribute__((ext_vector_type(2))) float f32x2;
typedef __attribute__((ext_vector_type(4))) unsigned u32x4;
typedef __attribute__((ext_vector_type(2))) unsigned u32x2;
#define LAS __attribute__((address_space(3)))
#define CAS __attribute__((address_space(4)))
#define DI __device__ __forceinline__

constexpr unsigned WS_PQF = 0;
constexpr unsigned WS_BT  = 2u << 20;
constexpr unsigned WS_KVP = 4u << 20;
constexpr unsigned WS_KVZ = 5u << 20;
constexpr unsigned WS_G   = (5u << 20) + 65536;
constexpr unsigned WS_USED = 6u << 20;

constexpr float LOG2E = 1.4426950408889634f;

#define STAMP_DECL
#define STAMP(i)
#define STAMP_FLUSH
#define SB __builtin_amdgcn_sched_barrier(0)
#define PIN(x) asm volatile("" : "+v"(x))
#define PINS(x) asm volatile("" : "+s"(x))

typedef LAS char* lptr;
DI float bf2f(unsigned v) { return __uint_as_float(v << 16); }
DI float bfhi(unsigned v) { return __uint_as_float(v & 0xffff0000u); }
DI unsigned pk_bf16(float a, float b) { f32x2 v = {a, b}; return __builtin_bit_cast(unsigned, __builtin_convertvector(v, bf16x2)); }
DI float rbf(float a) { return __uint_as_float(pk_bf16(a, a) << 16); }
DI f16x2 pk_f16(f32x2 v) { return __builtin_convertvector(v, f16x2); }
DI f32x2 cmul(f32x2 a, f32x2 b) { f32x2 t = a.xx * b; f32x2 bs = {-b.y, b.x}; return __builtin_elementwise_fma(a.yy, bs, t); }
DI void cmul2(f32x2& r0, f32x2 a0, f32x2 b0, f32x2& r1, f32x2 a1, f32x2 b1) {
  f32x2 t0, t1;
  asm("v_pk_mul_f32 %0, %2, %3 op_sel_hi:[0,1]\n\tv_pk_mul_f32 %1, %4, %5 op_sel_hi:[0,1]\n\t"
      "v_pk_fma_f32 %0, %2, %3, %0 op_sel:[1,1,0] op_sel_hi:[1,0,1] neg_lo:[0,1,0]\n\t"
      "v_pk_fma_f32 %1, %4, %5, %1 op_sel:[1,1,0] op_sel_hi:[1,0,1] neg_lo:[0,1,0]"
      : "=&v"(t0), "=&v"(t1) : "v"(a0), "v"(b0), "v"(a1), "v"(b1));
  r0 = t0; r1 = t1;
}
DI f16x2 pk_f16c(f32x2 v) { const f32x2 c = {v.x, -v.y}; return __builtin_convertvector(c, f16x2); }
DI f16x2 cmulh(f16x2 a, f16x2 b) { f16x2 t = a.xx * b; f16x2 bs = {-b.y, b.x}; return __builtin_elementwise_fma(a.yy, bs, t); }
DI u32x4 mk_rsrc(const void* p, unsigned bytes) { const u64 a = (u64)p; return u32x4{(unsigned)a, (unsigned)(a >> 32), bytes, 0x00020000u}; }
DI void dma16(u32x4 rs, unsigned voff, unsigned soff, lptr l) {
  asm volatile("s_mov_b32 m0, %3\n\ts_nop 0\n\tbuffer_load_dwordx4 %0, %1, %2 offen lds"
               :: "v"(voff), "s"(rs), "s"(soff), "s"((unsigned)(size_t)l) : "memory");
}
DI int sig(int w, int n) { return 32 * ((n >> 3) & 1) + ((n >> 2) & 1) + 16 * w + 8 * (n >> 4) + 2 * (n & 3); }
DI bf16x8 lds_b128(lptr p) { return *(const LAS bf16x8*)p; }
DI s16x4 lds_tr(lptr p) { return __builtin_amdgcn_ds_read_tr16_b64_v4i16((LAS s16x4*)p); }
DI bf16x8 lds_tr2(lptr p0, lptr p1) { s16x4 a = lds_tr(p0), b = lds_tr(p1); return __builtin_bit_cast(bf16x8, __builtin_shufflevector(a, b, 0, 1, 2, 3, 4, 5, 6, 7)); }
DI f32x16 mfma32(bf16x8 a, bf16x8 b, f32x16 c) { return __builtin_amdgcn_mfma_f32_32x32x16_bf16(a, b, c, 0, 0, 0); }
DI f32x4 mfma16(bf16x8 a, bf16x8 b, f32x4 c) { return __builtin_amdgcn_mfma_f32_16x16x32_bf16(a, b, c, 0, 0, 0); }
DI f32x4 mfma16h(f16x8 a, f16x8 b, f32x4 c) { return __builtin_amdgcn_mfma_f32_16x16x32_f16(a, b, c, 0, 0, 0); }
DI void bar() { asm volatile("" ::: "memory"); __builtin_amdgcn_s_barrier(); asm volatile("" ::: "memory"); }
DI void lgkm0() { asm volatile("s_waitcnt lgkmcnt(0)" ::: "memory"); }
#define WAIT_VM(n) asm volatile("s_waitcnt vmcnt(" #n ")" ::: "memory")
DI float phi(float tp) {
  const float e = __builtin_amdgcn_fmed3f(__builtin_amdgcn_exp2f(tp * LOG2E), 0.f, 1.f);
  const int ti = __float_as_int(tp);
  return __int_as_float(ti > 0x358637bd ? ti : 0x358637bd) + e;
}
DI bf16x8 pack8(const f32x16& a, int o) {
  u32x4 p = {pk_bf16(a[o], a[o + 1]), pk_bf16(a[o + 2], a[o + 3]), pk_bf16(a[o + 4], a[o + 5]), pk_bf16(a[o + 6], a[o + 7])};
  return __builtin_bit_cast(bf16x8, p);
}
template <int CTRL, int RM = 0xf> DI int dppi(int v) { return __builtin_amdgcn_update_dpp(0, v, CTRL, RM, 0xf, true); }
template <int CTRL, int RM = 0xf> DI float dppf(float v) { return __int_as_float(dppi<CTRL, RM>(__float_as_int(v))); }
DI unsigned umax(unsigned a, unsigned b) { return a > b ? a : b; }
DI unsigned wave_umax(unsigned v) {
  v = umax(v, dppi<0x111>(v)); v = umax(v, dppi<0x112>(v)); v = umax(v, dppi<0x114>(v)); v = umax(v, dppi<0x118>(v));
  v = umax(v, dppi<0x142, 0xa>(v)); v = umax(v, dppi<0x143, 0xc>(v));
  return __builtin_amdgcn_readlane(v, 63);
}
DI float wave_sum(float v) {
  v += dppf<0x111>(v); v += dppf<0x112>(v); v += dppf<0x114>(v); v += dppf<0x118>(v);
  v += dppf<0x142, 0xa>(v); v += dppf<0x143, 0xc>(v);
  return __int_as_float(__builtin_amdgcn_readlane(__float_as_int(v), 63));
}
#define CODE_PREFETCH(NAME, dummy) do { u64 pc_; unsigned len_; \
  asm volatile("s_getpc_b64 %0\n.L" NAME "_beg:\n\ts_mov_b32 %1, .L" NAME "_end-.L" NAME "_beg" : "=s"(pc_), "=s"(len_)); \
  unsigned o_ = threadIdx.x * 64; o_ = o_ < len_ ? o_ : 0; \
  asm volatile("global_load_dword %0, %1, %2" : "=v"(dummy) : "v"(o_), "s"(pc_)); } while (0)
#define CODE_END(NAME) asm volatile(".L" NAME "_end:")
typedef __amdgpu_buffer_rsrc_t rsrc_t;
DI rsrc_t make_rsrc(void* p, unsigned bytes) { return __builtin_amdgcn_make_buffer_rsrc(p, 0, bytes, 0x00020000); }
constexpr int WT = 16;
DI void st128(rsrc_t r, unsigned off, u32x4 v) { __builtin_amdgcn_raw_buffer_store_b128(v, r, off, 0, WT); }
DI void st64(rsrc_t r, unsigned off, u32x2 v) { __builtin_amdgcn_raw_buffer_store_b64(v, r, off, 0, WT); }
DI void st32(rsrc_t r, unsigned off, unsigned v) { __builtin_amdgcn_raw_buffer_store_b32(v, r, off, 0, WT); }

constexpr int SLOT = 32768, NSLOT = 4;
constexpr int GA_EX = NSLOT * SLOT;
constexpr int GA_GW = GA_EX + 8 * 544 * 4;

DI void gemm_role(lptr lds, const u16* __restrict__ x, const u16* __restrict__ w, const u16* __restrict__ gw, char* ws,
                  int rb, int cg, int rot, u64* stamps) {
  STAMP_DECL;
  const int tid = threadIdx.x, lane = tid & 63;
  const int wv = __builtin_amdgcn_readfirstlane(tid >> 6), rg = wv & 3, hp = wv >> 2;
  const u32x4 xr = mk_rsrc(x, 2048 * 1024), wr_ = mk_rsrc(w, 512 * 3072);
  const unsigned xrow = tid >> 3, xo = rb * 131072 + xrow * 1024 + (((tid & 7) ^ ((xrow >> 1) & 7)) << 4);
  const unsigned wrow = tid >> 4, wp = (tid & 15) ^ ((wrow & 3) << 2);
  const unsigned wo_ = cg * 128 + wrow * 3072 + 1024 + (wp >> 3) * 1024 + ((wp & 7) << 4);
  const unsigned qo = cg * 128 + xrow * 3072 + (((tid & 7) ^ (((xrow >> 1) & 1) << 2)) << 4);
  const lptr lw = lds + wv * 1024;
  auto issue = [&](int c, int i) {
    const lptr slot = lw + (c % NSLOT) * SLOT;
    if (c < 8) {
      const unsigned kc = (c + rot) & 7;
      if (i == 0) dma16(xr, xo, kc * 128, slot);
      if (i == 1) dma16(wr_, wo_, kc * (64 * 3072), slot + 16384);
      if (i == 2) dma16(xr, xo, kc * 128 + 65536, slot + 8192);
      if (i == 3) dma16(wr_, wo_, kc * (64 * 3072) + 32 * 3072, slot + 24576);
    } else {
      dma16(wr_, qo, (((c - 8) * 4 + i + rot) & 7) * (64 * 3072), slot + i * 8192);
    }
  };
  STAMP(0);
  const bool do_g = (wv == cg) && (cg < 4);
  if (do_g) dma16(mk_rsrc(gw, 1024), lane * 16, 0, lds + GA_GW);
#pragma unroll
  for (int c = 0; c < 3; ++c) {
#pragma unroll
    for (int i = 0; i < 4; ++i) issue(c, i);
    if (c < 2) bar();
  }
  const int h = lane >> 5, row = (rg >> 1) * 64 + sig(rg & 1, lane & 31);
  const int gi = lane >> 4, blk = gi & 1, q = (lane & 15) >> 2, p4 = lane & 3;
  lptr ap[4], bp[2];
#pragma unroll
  for (int s = 0; s < 4; ++s) ap[s] = lds + row * 128 + (((2 * s + h) ^ ((row >> 1) & 7)) << 4);
#pragma unroll
  for (int ct = 0; ct < 2; ++ct) bp[ct] = lds + 16384 + (8 * h + q) * 256 + (((4 * (2 * ct + hp) + 2 * blk + (p4 >> 1)) ^ (q << 2)) << 4) + (p4 & 1) * 8;
  f32x16 ak, av, ag = {};
#pragma unroll
  for (int i = 0; i < 16; ++i) { ak[i] = 1e-6f; av[i] = 0.f; }
  bf16x8 xf[32], Bf[2][2];
  STAMP(1);
#pragma unroll
  for (int n = 0; n <= 32; ++n) {
    const int c = n >> 2, s = n & 3, nb = n & 1, so = (c & 1) * SLOT;
    if (s == 0 && n < 32) {
      if (c > 0) {
        lgkm0();
        if ((c & 1) == 0) {
          const int dlt = (c == 4) ? -2 * SLOT : 2 * SLOT;
#pragma unroll
          for (int i = 0; i < 4; ++i) { ap[i] += dlt; PIN(ap[i]); }
#pragma unroll
          for (int i = 0; i < 2; ++i) { bp[i] += dlt; PIN(bp[i]); }
        }
      }
      WAIT_VM(8);
      bar();
      if (c == 0) STAMP(2);
      SB;
    }
    if (n > 0) { ak = mfma32(xf[n - 1], Bf[nb ^ 1][0], ak); SB; }
    if (n < 32) { xf[n] = lds_b128(ap[s] + so); Bf[nb][0] = lds_tr2(bp[0] + so + s * 4096, bp[0] + so + s * 4096 + 1024); SB; }
    if (n > 0) { av = mfma32(xf[n - 1], Bf[nb ^ 1][1], av); SB; }
    if (n < 32) { Bf[nb][1] = lds_tr2(bp[1] + so + s * 4096, bp[1] + so + s * 4096 + 1024); SB; }
    if (c + 3 < 10) { issue(c + 3, s); SB; }
    if (n > 0 && s == 0 && do_g) {
      const lptr gp = lds + GA_GW + ((c - 1 + rot) & 7) * 128 + h * 16;
#pragma unroll
      for (int i = 0; i < 4; ++i) ag = mfma32(lds_b128(gp + i * 32), xf[n - 4 + i], ag);
      SB;
    }
  }
  STAMP(3);
  const rsrc_t wsr = make_rsrc(ws, WS_USED);
  {
    bf16x8 ones;
#pragma unroll
    for (int i = 0; i < 8; ++i) ones[i] = (__bf16)1.0f;
    const int dp = lane & 31, d = lane & 15;
    const int pos = ((d >> 2) & 1) * 8 + (d & 3) + 4 * (d >> 3);
    const bool lo = dp < 16;
#pragma unroll
    for (int i = 0; i < 16; ++i) ak[i] = phi(ak[i]);
    f32x16 kv = {}, z = {};
#pragma unroll
    for (int s = 0; s < 2; ++s) {
      const bf16x8 pk = pack8(ak, 8 * s), vv = pack8(av, 8 * s);
      kv = mfma32(vv, pk, kv);
      z = mfma32(ones, pk, z);
    }
    LAS float* e0 = (LAS float*)(lds + GA_EX) + (rg * 4 + 2 * hp + (dp >> 4)) * 272 + pos + 64 * h;
#pragma unroll
    for (int i = 0; i < 8; ++i) {
      float a = kv[i], b = kv[8 + i];
      PIN(a); PIN(b);
      e0[((i & 3) + 8 * (i >> 2)) * 16] = lo ? a : b;
    }
    if (h == 0) e0[256] = z[0];
  }
  STAMP(4);
  f32x16 aq;
#pragma unroll
  for (int i = 0; i < 16; ++i) aq[i] = 1e-6f;
  const lptr qp = lds + (8 * h + q) * 128 + (((4 * hp + 2 * blk + (p4 >> 1)) ^ ((q >> 1) << 2)) << 4) + (p4 & 1) * 8;
  bf16x8 Aq[4];
  constexpr int PD = 3;
#pragma unroll
  for (int n = 0; n < 32 + PD; ++n) {
    if (n == 0) { lgkm0(); WAIT_VM(4); bar(); SB; }
    if (n == 16) { lgkm0(); WAIT_VM(1); bar(); SB; }
    if (n >= PD) { aq = mfma32(Aq[(n - PD) & 3], xf[n - PD], aq); SB; }
    if (n < 32) { Aq[n & 3] = lds_tr2(qp + n * 2048, qp + n * 2048 + 512); SB; }
    if (n == PD) {
      const LAS f32x4* ex = (const LAS f32x4*)(lds + GA_EX) + (tid < 272 ? tid : 0);
      const f32x4 sum = (ex[0] + ex[272]) + (ex[544] + ex[816]);
      st128(wsr, tid < 272 ? WS_KVP + ((rb * 32 + cg * 4) * 68 + tid) * 16 : 0xfffffff0u, __builtin_bit_cast(u32x4, sum));
      SB;
    }
  }
  STAMP(5);
  {
    const unsigned o = WS_PQF + (((rb * 4 + rg) * 32 + cg * 4 + 2 * hp) * 64 + lane) * 16;
#pragma unroll
    for (int i = 0; i < 16; ++i) aq[i] = phi(aq[i]);
    st128(wsr, o, __builtin_bit_cast(u32x4, pack8(aq, 0)));
    st128(wsr, o + 1024, __builtin_bit_cast(u32x4, pack8(aq, 8)));
    if (do_g && h == 0) {
      const float g = __builtin_amdgcn_rcpf(1.f + __builtin_amdgcn_exp2f(-ag[0] * LOG2E));
      st32(wsr, WS_G + (rb * 128 + row) * 4, __float_as_uint(g));
    }
  }
  STAMP(6);
  STAMP_FLUSH;
}

constexpr int SC_ROW = 2064;
constexpr int KC_CP = 4224, KC_CH = 2 * KC_CP;
constexpr int EX_BASE = 4 * KC_CH;
constexpr int U_BASE = 57 * SC_ROW, U_PAD = 224, U_CP = 4576, U_CH = 2 * U_CP;
constexpr int T_BASE = U_BASE + 4 * U_CH, T_CH = 1536;
static_assert(T_BASE + 4 * T_CH <= 163840 && U_BASE % 16 == 0, "lds");

DI f32x2 cexp_p(float l2, float rv) {
  const float m = __builtin_amdgcn_exp2f(l2), f = __builtin_amdgcn_fractf(rv);
  const f32x2 r = {m * __builtin_amdgcn_cosf(f), m * __builtin_amdgcn_sinf(f)};
  return r;
}
DI bf16x8 lds_2b64(lptr p) {
  const u32x2 a = *(const volatile LAS u32x2*)p, b = *(const volatile LAS u32x2*)(p + 8);
  const u32x4 v = {a.x, a.y, b.x, b.y};
  return __builtin_bit_cast(bf16x8, v);
}
DI unsigned perm(unsigned hi, unsigned lo, unsigned sel) { return __builtin_amdgcn_perm(hi, lo, sel); }
constexpr unsigned LL = 0x01000100, LH = 0x03020302, HL = 0x05040504, SWL = 0x01000302, SWH = 0x05040706, HL_LH = 0x03020504;
template <int E0, int E1, int E2, int E3>
DI void put4(unsigned base, f16x2 a, f16x2 b, f16x2 c, f16x2 d) {
  constexpr int O = E0 < 28 ? 0 : 28;
  static_assert((E1 < 28) == (E0 < 28) && (E2 < 28) == (E0 < 28) && (E3 < 28) == (E0 < 28), "one base");
  asm volatile("s_mov_b32 m0, %4\n\ts_nop 0\n\tds_write_addtid_b32 %0 offset:%5\n\tds_write_addtid_b32 %1 offset:%6\n\t"
               "ds_write_addtid_b32 %2 offset:%7\n\tds_write_addtid_b32 %3 offset:%8"
               :: "v"(a), "v"(b), "v"(c), "v"(d), "s"(base + O * SC_ROW), "i"((E0 - O) * SC_ROW), "i"((E1 - O) * SC_ROW),
                  "i"((E2 - O) * SC_ROW), "i"((E3 - O) * SC_ROW) : "memory");
}

DI void conv_role(lptr lds, const u16* __restrict__ x, const u16* __restrict__ lar, const u16* __restrict__ aim,
                  const u16* __restrict__ cre, const u16* __restrict__ cim, const u16* __restrict__ ldt,
                  const u16* __restrict__ dsk, char* ws, int ch0, int jrot, u64* stamps) {
  STAMP_DECL;
  const int tid = threadIdx.x, lane = tid & 63;
  const int wv = __builtin_amdgcn_readfirstlane(tid >> 6), cl = wv & 3, half = wv >> 2;
  const int d = ch0 + cl;
  STAMP(0);
  const unsigned r_lar = ((const unsigned*)lar)[d * 64 + lane], r_aim = ((const unsigned*)aim)[d * 64 + lane];
  const unsigned r_cr = ((const unsigned*)cre)[d * 64 + lane], r_ci = ((const unsigned*)cim)[d * 64 + lane];
  const unsigned s_ldt = ((const CAS unsigned*)ldt)[d >> 1], s_dsk = ((const CAS unsigned*)dsk)[d >> 1];
  bar();
  const int tt = (tid + jrot * 64) & 511;
  const char* xg = (const char*)x + ch0 * 2;
  const unsigned xoff = tt * 4096;
  u32x2 ur[4], up = {0u, 0u};
  ur[0] = *(const u32x2*)(xg + xoff); ur[1] = *(const u32x2*)(xg + xoff + 1024);
  if (lane == 0 && tt > 0) up = *(const u32x2*)(xg + xoff - 1024);
  {
    const u32x4 zero = {0, 0, 0, 0};
    if (lane < 28) *(LAS u32x4*)(lds + U_BASE + cl * U_CH + half * U_CP + lane * 16) = zero;
  }
  const int j = lane & 15, g = lane >> 4;
  const unsigned scb = (unsigned)(size_t)lds + cl * 512 + half * 256;
  const lptr fb = lds + cl * 512 + half * 256 + j * SC_ROW + g * 16;
  unsigned selA = lane < 32 ? LL : lane == 32 ? HL : SWH, selB = lane < 32 ? HL_LH : SWH, selC = lane < 32 ? LL : SWH, selE = lane < 32 ? LL : SWL;
  if (half == 0) { const bool odd = !(lane & 1); selA = odd ? LH : LL; selB = odd ? HL : LH; selC = odd ? HL : SWL; }
  PIN(selA); PIN(selB); PIN(selC); PIN(selE);
  const unsigned shl = half ? 0 : 16;
  const float f_ldt = (d & 1) ? bfhi(s_ldt) : bf2f(s_ldt), f_dsk = (d & 1) ? bfhi(s_dsk) : bf2f(s_dsk);
  const float dt = __int_as_float(__builtin_amdgcn_readfirstlane(__float_as_int(rbf(__expf(f_ldt)))));
  SB;
  STAMP(1);
  const float l2 = -rbf(__builtin_amdgcn_exp2f(bfhi(r_lar << shl) * LOG2E)) * dt * LOG2E;
  const float rv = bfhi(r_aim << shl) * dt * 0.15915494309189535f;
  unsigned cm;
  {
    typedef __attribute__((ext_vector_type(2))) unsigned short u16x2;
    const u16x2 m2 = __builtin_elementwise_max(__builtin_bit_cast(u16x2, r_cr & 0x7fff7fffu), __builtin_bit_cast(u16x2, r_ci & 0x7fff7fffu));
    const unsigned m = __builtin_bit_cast(unsigned, m2);
    cm = umax(wave_umax(umax(m << 16, m)), 0x0da24260u);
  }
  const float cmax = __uint_as_float(cm), cinv = __builtin_amdgcn_rcpf(cmax);
  f32x4 kt[3];
  {
    f16x8 rf[4], t0[4];
    const f16x2 one = {(_Float16)1.f, (_Float16)0.f};
    const f32x2 E1 = cexp_p(l2, rv), E8 = cexp_p(l2 * 8.f, rv * 8.f), E128 = cexp_p(l2 * 128.f, rv * 128.f);
    f32x2 E2, E3, E4, E16, E24, E32, E64, E96, E256, E384, E512, H1, H2, H3, H4, dummy;
    const f32x2 H0 = {bfhi(r_cr << shl) * cinv, bfhi(r_ci << shl) * cinv};
    cmul2(E16, E8, E8, E256, E128, E128);
    cmul2(E32, E16, E16, E512, E256, E256);
    cmul2(E24, E16, E8, E2, E1, E1);
    cmul2(E64, E32, E32, H1, H0, E512);
    cmul2(E3, E2, E1, E4, E2, E2);
    cmul2(E96, E64, E32, H2, H1, E512);
    cmul2(E384, E256, E128, H3, H2, E512);
    ur[2] = *(const u32x2*)(xg + xoff + 2048); ur[3] = *(const u32x2*)(xg + xoff + 3072);
    {
      const f16x2 P1 = pk_f16c(E8), P2 = pk_f16c(E16), P3 = pk_f16c(E24), Q1 = pk_f16c(E32), Q2 = pk_f16c(E64), Q3 = pk_f16c(E96);
      put4<0, 1, 2, 3>(scb, one, P1, P2, P3);
      put4<4, 5, 6, 7>(scb, Q1, cmulh(Q1, P1), cmulh(Q1, P2), cmulh(Q1, P3));
      put4<8, 9, 10, 11>(scb, Q2, cmulh(Q2, P1), cmulh(Q2, P2), cmulh(Q2, P3));
      put4<12, 13, 14, 15>(scb, Q3, cmulh(Q3, P1), cmulh(Q3, P2), cmulh(Q3, P3));
    }
    H4 = cmul(H3, E512); (void)dummy;
    const f16x2 e1 = pk_f16(E1), e2 = pk_f16(E2), e3 = pk_f16(E3), e4 = pk_f16(E4);
    const f16x2 e5 = cmulh(e4, e1), e6 = cmulh(e4, e2), e7 = cmulh(e4, e3);
    const f16x2 S1 = pk_f16(E128), S2 = pk_f16(E256), S3 = pk_f16(E384);
    const f16x2 h0 = pk_f16(H0), h1 = pk_f16(H1), h2 = pk_f16(H2), h3 = pk_f16(H3);
    {
      const f16x2 G1 = cmulh(h0, S1);
      put4<16, 17, 18, 19>(scb, h0, cmulh(h0, e1), cmulh(h0, e2), cmulh(h0, e3));
      put4<20, 21, 22, 23>(scb, cmulh(h0, e4), cmulh(h0, e5), cmulh(h0, e6), cmulh(h0, e7));
      put4<24, 25, 26, 27>(scb, G1, cmulh(G1, e1), cmulh(G1, e2), cmulh(G1, e3));
      put4<28, 29, 30, 31>(scb, cmulh(G1, e4), cmulh(G1, e5), cmulh(G1, e6), cmulh(G1, e7));
    }
#pragma unroll
    for (int s = 0; s < 4; ++s) { rf[s] = *(const LAS f16x8*)(fb + s * 64); t0[s] = *(const LAS f16x8*)(fb + 16 * SC_ROW + s * 64); }
    SB;
    {
      const f16x2 G2 = cmulh(h0, S2), G3 = cmulh(h0, S3), G5 = cmulh(h1, S1), G6 = cmulh(h1, S2), G7 = cmulh(h1, S3);
      put4<32, 33, 34, 35>(scb, G2, cmulh(G2, e2), cmulh(G2, e4), cmulh(G2, e6));
      kt[0] = mfma16h(t0[0], rf[0], f32x4{0.f, 0.f, 0.f, 0.f});
      kt[0] = mfma16h(t0[1], rf[1], kt[0]);
      put4<36, 37, 38, 39>(scb, G3, cmulh(G3, e2), cmulh(G3, e4), cmulh(G3, e6));
      kt[0] = mfma16h(t0[2], rf[2], kt[0]);
      put4<40, 41, 42, 43>(scb, h1, cmulh(h1, e4), G5, cmulh(G5, e4));
      kt[0] = mfma16h(t0[3], rf[3], kt[0]);
      put4<44, 45, 46, 47>(scb, G6, cmulh(G6, e4), G7, cmulh(G7, e4));
    }
#pragma unroll
    for (int s = 0; s < 4; ++s) t0[s] = *(const LAS f16x8*)(fb + 32 * SC_ROW + s * 64);
    SB;
    put4<48, 49, 50, 51>(scb, h2, cmulh(h2, S1), cmulh(h2, S2), cmulh(h2, S3));
    put4<52, 53, 54, 55>(scb, h3, cmulh(h3, S1), cmulh(h3, S2), cmulh(h3, S3));
    *(LAS f16x2*)(lds + cl * 512 + half * 256 + lane * 4 + 56 * SC_ROW) = pk_f16(H4);
    f16x8 t2[4];
#pragma unroll
    for (int s = 0; s < 4; ++s) t2[s] = *(const LAS f16x8*)(fb + 48 * SC_ROW + s * 64);
    SB;
    kt[1] = f32x4{0.f, 0.f, 0.f, 0.f}; kt[2] = kt[1];
#pragma unroll
    for (int s = 0; s < 4; ++s) kt[1] = mfma16h(t0[s], rf[s], kt[1]);
#pragma unroll
    for (int s = 0; s < 4; ++s) kt[2] = mfma16h(t2[s], rf[s], kt[2]);
  }
  STAMP(2);
  {
    const u32x2 pv = {(unsigned)__builtin_amdgcn_update_dpp((int)up.x, (int)ur[3].x, 0x138, 0xf, 0xf, false),
                      (unsigned)__builtin_amdgcn_update_dpp((int)up.y, (int)ur[3].y, 0x138, 0xf, 0xf, false)};
    const lptr ub = lds + U_BASE + (U_PAD + 4 * tt) * 2;
#pragma unroll
    for (int c = 0; c < 4; ++c) {
      const unsigned sel = (c & 1) ? 0x07060302 : 0x05040100;
      const unsigned p = pv[c >> 1], r0 = ur[0][c >> 1], r1 = ur[1][c >> 1], r2 = ur[2][c >> 1], r3 = ur[3][c >> 1];
      *(LAS u32x2*)(ub + c * U_CH) = u32x2{perm(r1, r0, sel), perm(r3, r2, sel)};
      *(LAS u32x2*)(ub + c * U_CH + U_CP) = u32x2{perm(r0, p, sel), perm(r2, r1, sel)};
    }
  }
  const lptr Tb = lds + T_BASE + cl * T_CH;
  {
    LAS f32x4* xw = (LAS f32x4*)(lds + cl * 512 + half * 256 + g * SC_ROW) + j;
    const LAS f32x4* xr = (const LAS f32x4*)(lds + cl * 512 + (half ^ 1) * 256 + g * SC_ROW) + j;
    if (half) xw[0] = kt[0]; else { xw[0] = kt[1]; xw[4 * SC_ROW / 16] = kt[2]; }
    const float ks = 2.f * dt * cmax;
    const lptr p1 = half ? Tb + (256 + 64 * g + (g < 2 ? 4 : 2) * j) * 2 : Tb + (128 * (g >> 1) + 4 * (g & 1) + 8 * j) * 2;
    lptr p1b = p1 + (g < 2 ? 4 : 64);
    PIN(p1b);
    lgkm0();
    STAMP(3);
    bar();
    if (half == 0) {
      f32x4 a0 = (kt[0] + xr[0]) * ks;
      if (lane == 0) a0[0] += f_dsk;
      *(LAS u32x2*)p1 = u32x2{pk_bf16(a0[0], a0[1]), pk_bf16(a0[2], a0[3])};
    } else {
      const f32x4 a1 = (kt[1] + xr[0]) * ks, a2 = (kt[2] + xr[4 * SC_ROW / 16]) * ks;
      *(LAS unsigned*)p1 = pk_bf16(a1[0], a1[1]);
      *(LAS unsigned*)p1b = pk_bf16(a1[2], a1[3]);
      const unsigned q0 = pk_bf16(a2[0], a2[1]), q1 = pk_bf16(a2[2], a2[3]);
      LAS u16* p2 = (LAS u16*)Tb + 512 + 64 * g + j;
      p2[0] = (u16)q0; p2[16] = (u16)(q0 >> 16); p2[32] = (u16)q1; p2[48] = (u16)(q1 >> 16);
    }
  }
  lgkm0();
  STAMP(4);
  bar();
  {
    const lptr kc = lds + cl * KC_CH + lane * 16;
    auto emit = [&](int Y0, unsigned P0, unsigned P1, unsigned P2, unsigned P3, unsigned P4) {
      *(LAS u32x4*)(kc + Y0 * 16) = u32x4{P0, P1, P2, P3};
      *(LAS u32x4*)(kc + KC_CP + Y0 * 16) = u32x4{P1, P2, P3, P4};
    };
    if (half == 0) {
      const LAS unsigned* p = (const LAS unsigned*)(Tb + ((575 - lane) >> 1) * 4);
      const unsigned D0 = p[32], D1 = p[33], F0 = p[0], F1 = p[1], Z = *(const LAS unsigned*)Tb;
      SB;
      const unsigned e1 = perm(D1, D0, selB), e0 = perm(D1, D0, selA), f1 = perm(F1, F0, selB), f0 = perm(F1, F0, selA);
      emit(0, e1, e1, perm(D1, D0, selC), e0, e0);
      emit(64, f1, f1, perm(F1, F0, selC), f0, f0);
      if (lane < 4) emit(256, lane == 0 ? perm(0, Z, SWL) : 0u, 0u, 0u, 0u, 0u);
    } else {
      const LAS unsigned* p = (const LAS unsigned*)(Tb + (255 - lane) * 4);
      const LAS unsigned* r = (const LAS unsigned*)(Tb + (lane < 32 ? 760 - 8 * lane : 1008 - 16 * lane));
      const unsigned F0 = p[0], F1 = p[1], D0 = r[0], D1 = r[1], D2 = r[2], D3 = r[3], D4 = r[4];
      SB;
      const unsigned e2 = perm(F1, F0, HL);
      emit(128, e2, e2, perm(F1, F0, LH), perm(F1, F0, SWL), perm(F1, F0, LL));
      emit(192, perm(D4, D2, selA), perm(lane < 32 ? D2 : D3, D1, selB), perm(D2, D1, selC), perm(D1, D0, selB), perm(D1, D0, selE));
    }
  }
  lgkm0();
  STAMP(5);
  bar();
  const int a_ = j;
  const lptr ab = lds + cl * KC_CH + ((a_ & 1) ? KC_CP + 4088 : 4096) - 8 * (a_ >> 1) + 16 * g - 3584 - 192 - half * 256;
  const lptr bb = lds + U_BASE + cl * U_CH + ((a_ & 1) ? 0 : U_CP) + (U_PAD + 32 * (a_ >> 1) + 8 * g) * 2 - 192 - half * 256;
  f32x4 acc[8];
#pragma unroll
  for (int i = 0; i < 8; ++i) acc[i] = f32x4{0.f, 0.f, 0.f, 0.f};
  LAS f32x4* exw = (LAS f32x4*)(lds + EX_BASE + wv * 4096) + lane;
  f32x4 keep[4];
  {
    bf16x8 A[2][8], B[2][8];
#pragma unroll
    for (int r = 0; r < 4; ++r) {
      const int nb = r & 1;
#pragma unroll
      for (int mu = 0; mu < 8; ++mu) {
        B[nb][mu] = lds_b128(bb + 512 * mu + 64 * (3 - r));
        A[nb][mu] = lds_2b64(ab + 512 * (7 - mu) + 64 * (3 - r));
        SB;
        if (r > 0) {
#pragma unroll
          for (int n = 0; n < 8; ++n) if (mu + n < 8) acc[mu + n] = mfma16(A[nb ^ 1][mu], B[nb ^ 1][n], acc[mu + n]);
          SB;
        }
      }
    }
    auto tiles = [&](int t0) {
#pragma unroll
      for (int mu = 0; mu < 8; ++mu)
#pragma unroll
        for (int n = 0; n < 8; ++n) if (mu + n >= t0 && mu + n < t0 + 4) acc[mu + n] = mfma16(A[1][mu], B[1][n], acc[mu + n]);
    };
    if (half) {
      tiles(0); SB; tiles(4);
#pragma unroll
      for (int i = 0; i < 4; ++i) { exw[64 * i] = acc[i]; keep[i] = acc[4 + i]; }
    } else {
      tiles(4); SB; tiles(0);
#pragma unroll
      for (int i = 0; i < 4; ++i) { exw[64 * i] = acc[4 + i]; keep[i] = acc[i]; }
    }
  }
  STAMP(6);
  {
    const LAS f32x4* exr = (const LAS f32x4*)(lds + EX_BASE + (wv ^ 4) * 4096) + lane;
    const rsrc_t wsr = make_rsrc(ws, WS_USED);
    const unsigned bto = WS_BT + d * 4096 + half * 2048 + (a_ >> 2) * 128 + g * 32 + (a_ & 3) * 8;
    lgkm0();
    STAMP(7);
    bar();
#pragma unroll
    for (int i = 0; i < 4; ++i) {
      const f32x4 v = keep[i] + exr[64 * i];
      st64(wsr, bto + 512 * i, u32x2{pk_bf16(v[0], v[1]), pk_bf16(v[2], v[3])});
    }
  }
  STAMP(8);
  STAMP_FLUSH;
}

__global__ void __launch_bounds__(512, 2) k1(const u16* __restrict__ x, const u16* __restrict__ wqkv, const u16* __restrict__ gw,
                                             const u16* __restrict__ lar, const u16* __restrict__ aim, const u16* __restrict__ cre,
                                             const u16* __restrict__ cim, const u16* __restrict__ ldt, const u16* __restrict__ dsk,
                                             char* ws
) {
  u64* stamps = nullptr;
  extern __shared__ __attribute__((aligned(16))) char lds_[];
  const lptr lds = (lptr)lds_;
  const int b = blockIdx.x, xcd = b & 7, idx = b >> 3;
  unsigned cpf;
  CODE_PREFETCH("k1", cpf);
  if (idx < 16) {
    conv_role(lds, x, lar, aim, cre, cim, ldt, dsk, ws, xcd * 64 + idx * 4, idx, stamps);
  } else {
    const int rl = idx & 3, cl = (idx >> 2) & 3;
    gemm_role(lds, x, wqkv, gw, ws, (xcd >> 1) * 4 + rl, (xcd & 1) * 4 + cl, 2 * ((rl + cl) & 3), stamps);
  }
  PIN(cpf);
  CODE_END("k1");
}

__global__ void __launch_bounds__(256) kr(char* ws) {
  const int i = blockIdx.x * 256 + threadIdx.x;
  if (i < 4352) {
    const f32x2* p = (const f32x2*)(ws + WS_KVP) + i;
    f32x2 s = {0.f, 0.f};
#pragma unroll
    for (int r = 0; r < 16; ++r) s += p[r * 4352];
    ((unsigned*)(ws + WS_KVZ))[i] = pk_bf16(s.x, s.y);
  }
}

constexpr int L_PQ = 0, L_WO = 65536, L_KVZ = 131072, L_BT = L_KVZ + 17408, K2_LDS = L_BT + 8192;

__global__ void __launch_bounds__(512, 2) k2(const u16* __restrict__ wo, const char* __restrict__ ws, u16* __restrict__ out
) {
  STAMP_DECL;
  extern __shared__ __attribute__((aligned(16))) char lds_[];
  const lptr lds = (lptr)lds_;
  const int b = blockIdx.x, xcd = b & 7, idx = b >> 3;
  const int rl = idx & 7, cl = idx >> 3, rt = (xcd >> 1) * 8 + rl, ct = (xcd & 1) * 4 + cl, rot = (rl + 2 * cl) & 7;
  const int tid = threadIdx.x, lane = tid & 63;
  const int wv = __builtin_amdgcn_readfirstlane(tid >> 6), wr = wv & 1, wc = (wv >> 1) & 1, par = wv >> 2;
  const unsigned r8 = tid >> 3, sp = ((tid & 7) ^ (((r8 >> 1) & 1) << 2)) << 4;
  const lptr lw = lds + wv * 1024;
  STAMP(0);
  unsigned cpf;
  CODE_PREFETCH("k2", cpf);
  const int h = lane >> 5, t = lane & 31;
  float gate;
  const int trow = rt * 64 + sig(wr, t);
  asm volatile("global_load_dword %0, %1, %2" : "=v"(gate) : "v"(trow * 4), "s"(ws + WS_G));
  const u32x4 wsr = mk_rsrc(ws, WS_USED), wor = mk_rsrc(wo, 512 * 1024);
  dma16(wsr, tid * 16, WS_KVZ, lw + L_KVZ);
  dma16(wsr, tid * 16, WS_KVZ + 8192, lw + L_KVZ + 8192);
  if (wv == 0) dma16(wsr, tid * 16, WS_KVZ + 16384, lw + L_KVZ + 16384);
  const unsigned pqo = WS_PQF + rt * 65536 + (tid >> 8) * 32768 + (tid & 255) * 16, woo = ct * 128 + r8 * 1024 + sp;
  const lptr lpq = lds + L_PQ + (wv >> 2) * 32768 + (wv & 3) * 1024;
  auto issue = [&](int gidx) {
    const int gr = (gidx + rot) & 7;
    dma16(wsr, pqo, gr * 4096, lpq + gr * 4096);
    dma16(wor, woo, gr * 65536, lw + L_WO + gr * 8192);
  };
  issue(0); issue(1);
  bar();
  issue(2); issue(3); issue(4);
  const int gi = lane >> 4, blk = gi & 1, q = (lane & 15) >> 2, p4 = lane & 3;
  const lptr kvb = lds + L_KVZ + (t < 16 ? t : 16) * 32 + h * 16 + par * 1088;
  const lptr pqb = lds + L_PQ + wr * 32768 + lane * 16 + par * 2048;
  const lptr wob = lds + L_WO + (4 * h + q) * 128 + (((4 * wc + 2 * blk + (p4 >> 1)) ^ ((q >> 1) << 2)) << 4) + (p4 & 1) * 8 + par * 4096;
  f32x16 acc = {};
  f32x16 nd[2];
  bf16x8 wf[2][2], kf[2], pf[2];
#pragma unroll
  for (int gidx = 0; gidx <= 8; ++gidx) {
    lptr wp;
    if (gidx < 8) {
      const int hd0 = ((gidx + rot) & 7) * 4;
      const lptr kp = kvb + hd0 * 544, pp = pqb + hd0 * 1024;
      wp = wob + hd0 * 2048;
      if (gidx <= 3) WAIT_VM(8); else if (gidx == 4) WAIT_VM(7); else if (gidx == 5) WAIT_VM(5); else if (gidx == 6) WAIT_VM(3); else WAIT_VM(1);
      bar();
      if (gidx == 0) STAMP(1);
      SB;
#pragma unroll
      for (int i = 0; i < 2; ++i) { kf[i] = lds_b128(kp + i * 544); pf[i] = lds_b128(pp + i * 1024); }
      SB;
      if (gidx + 5 < 8) issue(gidx + 5);
      if (gidx == 3) dma16(wsr, r8 * 4096 + sp, WS_BT + ct * 64 * 4096 + rt * 128, lw + L_BT);
      SB;
    }
#pragma unroll
    for (int i = 0; i < 2; ++i) {
      if (gidx > 0) {
        PIN(nd[i]);
        const float r = __builtin_amdgcn_rcpf(nd[i][8] + 1e-6f);
#pragma unroll
        for (int e = 0; e < 8; ++e) nd[i][e] *= r;
        acc = mfma32(wf[(gidx - 1) & 1][i], pack8(nd[i], 0), acc);
      }
      if (gidx < 8) {
        const f32x16 zero = {};
        nd[i] = mfma32(kf[i], pf[i], zero);
        wf[gidx & 1][i] = lds_tr2(wp + i * 2048, wp + i * 2048 + 1024);
      }
      SB;
    }
  }
  STAMP(2);
  lgkm0();
  WAIT_VM(0);
  bar();
  {
    LAS f32x4* xw = (LAS f32x4*)(lds + L_PQ + wv * 2048) + lane;
    const LAS f32x4* xr = (const LAS f32x4*)(lds + L_PQ + (wv ^ 4) * 2048) + lane;
    f32x4 keep[2];
#pragma unroll
    for (int G = 0; G < 2; ++G) {
      const f32x4 lo = {acc[4 * G], acc[4 * G + 1], acc[4 * G + 2], acc[4 * G + 3]};
      const f32x4 hi = {acc[8 + 4 * G], acc[9 + 4 * G], acc[10 + 4 * G], acc[11 + 4 * G]};
      if (par) { xw[64 * G] = lo; keep[G] = hi; } else { xw[64 * G] = hi; keep[G] = lo; }
    }
    const lptr bb = lds + L_BT + (wc * 32 + par * 16 + 4 * h + q) * 128 + (((4 * wr + 2 * blk + (p4 >> 1)) ^ ((q >> 1) << 2)) << 4) + (p4 & 1) * 8;
    const u32x2 bv0 = __builtin_bit_cast(u32x2, lds_tr(bb)), bv1 = __builtin_bit_cast(u32x2, lds_tr(bb + 1024));
    lgkm0();
    bar();
    PIN(gate);
    u32x2 o[2];
#pragma unroll
    for (int G = 0; G < 2; ++G) {
      const f32x4 a = keep[G] + xr[64 * G];
      const unsigned b0 = G ? bv1.x : bv0.x, b1 = G ? bv1.y : bv0.y;
      const float bf[4] = {bf2f(b0), bfhi(b0), bf2f(b1), bfhi(b1)};
      float r[4];
#pragma unroll
      for (int e = 0; e < 4; ++e) r[e] = bf[e] + gate * (a[e] - bf[e]);
      o[G] = u32x2{pk_bf16(r[0], r[1]), pk_bf16(r[2], r[3])};
    }
    char* dst = (char*)out + (unsigned)(trow * 1024 + ct * 128 + wc * 64 + par * 32 + h * 16);
    auto sx = __builtin_amdgcn_permlane32_swap(o[0].x, o[1].x, false, false);
    auto sy = __builtin_amdgcn_permlane32_swap(o[0].y, o[1].y, false, false);
    const u32x4 v = {sx[0], sy[0], sx[1], sy[1]};
    *(u32x4*)dst = v;
  }
  STAMP(3);
  STAMP_FLUSH;
  PIN(cpf);
  CODE_END("k2");
}

#define STARG(o)

extern "C" void kernel_launch(void* const* d_in, const int* in_sizes, int n_in, void* d_out, int out_size, void* d_ws,
                              size_t ws_size, hipStream_t stream) {
  const u16* x = (const u16*)d_in[0];
  const u16* wqkv = (const u16*)d_in[1];
  const u16* wo = (const u16*)d_in[2];
  const u16* lar = (const u16*)d_in[4];
  const u16* aim = (const u16*)d_in[5];
  const u16* cre = (const u16*)d_in[6];
  const u16* cim = (const u16*)d_in[7];
  const u16* ldt = (const u16*)d_in[8];
  const u16* dsk = (const u16*)d_in[9];
  const u16* gw = (const u16*)d_in[10];
  char* ws = (char*)d_ws;
  (void)hipFuncSetAttribute((const void*)k1, hipFuncAttributeMaxDynamicSharedMemorySize, 163840);
  (void)hipFuncSetAttribute((const void*)k2, hipFuncAttributeMaxDynamicSharedMemorySize, K2_LDS);
  k1<<<256, 512, 163840, stream>>>(x, wqkv, gw, lar, aim, cre, cim, ldt, dsk, ws STARG(0));
  kr<<<17, 256, 0, stream>>>(ws);
  k2<<<256, 512, K2_LDS, stream>>>(wo, ws, (u16*)d_out STARG(32768));
}
```

```cpp
#define STAMPS
#define PROBE_K 0
#define PROBE_ROLE 0
#define PROBE_S 0
#include <hip/hip_runtime.h>
#include <stdint.h>

typedef unsigned short u16;
typedef unsigned long long u64;
typedef __attribute__((ext_vector_type(8))) __bf16 bf16x8;
typedef __attribute__((ext_vector_type(2))) __bf16 bf16x2;
typedef __attribute__((ext_vector_type(8))) _Float16 f16x8;
typedef __attribute__((ext_vector_type(2))) _Float16 f16x2;
typedef __attribute__((ext_vector_type(4))) short s16x4;
typedef __attribute__((ext_vector_type(16))) float f32x16;
typedef __attribute__((ext_vector_type(4))) float f32x4;
typedef __attribute__((ext_vector_type(2))) float f32x2;
typedef __attribute__((ext_vector_type(4))) unsigned u32x4;
typedef __attribute__((ext_vector_type(2))) unsigned u32x2;
#define LAS __attribute__((address_space(3)))
#define CAS __attribute__((address_space(4)))
#define DI __device__ __forceinline__

constexpr unsigned WS_PQF = 0;
constexpr unsigned WS_BT  = 2u << 20;
constexpr unsigned WS_KVP = 4u << 20;
constexpr unsigned WS_KVZ = 5u << 20;
constexpr unsigned WS_G   = (5u << 20) + 65536;
constexpr unsigned WS_USED = 6u << 20;

constexpr float LOG2E = 1.4426950408889634f;

#define STAMP_INS "s_memrealtime"
#define STAMP_DECL u64 st_[12] = {}
#define STAMP(i) do { __builtin_amdgcn_sched_barrier(0); asm volatile(STAMP_INS " %0\n\ts_waitcnt lgkmcnt(0)" : "=s"(st_[i]) :: "memory"); __builtin_amdgcn_sched_barrier(0); } while (0)
#define STAMP_FLUSH do { if ((threadIdx.x & 63) == 0) { _Pragma("unroll") for (int i_ = 0; i_ < 12; ++i_) stamps[(blockIdx.x * 8 + (threadIdx.x >> 6)) * 16 + i_] = st_[i_]; } } while (0)
#define SB __builtin_amdgcn_sched_barrier(0)
#define PIN(x) asm volatile("" : "+v"(x))
#define PINS(x) asm volatile("" : "+s"(x))

typedef LAS char* lptr;
DI float bf2f(unsigned v) { return __uint_as_float(v << 16); }
DI float bfhi(unsigned v) { return __uint_as_float(v & 0xffff0000u); }
DI unsigned pk_bf16(float a, float b) { f32x2 v = {a, b}; return __builtin_bit_cast(unsigned, __builtin_convertvector(v, bf16x2)); }
DI float rbf(float a) { return __uint_as_float(pk_bf16(a, a) << 16); }
DI f16x2 pk_f16(f32x2 v) { return __builtin_convertvector(v, f16x2); }
DI f32x2 cmul(f32x2 a, f32x2 b) { f32x2 t = a.xx * b; f32x2 bs = {-b.y, b.x}; return __builtin_elementwise_fma(a.yy, bs, t); }
DI void cmul2(f32x2& r0, f32x2 a0, f32x2 b0, f32x2& r1, f32x2 a1, f32x2 b1) {
  f32x2 t0, t1;
  asm("v_pk_mul_f32 %0, %2, %3 op_sel_hi:[0,1]\n\tv_pk_mul_f32 %1, %4, %5 op_sel_hi:[0,1]\n\t"
      "v_pk_fma_f32 %0, %2, %3, %0 op_sel:[1,1,0] op_sel_hi:[1,0,1] neg_lo:[0,1,0]\n\t"
      "v_pk_fma_f32 %1, %4, %5, %1 op_sel:[1,1,0] op_sel_hi:[1,0,1] neg_lo:[0,1,0]"
      : "=&v"(t0), "=&v"(t1) : "v"(a0), "v"(b0), "v"(a1), "v"(b1));
  r0 = t0; r1 = t1;
}
DI f16x2 pk_f16c(f32x2 v) { const f32x2 c = {v.x, -v.y}; return __builtin_convertvector(c, f16x2); }
DI f16x2 cmulh(f16x2 a, f16x2 b) { f16x2 t = a.xx * b; f16x2 bs = {-b.y, b.x}; return __builtin_elementwise_fma(a.yy, bs, t); }
DI u32x4 mk_rsrc(const void* p, unsigned bytes) { const u64 a = (u64)p; return u32x4{(unsigned)a, (unsigned)(a >> 32), bytes, 0x00020000u}; }
DI void dma16(u32x4 rs, unsigned voff, unsigned soff, lptr l) {
  asm volatile("s_mov_b32 m0, %3\n\ts_nop 0\n\tbuffer_load_dwordx4 %0, %1, %2 offen lds"
               :: "v"(voff), "s"(rs), "s"(soff), "s"((unsigned)(size_t)l) : "memory");
}
DI int sig(int w, int n) { return 32 * ((n >> 3) & 1) + ((n >> 2) & 1) + 16 * w + 8 * (n >> 4) + 2 * (n & 3); }
DI bf16x8 lds_b128(lptr p) { return *(const LAS bf16x8*)p; }
DI s16x4 lds_tr(lptr p) { return __builtin_amdgcn_ds_read_tr16_b64_v4i16((LAS s16x4*)p); }
DI bf16x8 lds_tr2(lptr p0, lptr p1) { s16x4 a = lds_tr(p0), b = lds_tr(p1); return __builtin_bit_cast(bf16x8, __builtin_shufflevector(a, b, 0, 1, 2, 3, 4, 5, 6, 7)); }
DI f32x16 mfma32(bf16x8 a, bf16x8 b, f32x16 c) { return __builtin_amdgcn_mfma_f32_32x32x16_bf16(a, b, c, 0, 0, 0); }
DI f32x4 mfma16(bf16x8 a, bf16x8 b, f32x4 c) { return __builtin_amdgcn_mfma_f32_16x16x32_bf16(a, b, c, 0, 0, 0); }
DI f32x4 mfma16h(f16x8 a, f16x8 b, f32x4 c) { return __builtin_amdgcn_mfma_f32_16x16x32_f16(a, b, c, 0, 0, 0); }
DI void bar() { asm volatile("" ::: "memory"); __builtin_amdgcn_s_barrier(); asm volatile("" ::: "memory"); }
DI void lgkm0() { asm volatile("s_waitcnt lgkmcnt(0)" ::: "memory"); }
#define WAIT_VM(n) asm volatile("s_waitcnt vmcnt(" #n ")" ::: "memory")
DI float phi(float tp) {
  const float e = __builtin_amdgcn_fmed3f(__builtin_amdgcn_exp2f(tp * LOG2E), 0.f, 1.f);
  const int ti = __float_as_int(tp);
  return __int_as_float(ti > 0x358637bd ? ti : 0x358637bd) + e;
}
DI bf16x8 pack8(const f32x16& a, int o) {
  u32x4 p = {pk_bf16(a[o], a[o + 1]), pk_bf16(a[o + 2], a[o + 3]), pk_bf16(a[o + 4], a[o + 5]), pk_bf16(a[o + 6], a[o + 7])};
  return __builtin_bit_cast(bf16x8, p);
}
template <int CTRL, int RM = 0xf> DI int dppi(int v) { return __builtin_amdgcn_update_dpp(0, v, CTRL, RM, 0xf, true); }
template <int CTRL, int RM = 0xf> DI float dppf(float v) { return __int_as_float(dppi<CTRL, RM>(__float_as_int(v))); }
DI unsigned umax(unsigned a, unsigned b) { return a > b ? a : b; }
DI unsigned wave_umax(unsigned v) {
  v = umax(v, dppi<0x111>(v)); v = umax(v, dppi<0x112>(v)); v = umax(v, dppi<0x114>(v)); v = umax(v, dppi<0x118>(v));
  v = umax(v, dppi<0x142, 0xa>(v)); v = umax(v, dppi<0x143, 0xc>(v));
  return __builtin_amdgcn_readlane(v, 63);
}
DI float wave_sum(float v) {
  v += dppf<0x111>(v); v += dppf<0x112>(v); v += dppf<0x114>(v); v += dppf<0x118>(v);
  v += dppf<0x142, 0xa>(v); v += dppf<0x143, 0xc>(v);
  return __int_as_float(__builtin_amdgcn_readlane(__float_as_int(v), 63));
}
#define CODE_PREFETCH(NAME, dummy) do { u64 pc_; unsigned len_; \
  asm volatile("s_getpc_b64 %0\n.L" NAME "_beg:\n\ts_mov_b32 %1, .L" NAME "_end-.L" NAME "_beg" : "=s"(pc_), "=s"(len_)); \
  unsigned o_ = threadIdx.x * 64; o_ = o_ < len_ ? o_ : 0; \
  asm volatile("global_load_dword %0, %1, %2" : "=v"(dummy) : "v"(o_), "s"(pc_)); } while (0)
#define CODE_END(NAME) asm volatile(".L" NAME "_end:")
typedef __amdgpu_buffer_rsrc_t rsrc_t;
DI rsrc_t make_rsrc(void* p, unsigned bytes) { return __builtin_amdgcn_make_buffer_rsrc(p, 0, bytes, 0x00020000); }
constexpr int WT = 16;
DI void st128(rsrc_t r, unsigned off, u32x4 v) { __builtin_amdgcn_raw_buffer_store_b128(v, r, off, 0, WT); }
DI void st64(rsrc_t r, unsigned off, u32x2 v) { __builtin_amdgcn_raw_buffer_store_b64(v, r, off, 0, WT); }
DI void st32(rsrc_t r, unsigned off, unsigned v) { __builtin_amdgcn_raw_buffer_store_b32(v, r, off, 0, WT); }

constexpr int SLOT = 32768, NSLOT = 4;
constexpr int GA_EX = NSLOT * SLOT;
constexpr int GA_GW = GA_EX + 8 * 544 * 4;

DI void gemm_role(lptr lds, const u16* __restrict__ x, const u16* __restrict__ w, const u16* __restrict__ gw, char* ws,
                  int rb, int cg, int rot, u64* stamps) {
  STAMP_DECL;
  const int tid = threadIdx.x, lane = tid & 63;
  const int wv = __builtin_amdgcn_readfirstlane(tid >> 6), rg = wv & 3, hp = wv >> 2;
  const u32x4 xr = mk_rsrc(x, 2048 * 1024), wr_ = mk_rsrc(w, 512 * 3072);
  const unsigned xrow = tid >> 3, xo = rb * 131072 + xrow * 1024 + (((tid & 7) ^ ((xrow >> 1) & 7)) << 4);
  const unsigned wrow = tid >> 4, wp = (tid & 15) ^ ((wrow & 3) << 2);
  const unsigned wo_ = cg * 128 + wrow * 3072 + 1024 + (wp >> 3) * 1024 + ((wp & 7) << 4);
  const unsigned qo = cg * 128 + xrow * 3072 + (((tid & 7) ^ (((xrow >> 1) & 1) << 2)) << 4);
  const lptr lw = lds + wv * 1024;
  auto issue = [&](int c, int i) {
    const lptr slot = lw + (c % NSLOT) * SLOT;
    if (c < 8) {
      const unsigned kc = (c + rot) & 7;
      if (i == 0) dma16(xr, xo, kc * 128, slot);
      if (i == 1) dma16(wr_, wo_, kc * (64 * 3072), slot + 16384);
      if (i == 2) dma16(xr, xo, kc * 128 + 65536, slot + 8192);
      if (i == 3) dma16(wr_, wo_, kc * (64 * 3072) + 32 * 3072, slot + 24576);
    } else {
      dma16(wr_, qo, (((c - 8) * 4 + i + rot) & 7) * (64 * 3072), slot + i * 8192);
    }
  };
  STAMP(0);
  const bool do_g = (wv == cg) && (cg < 4);
  if (do_g) dma16(mk_rsrc(gw, 1024), lane * 16, 0, lds + GA_GW);
#pragma unroll
  for (int c = 0; c < 3; ++c) {
#pragma unroll
    for (int i = 0; i < 4; ++i) issue(c, i);
    if (c < 2) bar();
  }
  const int h = lane >> 5, row = (rg >> 1) * 64 + sig(rg & 1, lane & 31);
  const int gi = lane >> 4, blk = gi & 1, q = (lane & 15) >> 2, p4 = lane & 3;
  lptr ap[4], bp[2];
#pragma unroll
  for (int s = 0; s < 4; ++s) ap[s] = lds + row * 128 + (((2 * s + h) ^ ((row >> 1) & 7)) << 4);
#pragma unroll
  for (int ct = 0; ct < 2; ++ct) bp[ct] = lds + 16384 + (8 * h + q) * 256 + (((4 * (2 * ct + hp) + 2 * blk + (p4 >> 1)) ^ (q << 2)) << 4) + (p4 & 1) * 8;
  f32x16 ak, av, ag = {};
#pragma unroll
  for (int i = 0; i < 16; ++i) { ak[i] = 1e-6f; av[i] = 0.f; }
  bf16x8 xf[32], Bf[2][2];
  STAMP(1);
#pragma unroll
  for (int n = 0; n <= 32; ++n) {
    const int c = n >> 2, s = n & 3, nb = n & 1, so = (c & 1) * SLOT;
    if (s == 0 && n < 32) {
      if (c > 0) {
        lgkm0();
        if ((c & 1) == 0) {
          const int dlt = (c == 4) ? -2 * SLOT : 2 * SLOT;
#pragma unroll
          for (int i = 0; i < 4; ++i) { ap[i] += dlt; PIN(ap[i]); }
#pragma unroll
          for (int i = 0; i < 2; ++i) { bp[i] += dlt; PIN(bp[i]); }
        }
      }
      WAIT_VM(8);
      bar();
      if (c == 0) STAMP(2);
      SB;
    }
    if (n > 0) { ak = mfma32(xf[n - 1], Bf[nb ^ 1][0], ak); SB; }
    if (n < 32) { xf[n] = lds_b128(ap[s] + so); Bf[nb][0] = lds_tr2(bp[0] + so + s * 4096, bp[0] + so + s * 4096 + 1024); SB; }
    if (n > 0) { av = mfma32(xf[n - 1], Bf[nb ^ 1][1], av); SB; }
    if (n < 32) { Bf[nb][1] = lds_tr2(bp[1] + so + s * 4096, bp[1] + so + s * 4096 + 1024); SB; }
    if (c + 3 < 10) { issue(c + 3, s); SB; }
    if (n > 0 && s == 0 && do_g) {
      const lptr gp = lds + GA_GW + ((c - 1 + rot) & 7) * 128 + h * 16;
#pragma unroll
      for (int i = 0; i < 4; ++i) ag = mfma32(lds_b128(gp + i * 32), xf[n - 4 + i], ag);
      SB;
    }
  }
  STAMP(3);
  const rsrc_t wsr = make_rsrc(ws, WS_USED);
  {
    bf16x8 ones;
#pragma unroll
    for (int i = 0; i < 8; ++i) ones[i] = (__bf16)1.0f;
    const int dp = lane & 31, d = lane & 15;
    const int pos = ((d >> 2) & 1) * 8 + (d & 3) + 4 * (d >> 3);
    const bool lo = dp < 16;
#pragma unroll
    for (int i = 0; i < 16; ++i) ak[i] = phi(ak[i]);
    f32x16 kv = {}, z = {};
#pragma unroll
    for (int s = 0; s < 2; ++s) {
      const bf16x8 pk = pack8(ak, 8 * s), vv = pack8(av, 8 * s);
      kv = mfma32(vv, pk, kv);
      z = mfma32(ones, pk, z);
    }
    LAS float* e0 = (LAS float*)(lds + GA_EX) + (rg * 4 + 2 * hp + (dp >> 4)) * 272 + pos + 64 * h;
#pragma unroll
    for (int i = 0; i < 8; ++i) {
      float a = kv[i], b = kv[8 + i];
      PIN(a); PIN(b);
      e0[((i & 3) + 8 * (i >> 2)) * 16] = lo ? a : b;
    }
    if (h == 0) e0[256] = z[0];
  }
  STAMP(4);
  f32x16 aq;
#pragma unroll
  for (int i = 0; i < 16; ++i) aq[i] = 1e-6f;
  const lptr qp = lds + (8 * h + q) * 128 + (((4 * hp + 2 * blk + (p4 >> 1)) ^ ((q >> 1) << 2)) << 4) + (p4 & 1) * 8;
  bf16x8 Aq[4];
  constexpr int PD = 3;
#pragma unroll
  for (int n = 0; n < 32 + PD; ++n) {
    if (n == 0) { lgkm0(); WAIT_VM(4); bar(); SB; }
    if (n == 16) { lgkm0(); WAIT_VM(1); bar(); SB; }
    if (n >= PD) { aq = mfma32(Aq[(n - PD) & 3], xf[n - PD], aq); SB; }
    if (n < 32) { Aq[n & 3] = lds_tr2(qp + n * 2048, qp + n * 2048 + 512); SB; }
    if (n == PD) {
      const LAS f32x4* ex = (const LAS f32x4*)(lds + GA_EX) + (tid < 272 ? tid : 0);
      const f32x4 sum = (ex[0] + ex[272]) + (ex[544] + ex[816]);
      st128(wsr, tid < 272 ? WS_KVP + ((rb * 32 + cg * 4) * 68 + tid) * 16 : 0xfffffff0u, __builtin_bit_cast(u32x4, sum));
      SB;
    }
  }
  STAMP(5);
  {
    const unsigned o = WS_PQF + (((rb * 4 + rg) * 32 + cg * 4 + 2 * hp) * 64 + lane) * 16;
#pragma unroll
    for (int i = 0; i < 16; ++i) aq[i] = phi(aq[i]);
    st128(wsr, o, __builtin_bit_cast(u32x4, pack8(aq, 0)));
    st128(wsr, o + 1024, __builtin_bit_cast(u32x4, pack8(aq, 8)));
    if (do_g && h == 0) {
      const float g = __builtin_amdgcn_rcpf(1.f + __builtin_amdgcn_exp2f(-ag[0] * LOG2E));
      st32(wsr, WS_G + (rb * 128 + row) * 4, __float_as_uint(g));
    }
  }
  STAMP(6);
  STAMP_FLUSH;
}

constexpr int SC_ROW = 2064;
constexpr int KC_CP = 4224, KC_CH = 2 * KC_CP;
constexpr int EX_BASE = 4 * KC_CH;
constexpr int U_BASE = 57 * SC_ROW, U_PAD = 224, U_CP = 4576, U_CH = 2 * U_CP;
constexpr int T_BASE = U_BASE + 4 * U_CH, T_CH = 1536;
static_assert(T_BASE + 4 * T_CH <= 163840 && U_BASE % 16 == 0, "lds");

DI f32x2 cexp_p(float l2, float rv) {
  const float m = __builtin_amdgcn_exp2f(l2), f = __builtin_amdgcn_fractf(rv);
  const f32x2 r = {m * __builtin_amdgcn_cosf(f), m * __builtin_amdgcn_sinf(f)};
  return r;
}
DI bf16x8 lds_2b64(lptr p) {
  const u32x2 a = *(const volatile LAS u32x2*)p, b = *(const volatile LAS u32x2*)(p + 8);
  const u32x4 v = {a.x, a.y, b.x, b.y};
  return __builtin_bit_cast(bf16x8, v);
}
DI unsigned perm(unsigned hi, unsigned lo, unsigned sel) { return __builtin_amdgcn_perm(hi, lo, sel); }
constexpr unsigned LL = 0x01000100, LH = 0x03020302, HL = 0x05040504, SWL = 0x01000302, SWH = 0x05040706, HL_LH = 0x03020504;
template <int E0, int E1, int E2, int E3>
DI void put4(unsigned base, f16x2 a, f16x2 b, f16x2 c, f16x2 d) {
  constexpr int O = E0 < 28 ? 0 : 28;
  static_assert((E1 < 28) == (E0 < 28) && (E2 < 28) == (E0 < 28) && (E3 < 28) == (E0 < 28), "one base");
  asm volatile("s_mov_b32 m0, %4\n\ts_nop 0\n\tds_write_addtid_b32 %0 offset:%5\n\tds_write_addtid_b32 %1 offset:%6\n\t"
               "ds_write_addtid_b32 %2 offset:%7\n\tds_write_addtid_b32 %3 offset:%8"
               :: "v"(a), "v"(b), "v"(c), "v"(d), "s"(base + O * SC_ROW), "i"((E0 - O) * SC_ROW), "i"((E1 - O) * SC_ROW),
                  "i"((E2 - O) * SC_ROW), "i"((E3 - O) * SC_ROW) : "memory");
}

DI void conv_role(lptr lds, const u16* __restrict__ x, const u16* __restrict__ lar, const u16* __restrict__ aim,
                  const u16* __restrict__ cre, const u16* __restrict__ cim, const u16* __restrict__ ldt,
                  const u16* __restrict__ dsk, char* ws, int ch0, int jrot, u64* stamps) {
  STAMP_DECL;
  const int tid = threadIdx.x, lane = tid & 63;
  const int wv = __builtin_amdgcn_readfirstlane(tid >> 6), cl = wv & 3, half = wv >> 2;
  const int d = ch0 + cl;
  STAMP(0);
  const unsigned r_lar = ((const unsigned*)lar)[d * 64 + lane], r_aim = ((const unsigned*)aim)[d * 64 + lane];
  const unsigned r_cr = ((const unsigned*)cre)[d * 64 + lane], r_ci = ((const unsigned*)cim)[d * 64 + lane];
  const unsigned s_ldt = ((const CAS unsigned*)ldt)[d >> 1], s_dsk = ((const CAS unsigned*)dsk)[d >> 1];
  bar();
  const int tt = (tid + jrot * 64) & 511;
  const char* xg = (const char*)x + ch0 * 2;
  const unsigned xoff = tt * 4096;
  u32x2 ur[4], up = {0u, 0u};
  ur[0] = *(const u32x2*)(xg + xoff); ur[1] = *(const u32x2*)(xg + xoff + 1024);
  if (lane == 0 && tt > 0) up = *(const u32x2*)(xg + xoff - 1024);
  {
    const u32x4 zero = {0, 0, 0, 0};
    if (lane < 28) *(LAS u32x4*)(lds + U_BASE + cl * U_CH + half * U_CP + lane * 16) = zero;
  }
  const int j = lane & 15, g = lane >> 4;
  const unsigned scb = (unsigned)(size_t)lds + cl * 512 + half * 256;
  const lptr fb = lds + cl * 512 + half * 256 + j * SC_ROW + g * 16;
  unsigned selA = lane < 32 ? LL : lane == 32 ? HL : SWH, selB = lane < 32 ? HL_LH : SWH, selC = lane < 32 ? LL : SWH, selE = lane < 32 ? LL : SWL;
  if (half == 0) { const bool odd = !(lane & 1); selA = odd ? LH : LL; selB = odd ? HL : LH; selC = odd ? HL : SWL; }
  PIN(selA); PIN(selB); PIN(selC); PIN(selE);
  const unsigned shl = half ? 0 : 16;
  const float f_ldt = (d & 1) ? bfhi(s_ldt) : bf2f(s_ldt), f_dsk = (d & 1) ? bfhi(s_dsk) : bf2f(s_dsk);
  const float dt = __int_as_float(__builtin_amdgcn_readfirstlane(__float_as_int(rbf(__expf(f_ldt)))));
  SB;
  STAMP(1);
  const float l2 = -rbf(__builtin_amdgcn_exp2f(bfhi(r_lar << shl) * LOG2E)) * dt * LOG2E;
  const float rv = bfhi(r_aim << shl) * dt * 0.15915494309189535f;
  unsigned cm;
  {
    typedef __attribute__((ext_vector_type(2))) unsigned short u16x2;
    const u16x2 m2 = __builtin_elementwise_max(__builtin_bit_cast(u16x2, r_cr & 0x7fff7fffu), __builtin_bit_cast(u16x2, r_ci & 0x7fff7fffu));
    const unsigned m = __builtin_bit_cast(unsigned, m2);
    cm = umax(wave_umax(umax(m << 16, m)), 0x0da24260u);
  }
  const float cmax = __uint_as_float(cm), cinv = __builtin_amdgcn_rcpf(cmax);
  f32x4 kt[3];
  {
    f16x8 rf[4], t0[4];
    const f16x2 one = {(_Float16)1.f, (_Float16)0.f};
    const f32x2 E1 = cexp_p(l2, rv), E8 = cexp_p(l2 * 8.f, rv * 8.f), E128 = cexp_p(l2 * 128.f, rv * 128.f);
    f32x2 E2, E3, E4, E16, E24, E32, E64, E96, E256, E384, E512, H1, H2, H3, H4, dummy;
    const f32x2 H0 = {bfhi(r_cr << shl) * cinv, bfhi(r_ci << shl) * cinv};
    cmul2(E16, E8, E8, E256, E128, E128);
    cmul2(E32, E16, E16, E512, E256, E256);
    cmul2(E24, E16, E8, E2, E1, E1);
    cmul2(E64, E32, E32, H1, H0, E512);
    cmul2(E3, E2, E1, E4, E2, E2);
    cmul2(E96, E64, E32, H2, H1, E512);
    cmul2(E384, E256, E128, H3, H2, E512);
    ur[2] = *(const u32x2*)(xg + xoff + 2048); ur[3] = *(const u32x2*)(xg + xoff + 3072);
    {
      const f16x2 P1 = pk_f16c(E8), P2 = pk_f16c(E16), P3 = pk_f16c(E24), Q1 = pk_f16c(E32), Q2 = pk_f16c(E64), Q3 = pk_f16c(E96);
      put4<0, 1, 2, 3>(scb, one, P1, P2, P3);
      put4<4, 5, 6, 7>(scb, Q1, cmulh(Q1, P1), cmulh(Q1, P2), cmulh(Q1, P3));
      put4<8, 9, 10, 11>(scb, Q2, cmulh(Q2, P1), cmulh(Q2, P2), cmulh(Q2, P3));
      put4<12, 13, 14, 15>(scb, Q3, cmulh(Q3, P1), cmulh(Q3, P2), cmulh(Q3, P3));
    }
    H4 = cmul(H3, E512); (void)dummy;
    const f16x2 e1 = pk_f16(E1), e2 = pk_f16(E2), e3 = pk_f16(E3), e4 = pk_f16(E4);
    const f16x2 e5 = cmulh(e4, e1), e6 = cmulh(e4, e2), e7 = cmulh(e4, e3);
    const f16x2 S1 = pk_f16(E128), S2 = pk_f16(E256), S3 = pk_f16(E384);
    const f16x2 h0 = pk_f16(H0), h1 = pk_f16(H1), h2 = pk_f16(H2), h3 = pk_f16(H3);
    {
      const f16x2 G1 = cmulh(h0, S1);
      put4<16, 17, 18, 19>(scb, h0, cmulh(h0, e1), cmulh(h0, e2), cmulh(h0, e3));
      put4<20, 21, 22, 23>(scb, cmulh(h0, e4), cmulh(h0, e5), cmulh(h0, e6), cmulh(h0, e7));
      put4<24, 25, 26, 27>(scb, G1, cmulh(G1, e1), cmulh(G1, e2), cmulh(G1, e3));
      put4<28, 29, 30, 31>(scb, cmulh(G1, e4), cmulh(G1, e5), cmulh(G1, e6), cmulh(G1, e7));
    }
#pragma unroll
    for (int s = 0; s < 4; ++s) { rf[s] = *(const LAS f16x8*)(fb + s * 64); t0[s] = *(const LAS f16x8*)(fb + 16 * SC_ROW + s * 64); }
    SB;
    {
      const f16x2 G2 = cmulh(h0, S2), G3 = cmulh(h0, S3), G5 = cmulh(h1, S1), G6 = cmulh(h1, S2), G7 = cmulh(h1, S3);
      put4<32, 33, 34, 35>(scb, G2, cmulh(G2, e2), cmulh(G2, e4), cmulh(G2, e6));
      kt[0] = mfma16h(t0[0], rf[0], f32x4{0.f, 0.f, 0.f, 0.f});
      kt[0] = mfma16h(t0[1], rf[1], kt[0]);
      put4<36, 37, 38, 39>(scb, G3, cmulh(G3, e2), cmulh(G3, e4), cmulh(G3, e6));
      kt[0] = mfma16h(t0[2], rf[2], kt[0]);
      put4<40, 41, 42, 43>(scb, h1, cmulh(h1, e4), G5, cmulh(G5, e4));
      kt[0] = mfma16h(t0[3], rf[3], kt[0]);
      put4<44, 45, 46, 47>(scb, G6, cmulh(G6, e4), G7, cmulh(G7, e4));
    }
#pragma unroll
    for (int s = 0; s < 4; ++s) t0[s] = *(const LAS f16x8*)(fb + 32 * SC_ROW + s * 64);
    SB;
    put4<48, 49, 50, 51>(scb, h2, cmulh(h2, S1), cmulh(h2, S2), cmulh(h2, S3));
    put4<52, 53, 54, 55>(scb, h3, cmulh(h3, S1), cmulh(h3, S2), cmulh(h3, S3));
    *(LAS f16x2*)(lds + cl * 512 + half * 256 + lane * 4 + 56 * SC_ROW) = pk_f16(H4);
    f16x8 t2[4];
#pragma unroll
    for (int s = 0; s < 4; ++s) t2[s] = *(const LAS f16x8*)(fb + 48 * SC_ROW + s * 64);
    SB;
    kt[1] = f32x4{0.f, 0.f, 0.f, 0.f}; kt[2] = kt[1];
#pragma unroll
    for (int s = 0; s < 4; ++s) kt[1] = mfma16h(t0[s], rf[s], kt[1]);
#pragma unroll
    for (int s = 0; s < 4; ++s) kt[2] = mfma16h(t2[s], rf[s], kt[2]);
  }
  STAMP(2);
  {
    const u32x2 pv = {(unsigned)__builtin_amdgcn_update_dpp((int)up.x, (int)ur[3].x, 0x138, 0xf, 0xf, false),
                      (unsigned)__builtin_amdgcn_update_dpp((int)up.y, (int)ur[3].y, 0x138, 0xf, 0xf, false)};
    const lptr ub = lds + U_BASE + (U_PAD + 4 * tt) * 2;
#pragma unroll
    for (int c = 0; c < 4; ++c) {
      const unsigned sel = (c & 1) ? 0x07060302 : 0x05040100;
      const unsigned p = pv[c >> 1], r0 = ur[0][c >> 1], r1 = ur[1][c >> 1], r2 = ur[2][c >> 1], r3 = ur[3][c >> 1];
      *(LAS u32x2*)(ub + c * U_CH) = u32x2{perm(r1, r0, sel), perm(r3, r2, sel)};
      *(LAS u32x2*)(ub + c * U_CH + U_CP) = u32x2{perm(r0, p, sel), perm(r2, r1, sel)};
    }
  }
  const lptr Tb = lds + T_BASE + cl * T_CH;
  {
    LAS f32x4* xw = (LAS f32x4*)(lds + cl * 512 + half * 256 + g * SC_ROW) + j;
    const LAS f32x4* xr = (const LAS f32x4*)(lds + cl * 512 + (half ^ 1) * 256 + g * SC_ROW) + j;
    if (half) xw[0] = kt[0]; else { xw[0] = kt[1]; xw[4 * SC_ROW / 16] = kt[2]; }
    const float ks = 2.f * dt * cmax;
    const lptr p1 = half ? Tb + (256 + 64 * g + (g < 2 ? 4 : 2) * j) * 2 : Tb + (128 * (g >> 1) + 4 * (g & 1) + 8 * j) * 2;
    lptr p1b = p1 + (g < 2 ? 4 : 64);
    PIN(p1b);
    lgkm0();
    STAMP(3);
    bar();
    if (half == 0) {
      f32x4 a0 = (kt[0] + xr[0]) * ks;
      if (lane == 0) a0[0] += f_dsk;
      *(LAS u32x2*)p1 = u32x2{pk_bf16(a0[0], a0[1]), pk_bf16(a0[2], a0[3])};
    } else {
      const f32x4 a1 = (kt[1] + xr[0]) * ks, a2 = (kt[2] + xr[4 * SC_ROW / 16]) * ks;
      *(LAS unsigned*)p1 = pk_bf16(a1[0], a1[1]);
      *(LAS unsigned*)p1b = pk_bf16(a1[2], a1[3]);
      const unsigned q0 = pk_bf16(a2[0], a2[1]), q1 = pk_bf16(a2[2], a2[3]);
      LAS u16* p2 = (LAS u16*)Tb + 512 + 64 * g + j;
      p2[0] = (u16)q0; p2[16] = (u16)(q0 >> 16); p2[32] = (u16)q1; p2[48] = (u16)(q1 >> 16);
    }
  }
  lgkm0();
  STAMP(4);
  bar();
  {
    const lptr kc = lds + cl * KC_CH + lane * 16;
    auto emit = [&](int Y0, unsigned P0, unsigned P1, unsigned P2, unsigned P3, unsigned P4) {
      *(LAS u32x4*)(kc + Y0 * 16) = u32x4{P0, P1, P2, P3};
      *(LAS u32x4*)(kc + KC_CP + Y0 * 16) = u32x4{P1, P2, P3, P4};
    };
    if (half == 0) {
      const LAS unsigned* p = (const LAS unsigned*)(Tb + ((575 - lane) >> 1) * 4);
      const unsigned D0 = p[32], D1 = p[33], F0 = p[0], F1 = p[1], Z = *(const LAS unsigned*)Tb;
      SB;
      const unsigned e1 = perm(D1, D0, selB), e0 = perm(D1, D0, selA), f1 = perm(F1, F0, selB), f0 = perm(F1, F0, selA);
      emit(0, e1, e1, perm(D1, D0, selC), e0, e0);
      emit(64, f1, f1, perm(F1, F0, selC), f0, f0);
      if (lane < 4) emit(256, lane == 0 ? perm(0, Z, SWL) : 0u, 0u, 0u, 0u, 0u);
    } else {
      const LAS unsigned* p = (const LAS unsigned*)(Tb + (255 - lane) * 4);
      const LAS unsigned* r = (const LAS unsigned*)(Tb + (lane < 32 ? 760 - 8 * lane : 1008 - 16 * lane));
      const unsigned F0 = p[0], F1 = p[1], D0 = r[0], D1 = r[1], D2 = r[2], D3 = r[3], D4 = r[4];
      SB;
      const unsigned e2 = perm(F1, F0, HL);
      emit(128, e2, e2, perm(F1, F0, LH), perm(F1, F0, SWL), perm(F1, F0, LL));
      emit(192, perm(D4, D2, selA), perm(lane < 32 ? D2 : D3, D1, selB), perm(D2, D1, selC), perm(D1, D0, selB), perm(D1, D0, selE));
    }
  }
  lgkm0();
  STAMP(5);
  bar();
  const int a_ = j;
  const lptr ab = lds + cl * KC_CH + ((a_ & 1) ? KC_CP + 4088 : 4096) - 8 * (a_ >> 1) + 16 * g - 3584 - 192 - half * 256;
  const lptr bb = lds + U_BASE + cl * U_CH + ((a_ & 1) ? 0 : U_CP) + (U_PAD + 32 * (a_ >> 1) + 8 * g) * 2 - 192 - half * 256;
  f32x4 acc[8];
#pragma unroll
  for (int i = 0; i < 8; ++i) acc[i] = f32x4{0.f, 0.f, 0.f, 0.f};
  LAS f32x4* exw = (LAS f32x4*)(lds + EX_BASE + wv * 4096) + lane;
  f32x4 keep[4];
  {
    bf16x8 A[2][8], B[2][8];
#pragma unroll
    for (int r = 0; r < 4; ++r) {
      const int nb = r & 1;
#pragma unroll
      for (int mu = 0; mu < 8; ++mu) {
        B[nb][mu] = lds_b128(bb + 512 * mu + 64 * (3 - r));
        A[nb][mu] = lds_2b64(ab + 512 * (7 - mu) + 64 * (3 - r));
        SB;
        if (r > 0) {
#pragma unroll
          for (int n = 0; n < 8; ++n) if (mu + n < 8) acc[mu + n] = mfma16(A[nb ^ 1][mu], B[nb ^ 1][n], acc[mu + n]);
          SB;
        }
      }
    }
    auto tiles = [&](int t0) {
#pragma unroll
      for (int mu = 0; mu < 8; ++mu)
#pragma unroll
        for (int n = 0; n < 8; ++n) if (mu + n >= t0 && mu + n < t0 + 4) acc[mu + n] = mfma16(A[1][mu], B[1][n], acc[mu + n]);
    };
    if (half) {
      tiles(0); SB; tiles(4);
#pragma unroll
      for (int i = 0; i < 4; ++i) { exw[64 * i] = acc[i]; keep[i] = acc[4 + i]; }
    } else {
      tiles(4); SB; tiles(0);
#pragma unroll
      for (int i = 0; i < 4; ++i) { exw[64 * i] = acc[4 + i]; keep[i] = acc[i]; }
    }
  }
  STAMP(6);
  {
    const LAS f32x4* exr = (const LAS f32x4*)(lds + EX_BASE + (wv ^ 4) * 4096) + lane;
    const rsrc_t wsr = make_rsrc(ws, WS_USED);
    const unsigned bto = WS_BT + d * 4096 + half * 2048 + (a_ >> 2) * 128 + g * 32 + (a_ & 3) * 8;
    lgkm0();
    STAMP(7);
    bar();
#pragma unroll
    for (int i = 0; i < 4; ++i) {
      const f32x4 v = keep[i] + exr[64 * i];
      st64(wsr, bto + 512 * i, u32x2{pk_bf16(v[0], v[1]), pk_bf16(v[2], v[3])});
    }
  }
  STAMP(8);
  STAMP_FLUSH;
}

__global__ void __launch_bounds__(512, 2) k1(const u16* __restrict__ x, const u16* __restrict__ wqkv, const u16* __restrict__ gw,
                                             const u16* __restrict__ lar, const u16* __restrict__ aim, const u16* __restrict__ cre,
                                             const u16* __restrict__ cim, const u16* __restrict__ ldt, const u16* __restrict__ dsk,
                                             char* ws
                                             , u64* stamps
) {
  extern __shared__ __attribute__((aligned(16))) char lds_[];
  const lptr lds = (lptr)lds_;
  const int b = blockIdx.x, xcd = b & 7, idx = b >> 3;
  unsigned cpf;
  CODE_PREFETCH("k1", cpf);
  if (idx < 16) {
    conv_role(lds, x, lar, aim, cre, cim, ldt, dsk, ws, xcd * 64 + idx * 4, idx, stamps);
  } else {
    const int rl = idx & 3, cl = (idx >> 2) & 3;
    gemm_role(lds, x, wqkv, gw, ws, (xcd >> 1) * 4 + rl, (xcd & 1) * 4 + cl, 2 * ((rl + cl) & 3), stamps);
  }
  PIN(cpf);
  CODE_END("k1");
}

__global__ void __launch_bounds__(256) kr(char* ws) {
  const int i = blockIdx.x * 256 + threadIdx.x;
  if (i < 4352) {
    const f32x2* p = (const f32x2*)(ws + WS_KVP) + i;
    f32x2 s = {0.f, 0.f};
#pragma unroll
    for (int r = 0; r < 16; ++r) s += p[r * 4352];
    ((unsigned*)(ws + WS_KVZ))[i] = pk_bf16(s.x, s.y);
  }
}

constexpr int L_PQ = 0, L_WO = 65536, L_KVZ = 131072, L_BT = L_KVZ + 17408, K2_LDS = L_BT + 8192;

__global__ void __launch_bounds__(512, 2) k2(const u16* __restrict__ wo, const char* __restrict__ ws, u16* __restrict__ out
                                             , u64* stamps
) {
  STAMP_DECL;
  extern __shared__ __attribute__((aligned(16))) char lds_[];
  const lptr lds = (lptr)lds_;
  const int b = blockIdx.x, xcd = b & 7, idx = b >> 3;
  const int rl = idx & 7, cl = idx >> 3, rt = (xcd >> 1) * 8 + rl, ct = (xcd & 1) * 4 + cl, rot = (rl + 2 * cl) & 7;
  const int tid = threadIdx.x, lane = tid & 63;
  const int wv = __builtin_amdgcn_readfirstlane(tid >> 6), wr = wv & 1, wc = (wv >> 1) & 1, par = wv >> 2;
  const unsigned r8 = tid >> 3, sp = ((tid & 7) ^ (((r8 >> 1) & 1) << 2)) << 4;
  const lptr lw = lds + wv * 1024;
  STAMP(0);
  unsigned cpf;
  CODE_PREFETCH("k2", cpf);
  const int h = lane >> 5, t = lane & 31;
  float gate;
  const int trow = rt * 64 + sig(wr, t);
  asm volatile("global_load_dword %0, %1, %2" : "=v"(gate) : "v"(trow * 4), "s"(ws + WS_G));
  const u32x4 wsr = mk_rsrc(ws, WS_USED), wor = mk_rsrc(wo, 512 * 1024);
  dma16(wsr, tid * 16, WS_KVZ, lw + L_KVZ);
  dma16(wsr, tid * 16, WS_KVZ + 8192, lw + L_KVZ + 8192);
  if (wv == 0) dma16(wsr, tid * 16, WS_KVZ + 16384, lw + L_KVZ + 16384);
  const unsigned pqo = WS_PQF + rt * 65536 + (tid >> 8) * 32768 + (tid & 255) * 16, woo = ct * 128 + r8 * 1024 + sp;
  const lptr lpq = lds + L_PQ + (wv >> 2) * 32768 + (wv & 3) * 1024;
  auto issue = [&](int gidx) {
    const int gr = (gidx + rot) & 7;
    dma16(wsr, pqo, gr * 4096, lpq + gr * 4096);
    dma16(wor, woo, gr * 65536, lw + L_WO + gr * 8192);
  };
  issue(0); issue(1);
  bar();
  issue(2); issue(3); issue(4);
  const int gi = lane >> 4, blk = gi & 1, q = (lane & 15) >> 2, p4 = lane & 3;
  const lptr kvb = lds + L_KVZ + (t < 16 ? t : 16) * 32 + h * 16 + par * 1088;
  const lptr pqb = lds + L_PQ + wr * 32768 + lane * 16 + par * 2048;
  const lptr wob = lds + L_WO + (4 * h + q) * 128 + (((4 * wc + 2 * blk + (p4 >> 1)) ^ ((q >> 1) << 2)) << 4) + (p4 & 1) * 8 + par * 4096;
  f32x16 acc = {};
  f32x16 nd[2];
  bf16x8 wf[2][2], kf[2], pf[2];
#pragma unroll
  for (int gidx = 0; gidx <= 8; ++gidx) {
    lptr wp;
    if (gidx < 8) {
      const int hd0 = ((gidx + rot) & 7) * 4;
      const lptr kp = kvb + hd0 * 544, pp = pqb + hd0 * 1024;
      wp = wob + hd0 * 2048;
      if (gidx <= 3) WAIT_VM(8); else if (gidx == 4) WAIT_VM(7); else if (gidx == 5) WAIT_VM(5); else if (gidx == 6) WAIT_VM(3); else WAIT_VM(1);
      bar();
      if (gidx == 0) STAMP(1);
      SB;
#pragma unroll
      for (int i = 0; i < 2; ++i) { kf[i] = lds_b128(kp + i * 544); pf[i] = lds_b128(pp + i * 1024); }
      SB;
      if (gidx + 5 < 8) issue(gidx + 5);
      if (gidx == 3) dma16(wsr, r8 * 4096 + sp, WS_BT + ct * 64 * 4096 + rt * 128, lw + L_BT);
      SB;
    }
#pragma unroll
    for (int i = 0; i < 2; ++i) {
      if (gidx > 0) {
        PIN(nd[i]);
        const float r = __builtin_amdgcn_rcpf(nd[i][8] + 1e-6f);
#pragma unroll
        for (int e = 0; e < 8; ++e) nd[i][e] *= r;
        acc = mfma32(wf[(gidx - 1) & 1][i], pack8(nd[i], 0), acc);
      }
      if (gidx < 8) {
        const f32x16 zero = {};
        nd[i] = mfma32(kf[i], pf[i], zero);
        wf[gidx & 1][i] = lds_tr2(wp + i * 2048, wp + i * 2048 + 1024);
      }
      SB;
    }
  }
  STAMP(2);
  lgkm0();
  WAIT_VM(0);
  bar();
  {
    LAS f32x4* xw = (LAS f32x4*)(lds + L_PQ + wv * 2048) + lane;
    const LAS f32x4* xr = (const LAS f32x4*)(lds + L_PQ + (wv ^ 4) * 2048) + lane;
    f32x4 keep[2];
#pragma unroll
    for (int G = 0; G < 2; ++G) {
      const f32x4 lo = {acc[4 * G], acc[4 * G + 1], acc[4 * G + 2], acc[4 * G + 3]};
      const f32x4 hi = {acc[8 + 4 * G], acc[9 + 4 * G], acc[10 + 4 * G], acc[11 + 4 * G]};
      if (par) { xw[64 * G] = lo; keep[G] = hi; } else { xw[64 * G] = hi; keep[G] = lo; }
    }
    const lptr bb = lds + L_BT + (wc * 32 + par * 16 + 4 * h + q) * 128 + (((4 * wr + 2 * blk + (p4 >> 1)) ^ ((q >> 1) << 2)) << 4) + (p4 & 1) * 8;
    const u32x2 bv0 = __builtin_bit_cast(u32x2, lds_tr(bb)), bv1 = __builtin_bit_cast(u32x2, lds_tr(bb + 1024));
    lgkm0();
    bar();
    PIN(gate);
    u32x2 o[2];
#pragma unroll
    for (int G = 0; G < 2; ++G) {
      const f32x4 a = keep[G] + xr[64 * G];
      const unsigned b0 = G ? bv1.x : bv0.x, b1 = G ? bv1.y : bv0.y;
      const float bf[4] = {bf2f(b0), bfhi(b0), bf2f(b1), bfhi(b1)};
      float r[4];
#pragma unroll
      for (int e = 0; e < 4; ++e) r[e] = bf[e] + gate * (a[e] - bf[e]);
      o[G] = u32x2{pk_bf16(r[0], r[1]), pk_bf16(r[2], r[3])};
    }
    char* dst = (char*)out + (unsigned)(trow * 1024 + ct * 128 + wc * 64 + par * 32 + h * 16);
    auto sx = __builtin_amdgcn_permlane32_swap(o[0].x, o[1].x, false, false);
    auto sy = __builtin_amdgcn_permlane32_swap(o[0].y, o[1].y, false, false);
    const u32x4 v = {sx[0], sy[0], sx[1], sy[1]};
    *(u32x4*)dst = v;
  }
  STAMP(3);
  STAMP_FLUSH;
  if (blockIdx.x == 0 && wv == 0) {
    const u64* s1 = stamps - 32768;
    u64 mn = ~0ull; u64 sum = 0; unsigned cnt = 0; u64 mx = 0;
    for (int i = lane; i < 2048; i += 64) {
      const u64 a = s1[i * 16]; mn = a < mn ? a : mn;
      const int role = ((i >> 3) >> 3) >= 16;
      const u64 v = s1[i * 16 + PROBE_S];
      if (role == PROBE_ROLE && v) { sum += v; cnt++; mx = v > mx ? v : mx; }
    }
    for (int o = 32; o; o >>= 1) { u64 t = __shfl_xor(mn, o); mn = t < mn ? t : mn; sum += __shfl_xor(sum, o); cnt += __shfl_xor(cnt, o); t = __shfl_xor(mx, o); mx = t > mx ? t : mx; }
    u64 d = PROBE_K == 0 ? 0 : PROBE_K == 1 ? sum / cnt - mn : PROBE_K == 3 ? mx - mn : st_[PROBE_S] - mn;
    if (d > 5000) d = 5000;
    const u64 t0 = __builtin_amdgcn_s_memrealtime();
    while (__builtin_amdgcn_s_memrealtime() - t0 < 4 * d) __builtin_amdgcn_s_sleep(1);
  }
  PIN(cpf);
  CODE_END("k2");
}

#define STARG(o) , (u64*)(ws + (8u << 20)) + (o)

extern "C" void kernel_launch(void* const* d_in, const int* in_sizes, int n_in, void* d_out, int out_size, void* d_ws,
                              size_t ws_size, hipStream_t stream) {
  const u16* x = (const u16*)d_in[0];
  const u16* wqkv = (const u16*)d_in[1];
  const u16* wo = (const u16*)d_in[2];
  const u16* lar = (const u16*)d_in[4];
  const u16* aim = (const u16*)d_in[5];
  const u16* cre = (const u16*)d_in[6];
  const u16* cim = (const u16*)d_in[7];
  const u16* ldt = (const u16*)d_in[8];
  const u16* dsk = (const u16*)d_in[9];
  const u16* gw = (const u16*)d_in[10];
  char* ws = (char*)d_ws;
  (void)hipFuncSetAttribute((const void*)k1, hipFuncAttributeMaxDynamicSharedMemorySize, 163840);
  (void)hipFuncSetAttribute((const void*)k2, hipFuncAttributeMaxDynamicSharedMemorySize, K2_LDS);
  k1<<<256, 512, 163840, stream>>>(x, wqkv, gw, lar, aim, cre, cim, ldt, dsk, ws STARG(0));
  kr<<<17, 256, 0, stream>>>(ws);
  k2<<<256, 512, K2_LDS, stream>>>(wo, ws, (u16*)d_out STARG(32768));
}
```

```cpp
#define STAMPS
#define PROBE_K 1
#define PROBE_ROLE 0
#define PROBE_S 0
#include <hip/hip_runtime.h>
#include <stdint.h>

typedef unsigned short u16;
typedef unsigned long long u64;
typedef __attribute__((ext_vector_type(8))) __bf16 bf16x8;
typedef __attribute__((ext_vector_type(2))) __bf16 bf16x2;
typedef __attribute__((ext_vector_type(8))) _Float16 f16x8;
typedef __attribute__((ext_vector_type(2))) _Float16 f16x2;
typedef __attribute__((ext_vector_type(4))) short s16x4;
typedef __attribute__((ext_vector_type(16))) float f32x16;
typedef __attribute__((ext_vector_type(4))) float f32x4;
typedef __attribute__((ext_vector_type(2))) float f32x2;
typedef __attribute__((ext_vector_type(4))) unsigned u32x4;
typedef __attribute__((ext_vector_type(2))) unsigned u32x2;
#define LAS __attribute__((address_space(3)))
#define CAS __attribute__((address_space(4)))
#define DI __device__ __forceinline__

constexpr unsigned WS_PQF = 0;
constexpr unsigned WS_BT  = 2u << 20;
constexpr unsigned WS_KVP = 4u << 20;
constexpr unsigned WS_KVZ = 5u << 20;
constexpr unsigned WS_G   = (5u << 20) + 65536;
constexpr unsigned WS_USED = 6u << 20;

constexpr float LOG2E = 1.4426950408889634f;

#define STAMP_INS "s_memrealtime"
#define STAMP_DECL u64 st_[12] = {}
#define STAMP(i) do { __builtin_amdgcn_sched_barrier(0); asm volatile(STAMP_INS " %0\n\ts_waitcnt lgkmcnt(0)" : "=s"(st_[i]) :: "memory"); __builtin_amdgcn_sched_barrier(0); } while (0)
#define STAMP_FLUSH do { if ((threadIdx.x & 63) == 0) { _Pragma("unroll") for (int i_ = 0; i_ < 12; ++i_) stamps[(blockIdx.x * 8 + (threadIdx.x >> 6)) * 16 + i_] = st_[i_]; } } while (0)
#define SB __builtin_amdgcn_sched_barrier(0)
#define PIN(x) asm volatile("" : "+v"(x))
#define PINS(x) asm volatile("" : "+s"(x))

typedef LAS char* lptr;
DI float bf2f(unsigned v) { return __uint_as_float(v << 16); }
DI float bfhi(unsigned v) { return __uint_as_float(v & 0xffff0000u); }
DI unsigned pk_bf16(float a, float b) { f32x2 v = {a, b}; return __builtin_bit_cast(unsigned, __builtin_convertvector(v, bf16x2)); }
DI float rbf(float a) { return __uint_as_float(pk_bf16(a, a) << 16); }
DI f16x2 pk_f16(f32x2 v) { return __builtin_convertvector(v, f16x2); }
DI f32x2 cmul(f32x2 a, f32x2 b) { f32x2 t = a.xx * b; f32x2 bs = {-b.y, b.x}; return __builtin_elementwise_fma(a.yy, bs, t); }
DI void cmul2(f32x2& r0, f32x2 a0, f32x2 b0, f32x2& r1, f32x2 a1, f32x2 b1) {
  f32x2 t0, t1;
  asm("v_pk_mul_f32 %0, %2, %3 op_sel_hi:[0,1]\n\tv_pk_mul_f32 %1, %4, %5 op_sel_hi:[0,1]\n\t"
      "v_pk_fma_f32 %0, %2, %3, %0 op_sel:[1,1,0] op_sel_hi:[1,0,1] neg_lo:[0,1,0]\n\t"
      "v_pk_fma_f32 %1, %4, %5, %1 op_sel:[1,1,0] op_sel_hi:[1,0,1] neg_lo:[0,1,0]"
      : "=&v"(t0), "=&v"(t1) : "v"(a0), "v"(b0), "v"(a1), "v"(b1));
  r0 = t0; r1 = t1;
}
DI f16x2 pk_f16c(f32x2 v) { const f32x2 c = {v.x, -v.y}; return __builtin_convertvector(c, f16x2); }
DI f16x2 cmulh(f16x2 a, f16x2 b) { f16x2 t = a.xx * b; f16x2 bs = {-b.y, b.x}; return __builtin_elementwise_fma(a.yy, bs, t); }
DI u32x4 mk_rsrc(const void* p, unsigned bytes) { const u64 a = (u64)p; return u32x4{(unsigned)a, (unsigned)(a >> 32), bytes, 0x00020000u}; }
DI void dma16(u32x4 rs, unsigned voff, unsigned soff, lptr l) {
  asm volatile("s_mov_b32 m0, %3\n\ts_nop 0\n\tbuffer_load_dwordx4 %0, %1, %2 offen lds"
               :: "v"(voff), "s"(rs), "s"(soff), "s"((unsigned)(size_t)l) : "memory");
}
DI int sig(int w, int n) { return 32 * ((n >> 3) & 1) + ((n >> 2) & 1) + 16 * w + 8 * (n >> 4) + 2 * (n & 3); }
DI bf16x8 lds_b128(lptr p) { return *(const LAS bf16x8*)p; }
DI s16x4 lds_tr(lptr p) { return __builtin_amdgcn_ds_read_tr16_b64_v4i16((LAS s16x4*)p); }
DI bf16x8 lds_tr2(lptr p0, lptr p1) { s16x4 a = lds_tr(p0), b = lds_tr(p1); return __builtin_bit_cast(bf16x8, __builtin_shufflevector(a, b, 0, 1, 2, 3, 4, 5, 6, 7)); }
DI f32x16 mfma32(bf16x8 a, bf16x8 b, f32x16 c) { return __builtin_amdgcn_mfma_f32_32x32x16_bf16(a, b, c, 0, 0, 0); }
DI f32x4 mfma16(bf16x8 a, bf16x8 b, f32x4 c) { return __builtin_amdgcn_mfma_f32_16x16x32_bf16(a, b, c, 0, 0, 0); }
DI f32x4 mfma16h(f16x8 a, f16x8 b, f32x4 c) { return __builtin_amdgcn_mfma_f32_16x16x32_f16(a, b, c, 0, 0, 0); }
DI void bar() { asm volatile("" ::: "memory"); __builtin_amdgcn_s_barrier(); asm volatile("" ::: "memory"); }
DI void lgkm0() { asm volatile("s_waitcnt lgkmcnt(0)" ::: "memory"); }
#define WAIT_VM(n) asm volatile("s_waitcnt vmcnt(" #n ")" ::: "memory")
DI float phi(float tp) {
  const float e = __builtin_amdgcn_fmed3f(__builtin_amdgcn_exp2f(tp * LOG2E), 0.f, 1.f);
  const int ti = __float_as_int(tp);
  return __int_as_float(ti > 0x358637bd ? ti : 0x358637bd) + e;
}
DI bf16x8 pack8(const f32x16& a, int o) {
  u32x4 p = {pk_bf16(a[o], a[o + 1]), pk_bf16(a[o + 2], a[o + 3]), pk_bf16(a[o + 4], a[o + 5]), pk_bf16(a[o + 6], a[o + 7])};
  return __builtin_bit_cast(bf16x8, p);
}
template <int CTRL, int RM = 0xf> DI int dppi(int v) { return __builtin_amdgcn_update_dpp(0, v, CTRL, RM, 0xf, true); }
template <int CTRL, int RM = 0xf> DI float dppf(float v) { return __int_as_float(dppi<CTRL, RM>(__float_as_int(v))); }
DI unsigned umax(unsigned a, unsigned b) { return a > b ? a : b; }
DI unsigned wave_umax(unsigned v) {
  v = umax(v, dppi<0x111>(v)); v = umax(v, dppi<0x112>(v)); v = umax(v, dppi<0x114>(v)); v = umax(v, dppi<0x118>(v));
  v = umax(v, dppi<0x142, 0xa>(v)); v = umax(v, dppi<0x143, 0xc>(v));
  return __builtin_amdgcn_readlane(v, 63);
}
DI float wave_sum(float v) {
  v += dppf<0x111>(v); v += dppf<0x112>(v); v += dppf<0x114>(v); v += dppf<0x118>(v);
  v += dppf<0x142, 0xa>(v); v += dppf<0x143, 0xc>(v);
  return __int_as_float(__builtin_amdgcn_readlane(__float_as_int(v), 63));
}
#define CODE_PREFETCH(NAME, dummy) do { u64 pc_; unsigned len_; \
  asm volatile("s_getpc_b64 %0\n.L" NAME "_beg:\n\ts_mov_b32 %1, .L" NAME "_end-.L" NAME "_beg" : "=s"(pc_), "=s"(len_)); \
  unsigned o_ = threadIdx.x * 64; o_ = o_ < len_ ? o_ : 0; \
  asm volatile("global_load_dword %0, %1, %2" : "=v"(dummy) : "v"(o_), "s"(pc_)); } while (0)
#define CODE_END(NAME) asm volatile(".L" NAME "_end:")
typedef __amdgpu_buffer_rsrc_t rsrc_t;
DI rsrc_t make_rsrc(void* p, unsigned bytes) { return __builtin_amdgcn_make_buffer_rsrc(p, 0, bytes, 0x00020000); }
constexpr int WT = 16;
DI void st128(rsrc_t r, unsigned off, u32x4 v) { __builtin_amdgcn_raw_buffer_store_b128(v, r, off, 0, WT); }
DI void st64(rsrc_t r, unsigned off, u32x2 v) { __builtin_amdgcn_raw_buffer_store_b64(v, r, off, 0, WT); }
DI void st32(rsrc_t r, unsigned off, unsigned v) { __builtin_amdgcn_raw_buffer_store_b32(v, r, off, 0, WT); }

constexpr int SLOT = 32768, NSLOT = 4;
constexpr int GA_EX = NSLOT * SLOT;
constexpr int GA_GW = GA_EX + 8 * 544 * 4;

DI void gemm_role(lptr lds, const u16* __restrict__ x, const u16* __restrict__ w, const u16* __restrict__ gw, char* ws,
                  int rb, int cg, int rot, u64* stamps) {
  STAMP_DECL;
  const int tid = threadIdx.x, lane = tid & 63;
  const int wv = __builtin_amdgcn_readfirstlane(tid >> 6), rg = wv & 3, hp = wv >> 2;
  const u32x4 xr = mk_rsrc(x, 2048 * 1024), wr_ = mk_rsrc(w, 512 * 3072);
  const unsigned xrow = tid >> 3, xo = rb * 131072 + xrow * 1024 + (((tid & 7) ^ ((xrow >> 1) & 7)) << 4);
  const unsigned wrow = tid >> 4, wp = (tid & 15) ^ ((wrow & 3) << 2);
  const unsigned wo_ = cg * 128 + wrow * 3072 + 1024 + (wp >> 3) * 1024 + ((wp & 7) << 4);
  const unsigned qo = cg * 128 + xrow * 3072 + (((tid & 7) ^ (((xrow >> 1) & 1) << 2)) << 4);
  const lptr lw = lds + wv * 1024;
  auto issue = [&](int c, int i) {
    const lptr slot = lw + (c % NSLOT) * SLOT;
    if (c < 8) {
      const unsigned kc = (c + rot) & 7;
      if (i == 0) dma16(xr, xo, kc * 128, slot);
      if (i == 1) dma16(wr_, wo_, kc * (64 * 3072), slot + 16384);
      if (i == 2) dma16(xr, xo, kc * 128 + 65536, slot + 8192);
      if (i == 3) dma16(wr_, wo_, kc * (64 * 3072) + 32 * 3072, slot + 24576);
    } else {
      dma16(wr_, qo, (((c - 8) * 4 + i + rot) & 7) * (64 * 3072), slot + i * 8192);
    }
  };
  STAMP(0);
  const bool do_g = (wv == cg) && (cg < 4);
  if (do_g) dma16(mk_rsrc(gw, 1024), lane * 16, 0, lds + GA_GW);
#pragma unroll
  for (int c = 0; c < 3; ++c) {
#pragma unroll
    for (int i = 0; i < 4; ++i) issue(c, i);
    if (c < 2) bar();
  }
  const int h = lane >> 5, row = (rg >> 1) * 64 + sig(rg & 1, lane & 31);
  const int gi = lane >> 4, blk = gi & 1, q = (lane & 15) >> 2, p4 = lane & 3;
  lptr ap[4], bp[2];
#pragma unroll
  for (int s = 0; s < 4; ++s) ap[s] = lds + row * 128 + (((2 * s + h) ^ ((row >> 1) & 7)) << 4);
#pragma unroll
  for (int ct = 0; ct < 2; ++ct) bp[ct] = lds + 16384 + (8 * h + q) * 256 + (((4 * (2 * ct + hp) + 2 * blk + (p4 >> 1)) ^ (q << 2)) << 4) + (p4 & 1) * 8;
  f32x16 ak, av, ag = {};
#pragma unroll
  for (int i = 0; i < 16; ++i) { ak[i] = 1e-6f; av[i] = 0.f; }
  bf16x8 xf[32], Bf[2][2];
  STAMP(1);
#pragma unroll
  for (int n = 0; n <= 32; ++n) {
    const int c = n >> 2, s = n & 3, nb = n & 1, so = (c & 1) * SLOT;
    if (s == 0 && n < 32) {
      if (c > 0) {
        lgkm0();
        if ((c & 1) == 0) {
          const int dlt = (c == 4) ? -2 * SLOT : 2 * SLOT;
#pragma unroll
          for (int i = 0; i < 4; ++i) { ap[i] += dlt; PIN(ap[i]); }
#pragma unroll
          for (int i = 0; i < 2; ++i) { bp[i] += dlt; PIN(bp[i]); }
        }
      }
      WAIT_VM(8);
      bar();
      if (c == 0) STAMP(2);
      SB;
    }
    if (n > 0) { ak = mfma32(xf[n - 1], Bf[nb ^ 1][0], ak); SB; }
    if (n < 32) { xf[n] = lds_b128(ap[s] + so); Bf[nb][0] = lds_tr2(bp[0] + so + s * 4096, bp[0] + so + s * 4096 + 1024); SB; }
    if (n > 0) { av = mfma32(xf[n - 1], Bf[nb ^ 1][1], av); SB; }
    if (n < 32) { Bf[nb][1] = lds_tr2(bp[1] + so + s * 4096, bp[1] + so + s * 4096 + 1024); SB; }
    if (c + 3 < 10) { issue(c + 3, s); SB; }
    if (n > 0 && s == 0 && do_g) {
      const lptr gp = lds + GA_GW + ((c - 1 + rot) & 7) * 128 + h * 16;
#pragma unroll
      for (int i = 0; i < 4; ++i) ag = mfma32(lds_b128(gp + i * 32), xf[n - 4 + i], ag);
      SB;
    }
  }
  STAMP(3);
  const rsrc_t wsr = make_rsrc(ws, WS_USED);
  {
    bf16x8 ones;
#pragma unroll
    for (int i = 0; i < 8; ++i) ones[i] = (__bf16)1.0f;
    const int dp = lane & 31, d = lane & 15;
    const int pos = ((d >> 2) & 1) * 8 + (d & 3) + 4 * (d >> 3);
    const bool lo = dp < 16;
#pragma unroll
    for (int i = 0; i < 16; ++i) ak[i] = phi(ak[i]);
    f32x16 kv = {}, z = {};
#pragma unroll
    for (int s = 0; s < 2; ++s) {
      const bf16x8 pk = pack8(ak, 8 * s), vv = pack8(av, 8 * s);
      kv = mfma32(vv, pk, kv);
      z = mfma32(ones, pk, z);
    }
    LAS float* e0 = (LAS float*)(lds + GA_EX) + (rg * 4 + 2 * hp + (dp >> 4)) * 272 + pos + 64 * h;
#pragma unroll
    for (int i = 0; i < 8; ++i) {
      float a = kv[i], b = kv[8 + i];
      PIN(a); PIN(b);
      e0[((i & 3) + 8 * (i >> 2)) * 16] = lo ? a : b;
    }
    if (h == 0) e0[256] = z[0];
  }
  STAMP(4);
  f32x16 aq;
#pragma unroll
  for (int i = 0; i < 16; ++i) aq[i] = 1e-6f;
  const lptr qp = lds + (8 * h + q) * 128 + (((4 * hp + 2 * blk + (p4 >> 1)) ^ ((q >> 1) << 2)) << 4) + (p4 & 1) * 8;
  bf16x8 Aq[4];
  constexpr int PD = 3;
#pragma unroll
  for (int n = 0; n < 32 + PD; ++n) {
    if (n == 0) { lgkm0(); WAIT_VM(4); bar(); SB; }
    if (n == 16) { lgkm0(); WAIT_VM(1); bar(); SB; }
    if (n >= PD) { aq = mfma32(Aq[(n - PD) & 3], xf[n - PD], aq); SB; }
    if (n < 32) { Aq[n & 3] = lds_tr2(qp + n * 2048, qp + n * 2048 + 512); SB; }
    if (n == PD) {
      const LAS f32x4* ex = (const LAS f32x4*)(lds + GA_EX) + (tid < 272 ? tid : 0);
      const f32x4 sum = (ex[0] + ex[272]) + (ex[544] + ex[816]);
      st128(wsr, tid < 272 ? WS_KVP + ((rb * 32 + cg * 4) * 68 + tid) * 16 : 0xfffffff0u, __builtin_bit_cast(u32x4, sum));
      SB;
    }
  }
  STAMP(5);
  {
    const unsigned o = WS_PQF + (((rb * 4 + rg) * 32 + cg * 4 + 2 * hp) * 64 + lane) * 16;
#pragma unroll
    for (int i = 0; i < 16; ++i) aq[i] = phi(aq[i]);
    st128(wsr, o, __builtin_bit_cast(u32x4, pack8(aq, 0)));
    st128(wsr, o + 1024, __builtin_bit_cast(u32x4, pack8(aq, 8)));
    if (do_g && h == 0) {
      const float g = __builtin_amdgcn_rcpf(1.f + __builtin_amdgcn_exp2f(-ag[0] * LOG2E));
      st32(wsr, WS_G + (rb * 128 + row) * 4, __float_as_uint(g));
    }
  }
  STAMP(6);
  STAMP_FLUSH;
}

constexpr int SC_ROW = 2064;
constexpr int KC_CP = 4224, KC_CH = 2 * KC_CP;
constexpr int EX_BASE = 4 * KC_CH;
constexpr int U_BASE = 57 * SC_ROW, U_PAD = 224, U_CP = 4576, U_CH = 2 * U_CP;
constexpr int T_BASE = U_BASE + 4 * U_CH, T_CH = 1536;
static_assert(T_BASE + 4 * T_CH <= 163840 && U_BASE % 16 == 0, "lds");

DI f32x2 cexp_p(float l2, float rv) {
  const float m = __builtin_amdgcn_exp2f(l2), f = __builtin_amdgcn_fractf(rv);
  const f32x2 r = {m * __builtin_amdgcn_cosf(f), m * __builtin_amdgcn_sinf(f)};
  return r;
}
DI bf16x8 lds_2b64(lptr p) {
  const u32x2 a = *(const volatile LAS u32x2*)p, b = *(const volatile LAS u32x2*)(p + 8);
  const u32x4 v = {a.x, a.y, b.x, b.y};
  return __builtin_bit_cast(bf16x8, v);
}
DI unsigned perm(unsigned hi, unsigned lo, unsigned sel) { return __builtin_amdgcn_perm(hi, lo, sel); }
constexpr unsigned LL = 0x01000100, LH = 0x03020302, HL = 0x05040504, SWL = 0x01000302, SWH = 0x05040706, HL_LH = 0x03020504;
template <int E0, int E1, int E2, int E3>
DI void put4(unsigned base, f16x2 a, f16x2 b, f16x2 c, f16x2 d) {
  constexpr int O = E0 < 28 ? 0 : 28;
  static_assert((E1 < 28) == (E0 < 28) && (E2 < 28) == (E0 < 28) && (E3 < 28) == (E0 < 28), "one base");
  asm volatile("s_mov_b32 m0, %4\n\ts_nop 0\n\tds_write_addtid_b32 %0 offset:%5\n\tds_write_addtid_b32 %1 offset:%6\n\t"
               "ds_write_addtid_b32 %2 offset:%7\n\tds_write_addtid_b32 %3 offset:%8"
               :: "v"(a), "v"(b), "v"(c), "v"(d), "s"(base + O * SC_ROW), "i"((E0 - O) * SC_ROW), "i"((E1 - O) * SC_ROW),
                  "i"((E2 - O) * SC_ROW), "i"((E3 - O) * SC_ROW) : "memory");
}

DI void conv_role(lptr lds, const u16* __restrict__ x, const u16* __restrict__ lar, const u16* __restrict__ aim,
                  const u16* __restrict__ cre, const u16* __restrict__ cim, const u16* __restrict__ ldt,
                  const u16* __restrict__ dsk, char* ws, int ch0, int jrot, u64* stamps) {
  STAMP_DECL;
  const int tid = threadIdx.x, lane = tid & 63;
  const int wv = __builtin_amdgcn_readfirstlane(tid >> 6), cl = wv & 3, half = wv >> 2;
  const int d = ch0 + cl;
  STAMP(0);
  const unsigned r_lar = ((const unsigned*)lar)[d * 64 + lane], r_aim = ((const unsigned*)aim)[d * 64 + lane];
  const unsigned r_cr = ((const unsigned*)cre)[d * 64 + lane], r_ci = ((const unsigned*)cim)[d * 64 + lane];
  const unsigned s_ldt = ((const CAS unsigned*)ldt)[d >> 1], s_dsk = ((const CAS unsigned*)dsk)[d >> 1];
  bar();
  const int tt = (tid + jrot * 64) & 511;
  const char* xg = (const char*)x + ch0 * 2;
  const unsigned xoff = tt * 4096;
  u32x2 ur[4], up = {0u, 0u};
  ur[0] = *(const u32x2*)(xg + xoff); ur[1] = *(const u32x2*)(xg + xoff + 1024);
  if (lane == 0 && tt > 0) up = *(const u32x2*)(xg + xoff - 1024);
  {
    const u32x4 zero = {0, 0, 0, 0};
    if (lane < 28) *(LAS u32x4*)(lds + U_BASE + cl * U_CH + half * U_CP + lane * 16) = zero;
  }
  const int j = lane & 15, g = lane >> 4;
  const unsigned scb = (unsigned)(size_t)lds + cl * 512 + half * 256;
  const lptr fb = lds + cl * 512 + half * 256 + j * SC_ROW + g * 16;
  unsigned selA = lane < 32 ? LL : lane == 32 ? HL : SWH, selB = lane < 32 ? HL_LH : SWH, selC = lane < 32 ? LL : SWH, selE = lane < 32 ? LL : SWL;
  if (half == 0) { const bool odd = !(lane & 1); selA = odd ? LH : LL; selB = odd ? HL : LH; selC = odd ? HL : SWL; }
  PIN(selA); PIN(selB); PIN(selC); PIN(selE);
  const unsigned shl = half ? 0 : 16;
  const float f_ldt = (d & 1) ? bfhi(s_ldt) : bf2f(s_ldt), f_dsk = (d & 1) ? bfhi(s_dsk) : bf2f(s_dsk);
  const float dt = __int_as_float(__builtin_amdgcn_readfirstlane(__float_as_int(rbf(__expf(f_ldt)))));
  SB;
  STAMP(1);
  const float l2 = -rbf(__builtin_amdgcn_exp2f(bfhi(r_lar << shl) * LOG2E)) * dt * LOG2E;
  const float rv = bfhi(r_aim << shl) * dt * 0.15915494309189535f;
  unsigned cm;
  {
    typedef __attribute__((ext_vector_type(2))) unsigned short u16x2;
    const u16x2 m2 = __builtin_elementwise_max(__builtin_bit_cast(u16x2, r_cr & 0x7fff7fffu), __builtin_bit_cast(u16x2, r_ci & 0x7fff7fffu));
    const unsigned m = __builtin_bit_cast(unsigned, m2);
    cm = umax(wave_umax(umax(m << 16, m)), 0x0da24260u);
  }
  const float cmax = __uint_as_float(cm), cinv = __builtin_amdgcn_rcpf(cmax);
  f32x4 kt[3];
  {
    f16x8 rf[4], t0[4];
    const f16x2 one = {(_Float16)1.f, (_Float16)0.f};
    const f32x2 E1 = cexp_p(l2, rv), E8 = cexp_p(l2 * 8.f, rv * 8.f), E128 = cexp_p(l2 * 128.f, rv * 128.f);
    f32x2 E2, E3, E4, E16, E24, E32, E64, E96, E256, E384, E512, H1, H2, H3, H4, dummy;
    const f32x2 H0 = {bfhi(r_cr << shl) * cinv, bfhi(r_ci << shl) * cinv};
    cmul2(E16, E8, E8, E256, E128, E128);
    cmul2(E32, E16, E16, E512, E256, E256);
    cmul2(E24, E16, E8, E2, E1, E1);
    cmul2(E64, E32, E32, H1, H0, E512);
    cmul2(E3, E2, E1, E4, E2, E2);
    cmul2(E96, E64, E32, H2, H1, E512);
    cmul2(E384, E256, E128, H3, H2, E512);
    ur[2] = *(const u32x2*)(xg + xoff + 2048); ur[3] = *(const u32x2*)(xg + xoff + 3072);
    {
      const f16x2 P1 = pk_f16c(E8), P2 = pk_f16c(E16), P3 = pk_f16c(E24), Q1 = pk_f16c(E32), Q2 = pk_f16c(E64), Q3 = pk_f16c(E96);
      put4<0, 1, 2, 3>(scb, one, P1, P2, P3);
      put4<4, 5, 6, 7>(scb, Q1, cmulh(Q1, P1), cmulh(Q1, P2), cmulh(Q1, P3));
      put4<8, 9, 10, 11>(scb, Q2, cmulh(Q2, P1), cmulh(Q2, P2), cmulh(Q2, P3));
      put4<12, 13, 14, 15>(scb, Q3, cmulh(Q3, P1), cmulh(Q3, P2), cmulh(Q3, P3));
    }
    H4 = cmul(H3, E512); (void)dummy;
    const f16x2 e1 = pk_f16(E1), e2 = pk_f16(E2), e3 = pk_f16(E3), e4 = pk_f16(E4);
    const f16x2 e5 = cmulh(e4, e1), e6 = cmulh(e4, e2), e7 = cmulh(e4, e3);
    const f16x2 S1 = pk_f16(E128), S2 = pk_f16(E256), S3 = pk_f16(E384);
    const f16x2 h0 = pk_f16(H0), h1 = pk_f16(H1), h2 = pk_f16(H2), h3 = pk_f16(H3);
    {
      const f16x2 G1 = cmulh(h0, S1);
      put4<16, 17, 18, 19>(scb, h0, cmulh(h0, e1), cmulh(h0, e2), cmulh(h0, e3));
      put4<20, 21, 22, 23>(scb, cmulh(h0, e4), cmulh(h0, e5), cmulh(h0, e6), cmulh(h0, e7));
      put4<24, 25, 26, 27>(scb, G1, cmulh(G1, e1), cmulh(G1, e2), cmulh(G1, e3));
      put4<28, 29, 30, 31>(scb, cmulh(G1, e4), cmulh(G1, e5), cmulh(G1, e6), cmulh(G1, e7));
    }
#pragma unroll
    for (int s = 0; s < 4; ++s) { rf[s] = *(const LAS f16x8*)(fb + s * 64); t0[s] = *(const LAS f16x8*)(fb + 16 * SC_ROW + s * 64); }
    SB;
    {
      const f16x2 G2 = cmulh(h0, S2), G3 = cmulh(h0, S3), G5 = cmulh(h1, S1), G6 = cmulh(h1, S2), G7 = cmulh(h1, S3);
      put4<32, 33, 34, 35>(scb, G2, cmulh(G2, e2), cmulh(G2, e4), cmulh(G2, e6));
      kt[0] = mfma16h(t0[0], rf[0], f32x4{0.f, 0.f, 0.f, 0.f});
      kt[0] = mfma16h(t0[1], rf[1], kt[0]);
      put4<36, 37, 38, 39>(scb, G3, cmulh(G3, e2), cmulh(G3, e4), cmulh(G3, e6));
      kt[0] = mfma16h(t0[2], rf[2], kt[0]);
      put4<40, 41, 42, 43>(scb, h1, cmulh(h1, e4), G5, cmulh(G5, e4));
      kt[0] = mfma16h(t0[3], rf[3], kt[0]);
      put4<44, 45, 46, 47>(scb, G6, cmulh(G6, e4), G7, cmulh(G7, e4));
    }
#pragma unroll
    for (int s = 0; s < 4; ++s) t0[s] = *(const LAS f16x8*)(fb + 32 * SC_ROW + s * 64);
    SB;
    put4<48, 49, 50, 51>(scb, h2, cmulh(h2, S1), cmulh(h2, S2), cmulh(h2, S3));
    put4<52, 53, 54, 55>(scb, h3, cmulh(h3, S1), cmulh(h3, S2), cmulh(h3, S3));
    *(LAS f16x2*)(lds + cl * 512 + half * 256 + lane * 4 + 56 * SC_ROW) = pk_f16(H4);
    f16x8 t2[4];
#pragma unroll
    for (int s = 0; s < 4; ++s) t2[s] = *(const LAS f16x8*)(fb + 48 * SC_ROW + s * 64);
    SB;
    kt[1] = f32x4{0.f, 0.f, 0.f, 0.f}; kt[2] = kt[1];
#pragma unroll
    for (int s = 0; s < 4; ++s) kt[1] = mfma16h(t0[s], rf[s], kt[1]);
#pragma unroll
    for (int s = 0; s < 4; ++s) kt[2] = mfma16h(t2[s], rf[s], kt[2]);
  }
  STAMP(2);
  {
    const u32x2 pv = {(unsigned)__builtin_amdgcn_update_dpp((int)up.x, (int)ur[3].x, 0x138, 0xf, 0xf, false),
                      (unsigned)__builtin_amdgcn_update_dpp((int)up.y, (int)ur[3].y, 0x138, 0xf, 0xf, false)};
    const lptr ub = lds + U_BASE + (U_PAD + 4 * tt) * 2;
#pragma unroll
    for (int c = 0; c < 4; ++c) {
      const unsigned sel = (c & 1) ? 0x07060302 : 0x05040100;
      const unsigned p = pv[c >> 1], r0 = ur[0][c >> 1], r1 = ur[1][c >> 1], r2 = ur[2][c >> 1], r3 = ur[3][c >> 1];
      *(LAS u32x2*)(ub + c * U_CH) = u32x2{perm(r1, r0, sel), perm(r3, r2, sel)};
      *(LAS u32x2*)(ub + c * U_CH + U_CP) = u32x2{perm(r0, p, sel), perm(r2, r1, sel)};
    }
  }
  const lptr Tb = lds + T_BASE + cl * T_CH;
  {
    LAS f32x4* xw = (LAS f32x4*)(lds + cl * 512 + half * 256 + g * SC_ROW) + j;
    const LAS f32x4* xr = (const LAS f32x4*)(lds + cl * 512 + (half ^ 1) * 256 + g * SC_ROW) + j;
    if (half) xw[0] = kt[0]; else { xw[0] = kt[1]; xw[4 * SC_ROW / 16] = kt[2]; }
    const float ks = 2.f * dt * cmax;
    const lptr p1 = half ? Tb + (256 + 64 * g + (g < 2 ? 4 : 2) * j) * 2 : Tb + (128 * (g >> 1) + 4 * (g & 1) + 8 * j) * 2;
    lptr p1b = p1 + (g < 2 ? 4 : 64);
    PIN(p1b);
    lgkm0();
    STAMP(3);
    bar();
    if (half == 0) {
      f32x4 a0 = (kt[0] + xr[0]) * ks;
      if (lane == 0) a0[0] += f_dsk;
      *(LAS u32x2*)p1 = u32x2{pk_bf16(a0[0], a0[1]), pk_bf16(a0[2], a0[3])};
    } else {
      const f32x4 a1 = (kt[1] + xr[0]) * ks, a2 = (kt[2] + xr[4 * SC_ROW / 16]) * ks;
      *(LAS unsigned*)p1 = pk_bf16(a1[0], a1[1]);
      *(LAS unsigned*)p1b = pk_bf16(a1[2], a1[3]);
      const unsigned q0 = pk_bf16(a2[0], a2[1]), q1 = pk_bf16(a2[2], a2[3]);
      LAS u16* p2 = (LAS u16*)Tb + 512 + 64 * g + j;
      p2[0] = (u16)q0; p2[16] = (u16)(q0 >> 16); p2[32] = (u16)q1; p2[48] = (u16)(q1 >> 16);
    }
  }
  lgkm0();
  STAMP(4);
  bar();
  {
    const lptr kc = lds + cl * KC_CH + lane * 16;
    auto emit = [&](int Y0, unsigned P0, unsigned P1, unsigned P2, unsigned P3, unsigned P4) {
      *(LAS u32x4*)(kc + Y0 * 16) = u32x4{P0, P1, P2, P3};
      *(LAS u32x4*)(kc + KC_CP + Y0 * 16) = u32x4{P1, P2, P3, P4};
    };
    if (half == 0) {
      const LAS unsigned* p = (const LAS unsigned*)(Tb + ((575 - lane) >> 1) * 4);
      const unsigned D0 = p[32], D1 = p[33], F0 = p[0], F1 = p[1], Z = *(const LAS unsigned*)Tb;
      SB;
      const unsigned e1 = perm(D1, D0, selB), e0 = perm(D1, D0, selA), f1 = perm(F1, F0, selB), f0 = perm(F1, F0, selA);
      emit(0, e1, e1, perm(D1, D0, selC), e0, e0);
      emit(64, f1, f1, perm(F1, F0, selC), f0, f0);
      if (lane < 4) emit(256, lane == 0 ? perm(0, Z, SWL) : 0u, 0u, 0u, 0u, 0u);
    } else {
      const LAS unsigned* p = (const LAS unsigned*)(Tb + (255 - lane) * 4);
      const LAS unsigned* r = (const LAS unsigned*)(Tb + (lane < 32 ? 760 - 8 * lane : 1008 - 16 * lane));
      const unsigned F0 = p[0], F1 = p[1], D0 = r[0], D1 = r[1], D2 = r[2], D3 = r[3], D4 = r[4];
      SB;
      const unsigned e2 = perm(F1, F0, HL);
      emit(128, e2, e2, perm(F1, F0, LH), perm(F1, F0, SWL), perm(F1, F0, LL));
      emit(192, perm(D4, D2, selA), perm(lane < 32 ? D2 : D3, D1, selB), perm(D2, D1, selC), perm(D1, D0, selB), perm(D1, D0, selE));
    }
  }
  lgkm0();
  STAMP(5);
  bar();
  const int a_ = j;
  const lptr ab = lds + cl * KC_CH + ((a_ & 1) ? KC_CP + 4088 : 4096) - 8 * (a_ >> 1) + 16 * g - 3584 - 192 - half * 256;
  const lptr bb = lds + U_BASE + cl * U_CH + ((a_ & 1) ? 0 : U_CP) + (U_PAD + 32 * (a_ >> 1) + 8 * g) * 2 - 192 - half * 256;
  f32x4 acc[8];
#pragma unroll
  for (int i = 0; i < 8; ++i) acc[i] = f32x4{0.f, 0.f, 0.f, 0.f};
  LAS f32x4* exw = (LAS f32x4*)(lds + EX_BASE + wv * 4096) + lane;
  f32x4 keep[4];
  {
    bf16x8 A[2][8], B[2][8];
#pragma unroll
    for (int r = 0; r < 4; ++r) {
      const int nb = r & 1;
#pragma unroll
      for (int mu = 0; mu < 8; ++mu) {
        B[nb][mu] = lds_b128(bb + 512 * mu + 64 * (3 - r));
        A[nb][mu] = lds_2b64(ab + 512 * (7 - mu) + 64 * (3 - r));
        SB;
        if (r > 0) {
#pragma unroll
          for (int n = 0; n < 8; ++n) if (mu + n < 8) acc[mu + n] = mfma16(A[nb ^ 1][mu], B[nb ^ 1][n], acc[mu + n]);
          SB;
        }
      }
    }
    auto tiles = [&](int t0) {
#pragma unroll
      for (int mu = 0; mu < 8; ++mu)
#pragma unroll
        for (int n = 0; n < 8; ++n) if (mu + n >= t0 && mu + n < t0 + 4) acc[mu + n] = mfma16(A[1][mu], B[1][n], acc[mu + n]);
    };
    if (half) {
      tiles(0); SB; tiles(4);
#pragma unroll
      for (int i = 0; i < 4; ++i) { exw[64 * i] = acc[i]; keep[i] = acc[4 + i]; }
    } else {
      tiles(4); SB; tiles(0);
#pragma unroll
      for (int i = 0; i < 4; ++i) { exw[64 * i] = acc[4 + i]; keep[i] = acc[i]; }
    }
  }
  STAMP(6);
  {
    const LAS f32x4* exr = (const LAS f32x4*)(lds + EX_BASE + (wv ^ 4) * 4096) + lane;
    const rsrc_t wsr = make_rsrc(ws, WS_USED);
    const unsigned bto = WS_BT + d * 4096 + half * 2048 + (a_ >> 2) * 128 + g * 32 + (a_ & 3) * 8;
    lgkm0();
    STAMP(7);
    bar();
#pragma unroll
    for (int i = 0; i < 4; ++i) {
      const f32x4 v = keep[i] + exr[64 * i];
      st64(wsr, bto + 512 * i, u32x2{pk_bf16(v[0], v[1]), pk_bf16(v[2], v[3])});
    }
  }
  STAMP(8);
  STAMP_FLUSH;
}

__global__ void __launch_bounds__(512, 2) k1(const u16* __restrict__ x, const u16* __restrict__ wqkv, const u16* __restrict__ gw,
                                             const u16* __restrict__ lar, const u16* __restrict__ aim, const u16* __restrict__ cre,
                                             const u16* __restrict__ cim, const u16* __restrict__ ldt, const u16* __restrict__ dsk,
                                             char* ws
                                             , u64* stamps
) {
  extern __shared__ __attribute__((aligned(16))) char lds_[];
  const lptr lds = (lptr)lds_;
  const int b = blockIdx.x, xcd = b & 7, idx = b >> 3;
  unsigned cpf;
  CODE_PREFETCH("k1", cpf);
  if (idx < 16) {
    conv_role(lds, x, lar, aim, cre, cim, ldt, dsk, ws, xcd * 64 + idx * 4, idx, stamps);
  } else {
    const int rl = idx & 3, cl = (idx >> 2) & 3;
    gemm_role(lds, x, wqkv, gw, ws, (xcd >> 1) * 4 + rl, (xcd & 1) * 4 + cl, 2 * ((rl + cl) & 3), stamps);
  }
  PIN(cpf);
  CODE_END("k1");
}

__global__ void __launch_bounds__(256) kr(char* ws) {
  const int i = blockIdx.x * 256 + threadIdx.x;
  if (i < 4352) {
    const f32x2* p = (const f32x2*)(ws + WS_KVP) + i;
    f32x2 s = {0.f, 0.f};
#pragma unroll
    for (int r = 0; r < 16; ++r) s += p[r * 4352];
    ((unsigned*)(ws + WS_KVZ))[i] = pk_bf16(s.x, s.y);
  }
}

constexpr int L_PQ = 0, L_WO = 65536, L_KVZ = 131072, L_BT = L_KVZ + 17408, K2_LDS = L_BT + 8192;

__global__ void __launch_bounds__(512, 2) k2(const u16* __restrict__ wo, const char* __restrict__ ws, u16* __restrict__ out
                                             , u64* stamps
) {
  STAMP_DECL;
  extern __shared__ __attribute__((aligned(16))) char lds_[];
  const lptr lds = (lptr)lds_;
  const int b = blockIdx.x, xcd = b & 7, idx = b >> 3;
  const int rl = idx & 7, cl = idx >> 3, rt = (xcd >> 1) * 8 + rl, ct = (xcd & 1) * 4 + cl, rot = (rl + 2 * cl) & 7;
  const int tid = threadIdx.x, lane = tid & 63;
  const int wv = __builtin_amdgcn_readfirstlane(tid >> 6), wr = wv & 1, wc = (wv >> 1) & 1, par = wv >> 2;
  const unsigned r8 = tid >> 3, sp = ((tid & 7) ^ (((r8 >> 1) & 1) << 2)) << 4;
  const lptr lw = lds + wv * 1024;
  STAMP(0);
  unsigned cpf;
  CODE_PREFETCH("k2", cpf);
  const int h = lane >> 5, t = lane & 31;
  float gate;
  const int trow = rt * 64 + sig(wr, t);
  asm volatile("global_load_dword %0, %1, %2" : "=v"(gate) : "v"(trow * 4), "s"(ws + WS_G));
  const u32x4 wsr = mk_rsrc(ws, WS_USED), wor = mk_rsrc(wo, 512 * 1024);
  dma16(wsr, tid * 16, WS_KVZ, lw + L_KVZ);
  dma16(wsr, tid * 16, WS_KVZ + 8192, lw + L_KVZ + 8192);
  if (wv == 0) dma16(wsr, tid * 16, WS_KVZ + 16384, lw + L_KVZ + 16384);
  const unsigned pqo = WS_PQF + rt * 65536 + (tid >> 8) * 32768 + (tid & 255) * 16, woo = ct * 128 + r8 * 1024 + sp;
  const lptr lpq = lds + L_PQ + (wv >> 2) * 32768 + (wv & 3) * 1024;
  auto issue = [&](int gidx) {
    const int gr = (gidx + rot) & 7;
    dma16(wsr, pqo, gr * 4096, lpq + gr * 4096);
    dma16(wor, woo, gr * 65536, lw + L_WO + gr * 8192);
  };
  issue(0); issue(1);
  bar();
  issue(2); issue(3); issue(4);
  const int gi = lane >> 4, blk = gi & 1, q = (lane & 15) >> 2, p4 = lane & 3;
  const lptr kvb = lds + L_KVZ + (t < 16 ? t : 16) * 32 + h * 16 + par * 1088;
  const lptr pqb = lds + L_PQ + wr * 32768 + lane * 16 + par * 2048;
  const lptr wob = lds + L_WO + (4 * h + q) * 128 + (((4 * wc + 2 * blk + (p4 >> 1)) ^ ((q >> 1) << 2)) << 4) + (p4 & 1) * 8 + par * 4096;
  f32x16 acc = {};
  f32x16 nd[2];
  bf16x8 wf[2][2], kf[2], pf[2];
#pragma unroll
  for (int gidx = 0; gidx <= 8; ++gidx) {
    lptr wp;
    if (gidx < 8) {
      const int hd0 = ((gidx + rot) & 7) * 4;
      const lptr kp = kvb + hd0 * 544, pp = pqb + hd0 * 1024;
      wp = wob + hd0 * 2048;
      if (gidx <= 3) WAIT_VM(8); else if (gidx == 4) WAIT_VM(7); else if (gidx == 5) WAIT_VM(5); else if (gidx == 6) WAIT_VM(3); else WAIT_VM(1);
      bar();
      if (gidx == 0) STAMP(1);
      SB;
#pragma unroll
      for (int i = 0; i < 2; ++i) { kf[i] = lds_b128(kp + i * 544); pf[i] = lds_b128(pp + i * 1024); }
      SB;
      if (gidx + 5 < 8) issue(gidx + 5);
      if (gidx == 3) dma16(wsr, r8 * 4096 + sp, WS_BT + ct * 64 * 4096 + rt * 128, lw + L_BT);
      SB;
    }
#pragma unroll
    for (int i = 0; i < 2; ++i) {
      if (gidx > 0) {
        PIN(nd[i]);
        const float r = __builtin_amdgcn_rcpf(nd[i][8] + 1e-6f);
#pragma unroll
        for (int e = 0; e < 8; ++e) nd[i][e] *= r;
        acc = mfma32(wf[(gidx - 1) & 1][i], pack8(nd[i], 0), acc);
      }
      if (gidx < 8) {
        const f32x16 zero = {};
        nd[i] = mfma32(kf[i], pf[i], zero);
        wf[gidx & 1][i] = lds_tr2(wp + i * 2048, wp + i * 2048 + 1024);
      }
      SB;
    }
  }
  STAMP(2);
  lgkm0();
  WAIT_VM(0);
  bar();
  {
    LAS f32x4* xw = (LAS f32x4*)(lds + L_PQ + wv * 2048) + lane;
    const LAS f32x4* xr = (const LAS f32x4*)(lds + L_PQ + (wv ^ 4) * 2048) + lane;
    f32x4 keep[2];
#pragma unroll
    for (int G = 0; G < 2; ++G) {
      const f32x4 lo = {acc[4 * G], acc[4 * G + 1], acc[4 * G + 2], acc[4 * G + 3]};
      const f32x4 hi = {acc[8 + 4 * G], acc[9 + 4 * G], acc[10 + 4 * G], acc[11 + 4 * G]};
      if (par) { xw[64 * G] = lo; keep[G] = hi; } else { xw[64 * G] = hi; keep[G] = lo; }
    }
    const lptr bb = lds + L_BT + (wc * 32 + par * 16 + 4 * h + q) * 128 + (((4 * wr + 2 * blk + (p4 >> 1)) ^ ((q >> 1) << 2)) << 4) + (p4 & 1) * 8;
    const u32x2 bv0 = __builtin_bit_cast(u32x2, lds_tr(bb)), bv1 = __builtin_bit_cast(u32x2, lds_tr(bb + 1024));
    lgkm0();
    bar();
    PIN(gate);
    u32x2 o[2];
#pragma unroll
    for (int G = 0; G < 2; ++G) {
      const f32x4 a = keep[G] + xr[64 * G];
      const unsigned b0 = G ? bv1.x : bv0.x, b1 = G ? bv1.y : bv0.y;
      const float bf[4] = {bf2f(b0), bfhi(b0), bf2f(b1), bfhi(b1)};
      float r[4];
#pragma unroll
      for (int e = 0; e < 4; ++e) r[e] = bf[e] + gate * (a[e] - bf[e]);
      o[G] = u32x2{pk_bf16(r[0], r[1]), pk_bf16(r[2], r[3])};
    }
    char* dst = (char*)out + (unsigned)(trow * 1024 + ct * 128 + wc * 64 + par * 32 + h * 16);
    auto sx = __builtin_amdgcn_permlane32_swap(o[0].x, o[1].x, false, false);
    auto sy = __builtin_amdgcn_permlane32_swap(o[0].y, o[1].y, false, false);
    const u32x4 v = {sx[0], sy[0], sx[1], sy[1]};
    *(u32x4*)dst = v;
  }
  STAMP(3);
  STAMP_FLUSH;
  if (blockIdx.x == 0 && wv == 0) {
    const u64* s1 = stamps - 32768;
    u64 mn = ~0ull; u64 sum = 0; unsigned cnt = 0; u64 mx = 0;
    for (int i = lane; i < 2048; i += 64) {
      const u64 a = s1[i * 16]; mn = a < mn ? a : mn;
      const int role = ((i >> 3) >> 3) >= 16;
      const u64 v = s1[i * 16 + PROBE_S];
      if (role == PROBE_ROLE && v) { sum += v; cnt++; mx = v > mx ? v : mx; }
    }
    for (int o = 32; o; o >>= 1) { u64 t = __shfl_xor(mn, o); mn = t < mn ? t : mn; sum += __shfl_xor(sum, o); cnt += __shfl_xor(cnt, o); t = __shfl_xor(mx, o); mx = t > mx ? t : mx; }
    if (PROBE_K == 1) mn = s1[65536];
    { const u64 now = __builtin_amdgcn_s_memrealtime(); if (now - mn > 20000) mn = now; }
    while (__builtin_amdgcn_s_memrealtime() - mn < 6000) __builtin_amdgcn_s_sleep(1);
  }
  PIN(cpf);
  CODE_END("k2");
}

#define STARG(o) , (u64*)(ws + (8u << 20)) + (o)

__global__ void kstamp(u64* p) { *p = __builtin_amdgcn_s_memrealtime(); }
extern "C" void kernel_launch(void* const* d_in, const int* in_sizes, int n_in, void* d_out, int out_size, void* d_ws,
                              size_t ws_size, hipStream_t stream) {
  const u16* x = (const u16*)d_in[0];
  const u16* wqkv = (const u16*)d_in[1];
  const u16* wo = (const u16*)d_in[2];
  const u16* lar = (const u16*)d_in[4];
  const u16* aim = (const u16*)d_in[5];
  const u16* cre = (const u16*)d_in[6];
  const u16* cim = (const u16*)d_in[7];
  const u16* ldt = (const u16*)d_in[8];
  const u16* dsk = (const u16*)d_in[9];
  const u16* gw = (const u16*)d_in[10];
  char* ws = (char*)d_ws;
  (void)hipFuncSetAttribute((const void*)k1, hipFuncAttributeMaxDynamicSharedMemorySize, 163840);
  (void)hipFuncSetAttribute((const void*)k2, hipFuncAttributeMaxDynamicSharedMemorySize, K2_LDS);
  kstamp<<<1, 64, 0, stream>>>((u64*)(ws + (8u << 20)) + 65536);
  k1<<<256, 512, 163840, stream>>>(x, wqkv, gw, lar, aim, cre, cim, ldt, dsk, ws STARG(0));
  kr<<<17, 256, 0, stream>>>(ws);
  k2<<<256, 512, K2_LDS, stream>>>(wo, ws, (u16*)d_out STARG(32768));
}
```

```cpp
#define STAMPS
#define PROBE_K 1
#define PROBE_ROLE 0
#define PROBE_S 8
#include <hip/hip_runtime.h>
#include <stdint.h>

typedef unsigned short u16;
typedef unsigned long long u64;
typedef __attribute__((ext_vector_type(8))) __bf16 bf16x8;
typedef __attribute__((ext_vector_type(2))) __bf16 bf16x2;
typedef __attribute__((ext_vector_type(8))) _Float16 f16x8;
typedef __attribute__((ext_vector_type(2))) _Float16 f16x2;
typedef __attribute__((ext_vector_type(4))) short s16x4;
typedef __attribute__((ext_vector_type(16))) float f32x16;
typedef __attribute__((ext_vector_type(4))) float f32x4;
typedef __attribute__((ext_vector_type(2))) float f32x2;
typedef __attribute__((ext_vector_type(4))) unsigned u32x4;
typedef __attribute__((ext_vector_type(2))) unsigned u32x2;
#define LAS __attribute__((address_space(3)))
#define CAS __attribute__((address_space(4)))
#define DI __device__ __forceinline__

constexpr unsigned WS_PQF = 0;
constexpr unsigned WS_BT  = 2u << 20;
constexpr unsigned WS_KVP = 4u << 20;
constexpr unsigned WS_KVZ = 5u << 20;
constexpr unsigned WS_G   = (5u << 20) + 65536;
constexpr unsigned WS_USED = 6u << 20;

constexpr float LOG2E = 1.4426950408889634f;

#define STAMP_INS "s_memrealtime"
#define STAMP_DECL u64 st_[12] = {}
#define STAMP(i) do { __builtin_amdgcn_sched_barrier(0); asm volatile(STAMP_INS " %0\n\ts_waitcnt lgkmcnt(0)" : "=s"(st_[i]) :: "memory"); __builtin_amdgcn_sched_barrier(0); } while (0)
#define STAMP_FLUSH do { if ((threadIdx.x & 63) == 0) { _Pragma("unroll") for (int i_ = 0; i_ < 12; ++i_) stamps[(blockIdx.x * 8 + (threadIdx.x >> 6)) * 16 + i_] = st_[i_]; } } while (0)
#define SB __builtin_amdgcn_sched_barrier(0)
#define PIN(x) asm volatile("" : "+v"(x))
#define PINS(x) asm volatile("" : "+s"(x))

typedef LAS char* lptr;
DI float bf2f(unsigned v) { return __uint_as_float(v << 16); }
DI float bfhi(unsigned v) { return __uint_as_float(v & 0xffff0000u); }
DI unsigned pk_bf16(float a, float b) { f32x2 v = {a, b}; return __builtin_bit_cast(unsigned, __builtin_convertvector(v, bf16x2)); }
DI float rbf(float a) { return __uint_as_float(pk_bf16(a, a) << 16); }
DI f16x2 pk_f16(f32x2 v) { return __builtin_convertvector(v, f16x2); }
DI f32x2 cmul(f32x2 a, f32x2 b) { f32x2 t = a.xx * b; f32x2 bs = {-b.y, b.x}; return __builtin_elementwise_fma(a.yy, bs, t); }
DI void cmul2(f32x2& r0, f32x2 a0, f32x2 b0, f32x2& r1, f32x2 a1, f32x2 b1) {
  f32x2 t0, t1;
  asm("v_pk_mul_f32 %0, %2, %3 op_sel_hi:[0,1]\n\tv_pk_mul_f32 %1, %4, %5 op_sel_hi:[0,1]\n\t"
      "v_pk_fma_f32 %0, %2, %3, %0 op_sel:[1,1,0] op_sel_hi:[1,0,1] neg_lo:[0,1,0]\n\t"
      "v_pk_fma_f32 %1, %4, %5, %1 op_sel:[1,1,0] op_sel_hi:[1,0,1] neg_lo:[0,1,0]"
      : "=&v"(t0), "=&v"(t1) : "v"(a0), "v"(b0), "v"(a1), "v"(b1));
  r0 = t0; r1 = t1;
}
DI f16x2 pk_f16c(f32x2 v) { const f32x2 c = {v.x, -v.y}; return __builtin_convertvector(c, f16x2); }
DI f16x2 cmulh(f16x2 a, f16x2 b) { f16x2 t = a.xx * b; f16x2 bs = {-b.y, b.x}; return __builtin_elementwise_fma(a.yy, bs, t); }
DI u32x4 mk_rsrc(const void* p, unsigned bytes) { const u64 a = (u64)p; return u32x4{(unsigned)a, (unsigned)(a >> 32), bytes, 0x00020000u}; }
DI void dma16(u32x4 rs, unsigned voff, unsigned soff, lptr l) {
  asm volatile("s_mov_b32 m0, %3\n\ts_nop 0\n\tbuffer_load_dwordx4 %0, %1, %2 offen lds"
               :: "v"(voff), "s"(rs), "s"(soff), "s"((unsigned)(size_t)l) : "memory");
}
DI int sig(int w, int n) { return 32 * ((n >> 3) & 1) + ((n >> 2) & 1) + 16 * w + 8 * (n >> 4) + 2 * (n & 3); }
DI bf16x8 lds_b128(lptr p) { return *(const LAS bf16x8*)p; }
DI s16x4 lds_tr(lptr p) { return __builtin_amdgcn_ds_read_tr16_b64_v4i16((LAS s16x4*)p); }
DI bf16x8 lds_tr2(lptr p0, lptr p1) { s16x4 a = lds_tr(p0), b = lds_tr(p1); return __builtin_bit_cast(bf16x8, __builtin_shufflevector(a, b, 0, 1, 2, 3, 4, 5, 6, 7)); }
DI f32x16 mfma32(bf16x8 a, bf16x8 b, f32x16 c) { return __builtin_amdgcn_mfma_f32_32x32x16_bf16(a, b, c, 0, 0, 0); }
DI f32x4 mfma16(bf16x8 a, bf16x8 b, f32x4 c) { return __builtin_amdgcn_mfma_f32_16x16x32_bf16(a, b, c, 0, 0, 0); }
DI f32x4 mfma16h(f16x8 a, f16x8 b, f32x4 c) { return __builtin_amdgcn_mfma_f32_16x16x32_f16(a, b, c, 0, 0, 0); }
DI void bar() { asm volatile("" ::: "memory"); __builtin_amdgcn_s_barrier(); asm volatile("" ::: "memory"); }
DI void lgkm0() { asm volatile("s_waitcnt lgkmcnt(0)" ::: "memory"); }
#define WAIT_VM(n) asm volatile("s_waitcnt vmcnt(" #n ")" ::: "memory")
DI float phi(float tp) {
  const float e = __builtin_amdgcn_fmed3f(__builtin_amdgcn_exp2f(tp * LOG2E), 0.f, 1.f);
  const int ti = __float_as_int(tp);
  return __int_as_float(ti > 0x358637bd ? ti : 0x358637bd) + e;
}
DI bf16x8 pack8(const f32x16& a, int o) {
  u32x4 p = {pk_bf16(a[o], a[o + 1]), pk_bf16(a[o + 2], a[o + 3]), pk_bf16(a[o + 4], a[o + 5]), pk_bf16(a[o + 6], a[o + 7])};
  return __builtin_bit_cast(bf16x8, p);
}
template <int CTRL, int RM = 0xf> DI int dppi(int v) { return __builtin_amdgcn_update_dpp(0, v, CTRL, RM, 0xf, true); }
template <int CTRL, int RM = 0xf> DI float dppf(float v) { return __int_as_float(dppi<CTRL, RM>(__float_as_int(v))); }
DI unsigned umax(unsigned a, unsigned b) { return a > b ? a : b; }
DI unsigned wave_umax(unsigned v) {
  v = umax(v, dppi<0x111>(v)); v = umax(v, dppi<0x112>(v)); v = umax(v, dppi<0x114>(v)); v = umax(v, dppi<0x118>(v));
  v = umax(v, dppi<0x142, 0xa>(v)); v = umax(v, dppi<0x143, 0xc>(v));
  return __builtin_amdgcn_readlane(v, 63);
}
DI float wave_sum(float v) {
  v += dppf<0x111>(v); v += dppf<0x112>(v); v += dppf<0x114>(v); v += dppf<0x118>(v);
  v += dppf<0x142, 0xa>(v); v += dppf<0x143, 0xc>(v);
  return __int_as_float(__builtin_amdgcn_readlane(__float_as_int(v), 63));
}
#define CODE_PREFETCH(NAME, dummy) do { u64 pc_; unsigned len_; \
  asm volatile("s_getpc_b64 %0\n.L" NAME "_beg:\n\ts_mov_b32 %1, .L" NAME "_end-.L" NAME "_beg" : "=s"(pc_), "=s"(len_)); \
  unsigned o_ = threadIdx.x * 64; o_ = o_ < len_ ? o_ : 0; \
  asm volatile("global_load_dword %0, %1, %2" : "=v"(dummy) : "v"(o_), "s"(pc_)); } while (0)
#define CODE_END(NAME) asm volatile(".L" NAME "_end:")
typedef __amdgpu_buffer_rsrc_t rsrc_t;
DI rsrc_t make_rsrc(void* p, unsigned bytes) { return __builtin_amdgcn_make_buffer_rsrc(p, 0, bytes, 0x00020000); }
constexpr int WT = 16;
DI void st128(rsrc_t r, unsigned off, u32x4 v) { __builtin_amdgcn_raw_buffer_store_b128(v, r, off, 0, WT); }
DI void st64(rsrc_t r, unsigned off, u32x2 v) { __builtin_amdgcn_raw_buffer_store_b64(v, r, off, 0, WT); }
DI void st32(rsrc_t r, unsigned off, unsigned v) { __builtin_amdgcn_raw_buffer_store_b32(v, r, off, 0, WT); }

constexpr int SLOT = 32768, NSLOT = 4;
constexpr int GA_EX = NSLOT * SLOT;
constexpr int GA_GW = GA_EX + 8 * 544 * 4;

DI void gemm_role(lptr lds, const u16* __restrict__ x, const u16* __restrict__ w, const u16* __restrict__ gw, char* ws,
                  int rb, int cg, int rot, u64* stamps) {
  STAMP_DECL;
  const int tid = threadIdx.x, lane = tid & 63;
  const int wv = __builtin_amdgcn_readfirstlane(tid >> 6), rg = wv & 3, hp = wv >> 2;
  const u32x4 xr = mk_rsrc(x, 2048 * 1024), wr_ = mk_rsrc(w, 512 * 3072);
  const unsigned xrow = tid >> 3, xo = rb * 131072 + xrow * 1024 + (((tid & 7) ^ ((xrow >> 1) & 7)) << 4);
  const unsigned wrow = tid >> 4, wp = (tid & 15) ^ ((wrow & 3) << 2);
  const unsigned wo_ = cg * 128 + wrow * 3072 + 1024 + (wp >> 3) * 1024 + ((wp & 7) << 4);
  const unsigned qo = cg * 128 + xrow * 3072 + (((tid & 7) ^ (((xrow >> 1) & 1) << 2)) << 4);
  const lptr lw = lds + wv * 1024;
  auto issue = [&](int c, int i) {
    const lptr slot = lw + (c % NSLOT) * SLOT;
    if (c < 8) {
      const unsigned kc = (c + rot) & 7;
      if (i == 0) dma16(xr, xo, kc * 128, slot);
      if (i == 1) dma16(wr_, wo_, kc * (64 * 3072), slot + 16384);
      if (i == 2) dma16(xr, xo, kc * 128 + 65536, slot + 8192);
      if (i == 3) dma16(wr_, wo_, kc * (64 * 3072) + 32 * 3072, slot + 24576);
    } else {
      dma16(wr_, qo, (((c - 8) * 4 + i + rot) & 7) * (64 * 3072), slot + i * 8192);
    }
  };
  STAMP(0);
  const bool do_g = (wv == cg) && (cg < 4);
  if (do_g) dma16(mk_rsrc(gw, 1024), lane * 16, 0, lds + GA_GW);
#pragma unroll
  for (int c = 0; c < 3; ++c) {
#pragma unroll
    for (int i = 0; i < 4; ++i) issue(c, i);
    if (c < 2) bar();
  }
  const int h = lane >> 5, row = (rg >> 1) * 64 + sig(rg & 1, lane & 31);
  const int gi = lane >> 4, blk = gi & 1, q = (lane & 15) >> 2, p4 = lane & 3;
  lptr ap[4], bp[2];
#pragma unroll
  for (int s = 0; s < 4; ++s) ap[s] = lds + row * 128 + (((2 * s + h) ^ ((row >> 1) & 7)) << 4);
#pragma unroll
  for (int ct = 0; ct < 2; ++ct) bp[ct] = lds + 16384 + (8 * h + q) * 256 + (((4 * (2 * ct + hp) + 2 * blk + (p4 >> 1)) ^ (q << 2)) << 4) + (p4 & 1) * 8;
  f32x16 ak, av, ag = {};
#pragma unroll
  for (int i = 0; i < 16; ++i) { ak[i] = 1e-6f; av[i] = 0.f; }
  bf16x8 xf[32], Bf[2][2];
  STAMP(1);
#pragma unroll
  for (int n = 0; n <= 32; ++n) {
    const int c = n >> 2, s = n & 3, nb = n & 1, so = (c & 1) * SLOT;
    if (s == 0 && n < 32) {
      if (c > 0) {
        lgkm0();
        if ((c & 1) == 0) {
          const int dlt = (c == 4) ? -2 * SLOT : 2 * SLOT;
#pragma unroll
          for (int i = 0; i < 4; ++i) { ap[i] += dlt; PIN(ap[i]); }
#pragma unroll
          for (int i = 0; i < 2; ++i) { bp[i] += dlt; PIN(bp[i]); }
        }
      }
      WAIT_VM(8);
      bar();
      if (c == 0) STAMP(2);
      SB;
    }
    if (n > 0) { ak = mfma32(xf[n - 1], Bf[nb ^ 1][0], ak); SB; }
    if (n < 32) { xf[n] = lds_b128(ap[s] + so); Bf[nb][0] = lds_tr2(bp[0] + so + s * 4096, bp[0] + so + s * 4096 + 1024); SB; }
    if (n > 0) { av = mfma32(xf[n - 1], Bf[nb ^ 1][1], av); SB; }
    if (n < 32) { Bf[nb][1] = lds_tr2(bp[1] + so + s * 4096, bp[1] + so + s * 4096 + 1024); SB; }
    if (c + 3 < 10) { issue(c + 3, s); SB; }
    if (n > 0 && s == 0 && do_g) {
      const lptr gp = lds + GA_GW + ((c - 1 + rot) & 7) * 128 + h * 16;
#pragma unroll
      for (int i = 0; i < 4; ++i) ag = mfma32(lds_b128(gp + i * 32), xf[n - 4 + i], ag);
      SB;
    }
  }
  STAMP(3);
  const rsrc_t wsr = make_rsrc(ws, WS_USED);
  {
    bf16x8 ones;
#pragma unroll
    for (int i = 0; i < 8; ++i) ones[i] = (__bf16)1.0f;
    const int dp = lane & 31, d = lane & 15;
    const int pos = ((d >> 2) & 1) * 8 + (d & 3) + 4 * (d >> 3);
    const bool lo = dp < 16;
#pragma unroll
    for (int i = 0; i < 16; ++i) ak[i] = phi(ak[i]);
    f32x16 kv = {}, z = {};
#pragma unroll
    for (int s = 0; s < 2; ++s) {
      const bf16x8 pk = pack8(ak, 8 * s), vv = pack8(av, 8 * s);
      kv = mfma32(vv, pk, kv);
      z = mfma32(ones, pk, z);
    }
    LAS float* e0 = (LAS float*)(lds + GA_EX) + (rg * 4 + 2 * hp + (dp >> 4)) * 272 + pos + 64 * h;
#pragma unroll
    for (int i = 0; i < 8; ++i) {
      float a = kv[i], b = kv[8 + i];
      PIN(a); PIN(b);
      e0[((i & 3) + 8 * (i >> 2)) * 16] = lo ? a : b;
    }
    if (h == 0) e0[256] = z[0];
  }
  STAMP(4);
  f32x16 aq;
#pragma unroll
  for (int i = 0; i < 16; ++i) aq[i] = 1e-6f;
  const lptr qp = lds + (8 * h + q) * 128 + (((4 * hp + 2 * blk + (p4 >> 1)) ^ ((q >> 1) << 2)) << 4) + (p4 & 1) * 8;
  bf16x8 Aq[4];
  constexpr int PD = 3;
#pragma unroll
  for (int n = 0; n < 32 + PD; ++n) {
    if (n == 0) { lgkm0(); WAIT_VM(4); bar(); SB; }
    if (n == 16) { lgkm0(); WAIT_VM(1); bar(); SB; }
    if (n >= PD) { aq = mfma32(Aq[(n - PD) & 3], xf[n - PD], aq); SB; }
    if (n < 32) { Aq[n & 3] = lds_tr2(qp + n * 2048, qp + n * 2048 + 512); SB; }
    if (n == PD) {
      const LAS f32x4* ex = (const LAS f32x4*)(lds + GA_EX) + (tid < 272 ? tid : 0);
      const f32x4 sum = (ex[0] + ex[272]) + (ex[544] + ex[816]);
      st128(wsr, tid < 272 ? WS_KVP + ((rb * 32 + cg * 4) * 68 + tid) * 16 : 0xfffffff0u, __builtin_bit_cast(u32x4, sum));
      SB;
    }
  }
  STAMP(5);
  {
    const unsigned o = WS_PQF + (((rb * 4 + rg) * 32 + cg * 4 + 2 * hp) * 64 + lane) * 16;
#pragma unroll
    for (int i = 0; i < 16; ++i) aq[i] = phi(aq[i]);
    st128(wsr, o, __builtin_bit_cast(u32x4, pack8(aq, 0)));
    st128(wsr, o + 1024, __builtin_bit_cast(u32x4, pack8(aq, 8)));
    if (do_g && h == 0) {
      const float g = __builtin_amdgcn_rcpf(1.f + __builtin_amdgcn_exp2f(-ag[0] * LOG2E));
      st32(wsr, WS_G + (rb * 128 + row) * 4, __float_as_uint(g));
    }
  }
  STAMP(6);
  STAMP_FLUSH;
}

constexpr int SC_ROW = 2064;
constexpr int KC_CP = 4224, KC_CH = 2 * KC_CP;
constexpr int EX_BASE = 4 * KC_CH;
constexpr int U_BASE = 57 * SC_ROW, U_PAD = 224, U_CP = 4576, U_CH = 2 * U_CP;
constexpr int T_BASE = U_BASE + 4 * U_CH, T_CH = 1536;
static_assert(T_BASE + 4 * T_CH <= 163840 && U_BASE % 16 == 0, "lds");

DI f32x2 cexp_p(float l2, float rv) {
  const float m = __builtin_amdgcn_exp2f(l2), f = __builtin_amdgcn_fractf(rv);
  const f32x2 r = {m * __builtin_amdgcn_cosf(f), m * __builtin_amdgcn_sinf(f)};
  return r;
}
DI bf16x8 lds_2b64(lptr p) {
  const u32x2 a = *(const volatile LAS u32x2*)p, b = *(const volatile LAS u32x2*)(p + 8);
  const u32x4 v = {a.x, a.y, b.x, b.y};
  return __builtin_bit_cast(bf16x8, v);
}
DI unsigned perm(unsigned hi, unsigned lo, unsigned sel) { return __builtin_amdgcn_perm(hi, lo, sel); }
constexpr unsigned LL = 0x01000100, LH = 0x03020302, HL = 0x05040504, SWL = 0x01000302, SWH = 0x05040706, HL_LH = 0x03020504;
template <int E0, int E1, int E2, int E3>
DI void put4(unsigned base, f16x2 a, f16x2 b, f16x2 c, f16x2 d) {
  constexpr int O = E0 < 28 ? 0 : 28;
  static_assert((E1 < 28) == (E0 < 28) && (E2 < 28) == (E0 < 28) && (E3 < 28) == (E0 < 28), "one base");
  asm volatile("s_mov_b32 m0, %4\n\ts_nop 0\n\tds_write_addtid_b32 %0 offset:%5\n\tds_write_addtid_b32 %1 offset:%6\n\t"
               "ds_write_addtid_b32 %2 offset:%7\n\tds_write_addtid_b32 %3 offset:%8"
               :: "v"(a), "v"(b), "v"(c), "v"(d), "s"(base + O * SC_ROW), "i"((E0 - O) * SC_ROW), "i"((E1 - O) * SC_ROW),
                  "i"((E2 - O) * SC_ROW), "i"((E3 - O) * SC_ROW) : "memory");
}

DI void conv_role(lptr lds, const u16* __restrict__ x, const u16* __restrict__ lar, const u16* __restrict__ aim,
                  const u16* __restrict__ cre, const u16* __restrict__ cim, const u16* __restrict__ ldt,
                  const u16* __restrict__ dsk, char* ws, int ch0, int jrot, u64* stamps) {
  STAMP_DECL;
  const int tid = threadIdx.x, lane = tid & 63;
  const int wv = __builtin_amdgcn_readfirstlane(tid >> 6), cl = wv & 3, half = wv >> 2;
  const int d = ch0 + cl;
  STAMP(0);
  const unsigned r_lar = ((const unsigned*)lar)[d * 64 + lane], r_aim = ((const unsigned*)aim)[d * 64 + lane];
  const unsigned r_cr = ((const unsigned*)cre)[d * 64 + lane], r_ci = ((const unsigned*)cim)[d * 64 + lane];
  const unsigned s_ldt = ((const CAS unsigned*)ldt)[d >> 1], s_dsk = ((const CAS unsigned*)dsk)[d >> 1];
  bar();
  const int tt = (tid + jrot * 64) & 511;
  const char* xg = (const char*)x + ch0 * 2;
  const unsigned xoff = tt * 4096;
  u32x2 ur[4], up = {0u, 0u};
  ur[0] = *(const u32x2*)(xg + xoff); ur[1] = *(const u32x2*)(xg + xoff + 1024);
  if (lane == 0 && tt > 0) up = *(const u32x2*)(xg + xoff - 1024);
  {
    const u32x4 zero = {0, 0, 0, 0};
    if (lane < 28) *(LAS u32x4*)(lds + U_BASE + cl * U_CH + half * U_CP + lane * 16) = zero;
  }
  const int j = lane & 15, g = lane >> 4;
  const unsigned scb = (unsigned)(size_t)lds + cl * 512 + half * 256;
  const lptr fb = lds + cl * 512 + half * 256 + j * SC_ROW + g * 16;
  unsigned selA = lane < 32 ? LL : lane == 32 ? HL : SWH, selB = lane < 32 ? HL_LH : SWH, selC = lane < 32 ? LL : SWH, selE = lane < 32 ? LL : SWL;
  if (half == 0) { const bool odd = !(lane & 1); selA = odd ? LH : LL; selB = odd ? HL : LH; selC = odd ? HL : SWL; }
  PIN(selA); PIN(selB); PIN(selC); PIN(selE);
  const unsigned shl = half ? 0 : 16;
  const float f_ldt = (d & 1) ? bfhi(s_ldt) : bf2f(s_ldt), f_dsk = (d & 1) ? bfhi(s_dsk) : bf2f(s_dsk);
  const float dt = __int_as_float(__builtin_amdgcn_readfirstlane(__float_as_int(rbf(__expf(f_ldt)))));
  SB;
  STAMP(1);
  const float l2 = -rbf(__builtin_amdgcn_exp2f(bfhi(r_lar << shl) * LOG2E)) * dt * LOG2E;
  const float rv = bfhi(r_aim << shl) * dt * 0.15915494309189535f;
  unsigned cm;
  {
    typedef __attribute__((ext_vector_type(2))) unsigned short u16x2;
    const u16x2 m2 = __builtin_elementwise_max(__builtin_bit_cast(u16x2, r_cr & 0x7fff7fffu), __builtin_bit_cast(u16x2, r_ci & 0x7fff7fffu));
    const unsigned m = __builtin_bit_cast(unsigned, m2);
    cm = umax(wave_umax(umax(m << 16, m)), 0x0da24260u);
  }
  const float cmax = __uint_as_float(cm), cinv = __builtin_amdgcn_rcpf(cmax);
  f32x4 kt[3];
  {
    f16x8 rf[4], t0[4];
    const f16x2 one = {(_Float16)1.f, (_Float16)0.f};
    const f32x2 E1 = cexp_p(l2, rv), E8 = cexp_p(l2 * 8.f, rv * 8.f), E128 = cexp_p(l2 * 128.f, rv * 128.f);
    f32x2 E2, E3, E4, E16, E24, E32, E64, E96, E256, E384, E512, H1, H2, H3, H4, dummy;
    const f32x2 H0 = {bfhi(r_cr << shl) * cinv, bfhi(r_ci << shl) * cinv};
    cmul2(E16, E8, E8, E256, E128, E128);
    cmul2(E32, E16, E16, E512, E256, E256);
    cmul2(E24, E16, E8, E2, E1, E1);
    cmul2(E64, E32, E32, H1, H0, E512);
    cmul2(E3, E2, E1, E4, E2, E2);
    cmul2(E96, E64, E32, H2, H1, E512);
    cmul2(E384, E256, E128, H3, H2, E512);
    ur[2] = *(const u32x2*)(xg + xoff + 2048); ur[3] = *(const u32x2*)(xg + xoff + 3072);
    {
      const f16x2 P1 = pk_f16c(E8), P2 = pk_f16c(E16), P3 = pk_f16c(E24), Q1 = pk_f16c(E32), Q2 = pk_f16c(E64), Q3 = pk_f16c(E96);
      put4<0, 1, 2, 3>(scb, one, P1, P2, P3);
      put4<4, 5, 6, 7>(scb, Q1, cmulh(Q1, P1), cmulh(Q1, P2), cmulh(Q1, P3));
      put4<8, 9, 10, 11>(scb, Q2, cmulh(Q2, P1), cmulh(Q2, P2), cmulh(Q2, P3));
      put4<12, 13, 14, 15>(scb, Q3, cmulh(Q3, P1), cmulh(Q3, P2), cmulh(Q3, P3));
    }
    H4 = cmul(H3, E512); (void)dummy;
    const f16x2 e1 = pk_f16(E1), e2 = pk_f16(E2), e3 = pk_f16(E3), e4 = pk_f16(E4);
    const f16x2 e5 = cmulh(e4, e1), e6 = cmulh(e4, e2), e7 = cmulh(e4, e3);
    const f16x2 S1 = pk_f16(E128), S2 = pk_f16(E256), S3 = pk_f16(E384);
    const f16x2 h0 = pk_f16(H0), h1 = pk_f16(H1), h2 = pk_f16(H2), h3 = pk_f16(H3);
    {
      const f16x2 G1 = cmulh(h0, S1);
      put4<16, 17, 18, 19>(scb, h0, cmulh(h0, e1), cmulh(h0, e2), cmulh(h0, e3));
      put4<20, 21, 22, 23>(scb, cmulh(h0, e4), cmulh(h0, e5), cmulh(h0, e6), cmulh(h0, e7));
      put4<24, 25, 26, 27>(scb, G1, cmulh(G1, e1), cmulh(G1, e2), cmulh(G1, e3));
      put4<28, 29, 30, 31>(scb, cmulh(G1, e4), cmulh(G1, e5), cmulh(G1, e6), cmulh(G1, e7));
    }
#pragma unroll
    for (int s = 0; s < 4; ++s) { rf[s] = *(const LAS f16x8*)(fb + s * 64); t0[s] = *(const LAS f16x8*)(fb + 16 * SC_ROW + s * 64); }
    SB;
    {
      const f16x2 G2 = cmulh(h0, S2), G3 = cmulh(h0, S3), G5 = cmulh(h1, S1), G6 = cmulh(h1, S2), G7 = cmulh(h1, S3);
      put4<32, 33, 34, 35>(scb, G2, cmulh(G2, e2), cmulh(G2, e4), cmulh(G2, e6));
      kt[0] = mfma16h(t0[0], rf[0], f32x4{0.f, 0.f, 0.f, 0.f});
      kt[0] = mfma16h(t0[1], rf[1], kt[0]);
      put4<36, 37, 38, 39>(scb, G3, cmulh(G3, e2), cmulh(G3, e4), cmulh(G3, e6));
      kt[0] = mfma16h(t0[2], rf[2], kt[0]);
      put4<40, 41, 42, 43>(scb, h1, cmulh(h1, e4), G5, cmulh(G5, e4));
      kt[0] = mfma16h(t0[3], rf[3], kt[0]);
      put4<44, 45, 46, 47>(scb, G6, cmulh(G6, e4), G7, cmulh(G7, e4));
    }
#pragma unroll
    for (int s = 0; s < 4; ++s) t0[s] = *(const LAS f16x8*)(fb + 32 * SC_ROW + s * 64);
    SB;
    put4<48, 49, 50, 51>(scb, h2, cmulh(h2, S1), cmulh(h2, S2), cmulh(h2, S3));
    put4<52, 53, 54, 55>(scb, h3, cmulh(h3, S1), cmulh(h3, S2), cmulh(h3, S3));
    *(LAS f16x2*)(lds + cl * 512 + half * 256 + lane * 4 + 56 * SC_ROW) = pk_f16(H4);
    f16x8 t2[4];
#pragma unroll
    for (int s = 0; s < 4; ++s) t2[s] = *(const LAS f16x8*)(fb + 48 * SC_ROW + s * 64);
    SB;
    kt[1] = f32x4{0.f, 0.f, 0.f, 0.f}; kt[2] = kt[1];
#pragma unroll
    for (int s = 0; s < 4; ++s) kt[1] = mfma16h(t0[s], rf[s], kt[1]);
#pragma unroll
    for (int s = 0; s < 4; ++s) kt[2] = mfma16h(t2[s], rf[s], kt[2]);
  }
  STAMP(2);
  {
    const u32x2 pv = {(unsigned)__builtin_amdgcn_update_dpp((int)up.x, (int)ur[3].x, 0x138, 0xf, 0xf, false),
                      (unsigned)__builtin_amdgcn_update_dpp((int)up.y, (int)ur[3].y, 0x138, 0xf, 0xf, false)};
    const lptr ub = lds + U_BASE + (U_PAD + 4 * tt) * 2;
#pragma unroll
    for (int c = 0; c < 4; ++c) {
      const unsigned sel = (c & 1) ? 0x07060302 : 0x05040100;
      const unsigned p = pv[c >> 1], r0 = ur[0][c >> 1], r1 = ur[1][c >> 1], r2 = ur[2][c >> 1], r3 = ur[3][c >> 1];
      *(LAS u32x2*)(ub + c * U_CH) = u32x2{perm(r1, r0, sel), perm(r3, r2, sel)};
      *(LAS u32x2*)(ub + c * U_CH + U_CP) = u32x2{perm(r0, p, sel), perm(r2, r1, sel)};
    }
  }
  const lptr Tb = lds + T_BASE + cl * T_CH;
  {
    LAS f32x4* xw = (LAS f32x4*)(lds + cl * 512 + half * 256 + g * SC_ROW) + j;
    const LAS f32x4* xr = (const LAS f32x4*)(lds + cl * 512 + (half ^ 1) * 256 + g * SC_ROW) + j;
    if (half) xw[0] = kt[0]; else { xw[0] = kt[1]; xw[4 * SC_ROW / 16] = kt[2]; }
    const float ks = 2.f * dt * cmax;
    const lptr p1 = half ? Tb + (256 + 64 * g + (g < 2 ? 4 : 2) * j) * 2 : Tb + (128 * (g >> 1) + 4 * (g & 1) + 8 * j) * 2;
    lptr p1b = p1 + (g < 2 ? 4 : 64);
    PIN(p1b);
    lgkm0();
    STAMP(3);
    bar();
    if (half == 0) {
      f32x4 a0 = (kt[0] + xr[0]) * ks;
      if (lane == 0) a0[0] += f_dsk;
      *(LAS u32x2*)p1 = u32x2{pk_bf16(a0[0], a0[1]), pk_bf16(a0[2], a0[3])};
    } else {
      const f32x4 a1 = (kt[1] + xr[0]) * ks, a2 = (kt[2] + xr[4 * SC_ROW / 16]) * ks;
      *(LAS unsigned*)p1 = pk_bf16(a1[0], a1[1]);
      *(LAS unsigned*)p1b = pk_bf16(a1[2], a1[3]);
      const unsigned q0 = pk_bf16(a2[0], a2[1]), q1 = pk_bf16(a2[2], a2[3]);
      LAS u16* p2 = (LAS u16*)Tb + 512 + 64 * g + j;
      p2[0] = (u16)q0; p2[16] = (u16)(q0 >> 16); p2[32] = (u16)q1; p2[48] = (u16)(q1 >> 16);
    }
  }
  lgkm0();
  STAMP(4);
  bar();
  {
    const lptr kc = lds + cl * KC_CH + lane * 16;
    auto emit = [&](int Y0, unsigned P0, unsigned P1, unsigned P2, unsigned P3, unsigned P4) {
      *(LAS u32x4*)(kc + Y0 * 16) = u32x4{P0, P1, P2, P3};
      *(LAS u32x4*)(kc + KC_CP + Y0 * 16) = u32x4{P1, P2, P3, P4};
    };
    if (half == 0) {
      const LAS unsigned* p = (const LAS unsigned*)(Tb + ((575 - lane) >> 1) * 4);
      const unsigned D0 = p[32], D1 = p[33], F0 = p[0], F1 = p[1], Z = *(const LAS unsigned*)Tb;
      SB;
      const unsigned e1 = perm(D1, D0, selB), e0 = perm(D1, D0, selA), f1 = perm(F1, F0, selB), f0 = perm(F1, F0, selA);
      emit(0, e1, e1, perm(D1, D0, selC), e0, e0);
      emit(64, f1, f1, perm(F1, F0, selC), f0, f0);
      if (lane < 4) emit(256, lane == 0 ? perm(0, Z, SWL) : 0u, 0u, 0u, 0u, 0u);
    } else {
      const LAS unsigned* p = (const LAS unsigned*)(Tb + (255 - lane) * 4);
      const LAS unsigned* r = (const LAS unsigned*)(Tb + (lane < 32 ? 760 - 8 * lane : 1008 - 16 * lane));
      const unsigned F0 = p[0], F1 = p[1], D0 = r[0], D1 = r[1], D2 = r[2], D3 = r[3], D4 = r[4];
      SB;
      const unsigned e2 = perm(F1, F0, HL);
      emit(128, e2, e2, perm(F1, F0, LH), perm(F1, F0, SWL), perm(F1, F0, LL));
      emit(192, perm(D4, D2, selA), perm(lane < 32 ? D2 : D3, D1, selB), perm(D2, D1, selC), perm(D1, D0, selB), perm(D1, D0, selE));
    }
  }
  lgkm0();
  STAMP(5);
  bar();
  const int a_ = j;
  const lptr ab = lds + cl * KC_CH + ((a_ & 1) ? KC_CP + 4088 : 4096) - 8 * (a_ >> 1) + 16 * g - 3584 - 192 - half * 256;
  const lptr bb = lds + U_BASE + cl * U_CH + ((a_ & 1) ? 0 : U_CP) + (U_PAD + 32 * (a_ >> 1) + 8 * g) * 2 - 192 - half * 256;
  f32x4 acc[8];
#pragma unroll
  for (int i = 0; i < 8; ++i) acc[i] = f32x4{0.f, 0.f, 0.f, 0.f};
  LAS f32x4* exw = (LAS f32x4*)(lds + EX_BASE + wv * 4096) + lane;
  f32x4 keep[4];
  {
    bf16x8 A[2][8], B[2][8];
#pragma unroll
    for (int r = 0; r < 4; ++r) {
      const int nb = r & 1;
#pragma unroll
      for (int mu = 0; mu < 8; ++mu) {
        B[nb][mu] = lds_b128(bb + 512 * mu + 64 * (3 - r));
        A[nb][mu] = lds_2b64(ab + 512 * (7 - mu) + 64 * (3 - r));
        SB;
        if (r > 0) {
#pragma unroll
          for (int n = 0; n < 8; ++n) if (mu + n < 8) acc[mu + n] = mfma16(A[nb ^ 1][mu], B[nb ^ 1][n], acc[mu + n]);
          SB;
        }
      }
    }
    auto tiles = [&](int t0) {
#pragma unroll
      for (int mu = 0; mu < 8; ++mu)
#pragma unroll
        for (int n = 0; n < 8; ++n) if (mu + n >= t0 && mu + n < t0 + 4) acc[mu + n] = mfma16(A[1][mu], B[1][n], acc[mu + n]);
    };
    if (half) {
      tiles(0); SB; tiles(4);
#pragma unroll
      for (int i = 0; i < 4; ++i) { exw[64 * i] = acc[i]; keep[i] = acc[4 + i]; }
    } else {
      tiles(4); SB; tiles(0);
#pragma unroll
      for (int i = 0; i < 4; ++i) { exw[64 * i] = acc[4 + i]; keep[i] = acc[i]; }
    }
  }
  STAMP(6);
  {
    const LAS f32x4* exr = (const LAS f32x4*)(lds + EX_BASE + (wv ^ 4) * 4096) + lane;
    const rsrc_t wsr = make_rsrc(ws, WS_USED);
    const unsigned bto = WS_BT + d * 4096 + half * 2048 + (a_ >> 2) * 128 + g * 32 + (a_ & 3) * 8;
    lgkm0();
    STAMP(7);
    bar();
#pragma unroll
    for (int i = 0; i < 4; ++i) {
      const f32x4 v = keep[i] + exr[64 * i];
      st64(wsr, bto + 512 * i, u32x2{pk_bf16(v[0], v[1]), pk_bf16(v[2], v[3])});
    }
  }
  STAMP(8);
  STAMP_FLUSH;
}

__global__ void __launch_bounds__(512, 2) k1(const u16* __restrict__ x, const u16* __restrict__ wqkv, const u16* __restrict__ gw,
                                             const u16* __restrict__ lar, const u16* __restrict__ aim, const u16* __restrict__ cre,
                                             const u16* __restrict__ cim, const u16* __restrict__ ldt, const u16* __restrict__ dsk,
                                             char* ws
                                             , u64* stamps
) {
  extern __shared__ __attribute__((aligned(16))) char lds_[];
  const lptr lds = (lptr)lds_;
  const int b = blockIdx.x, xcd = b & 7, idx = b >> 3;
  unsigned cpf;
  CODE_PREFETCH("k1", cpf);
  if (idx < 16) {
    conv_role(lds, x, lar, aim, cre, cim, ldt, dsk, ws, xcd * 64 + idx * 4, idx, stamps);
  } else {
    const int rl = idx & 3, cl = (idx >> 2) & 3;
    gemm_role(lds, x, wqkv, gw, ws, (xcd >> 1) * 4 + rl, (xcd & 1) * 4 + cl, 2 * ((rl + cl) & 3), stamps);
  }
  PIN(cpf);
  CODE_END("k1");
}

__global__ void __launch_bounds__(256) kr(char* ws) {
  const int i = blockIdx.x * 256 + threadIdx.x;
  if (i < 4352) {
    const f32x2* p = (const f32x2*)(ws + WS_KVP) + i;
    f32x2 s = {0.f, 0.f};
#pragma unroll
    for (int r = 0; r < 16; ++r) s += p[r * 4352];
    ((unsigned*)(ws + WS_KVZ))[i] = pk_bf16(s.x, s.y);
  }
}

constexpr int L_PQ = 0, L_WO = 65536, L_KVZ = 131072, L_BT = L_KVZ + 17408, K2_LDS = L_BT + 8192;

__global__ void __launch_bounds__(512, 2) k2(const u16* __restrict__ wo, const char* __restrict__ ws, u16* __restrict__ out
                                             , u64* stamps
) {
  STAMP_DECL;
  extern __shared__ __attribute__((aligned(16))) char lds_[];
  const lptr lds = (lptr)lds_;
  const int b = blockIdx.x, xcd = b & 7, idx = b >> 3;
  const int rl = idx & 7, cl = idx >> 3, rt = (xcd >> 1) * 8 + rl, ct = (xcd & 1) * 4 + cl, rot = (rl + 2 * cl) & 7;
  const int tid = threadIdx.x, lane = tid & 63;
  const int wv = __builtin_amdgcn_readfirstlane(tid >> 6), wr = wv & 1, wc = (wv >> 1) & 1, par = wv >> 2;
  const unsigned r8 = tid >> 3, sp = ((tid & 7) ^ (((r8 >> 1) & 1) << 2)) << 4;
  const lptr lw = lds + wv * 1024;
  STAMP(0);
  unsigned cpf;
  CODE_PREFETCH("k2", cpf);
  const int h = lane >> 5, t = lane & 31;
  float gate;
  const int trow = rt * 64 + sig(wr, t);
  asm volatile("global_load_dword %0, %1, %2" : "=v"(gate) : "v"(trow * 4), "s"(ws + WS_G));
  const u32x4 wsr = mk_rsrc(ws, WS_USED), wor = mk_rsrc(wo, 512 * 1024);
  dma16(wsr, tid * 16, WS_KVZ, lw + L_KVZ);
  dma16(wsr, tid * 16, WS_KVZ + 8192, lw + L_KVZ + 8192);
  if (wv == 0) dma16(wsr, tid * 16, WS_KVZ + 16384, lw + L_KVZ + 16384);
  const unsigned pqo = WS_PQF + rt * 65536 + (tid >> 8) * 32768 + (tid & 255) * 16, woo = ct * 128 + r8 * 1024 + sp;
  const lptr lpq = lds + L_PQ + (wv >> 2) * 32768 + (wv & 3) * 1024;
  auto issue = [&](int gidx) {
    const int gr = (gidx + rot) & 7;
    dma16(wsr, pqo, gr * 4096, lpq + gr * 4096);
    dma16(wor, woo, gr * 65536, lw + L_WO + gr * 8192);
  };
  issue(0); issue(1);
  bar();
  issue(2); issue(3); issue(4);
  const int gi = lane >> 4, blk = gi & 1, q = (lane & 15) >> 2, p4 = lane & 3;
  const lptr kvb = lds + L_KVZ + (t < 16 ? t : 16) * 32 + h * 16 + par * 1088;
  const lptr pqb = lds + L_PQ + wr * 32768 + lane * 16 + par * 2048;
  const lptr wob = lds + L_WO + (4 * h + q) * 128 + (((4 * wc + 2 * blk + (p4 >> 1)) ^ ((q >> 1) << 2)) << 4) + (p4 & 1) * 8 + par * 4096;
  f32x16 acc = {};
  f32x16 nd[2];
  bf16x8 wf[2][2], kf[2], pf[2];
#pragma unroll
  for (int gidx = 0; gidx <= 8; ++gidx) {
    lptr wp;
    if (gidx < 8) {
      const int hd0 = ((gidx + rot) & 7) * 4;
      const lptr kp = kvb + hd0 * 544, pp = pqb + hd0 * 1024;
      wp = wob + hd0 * 2048;
      if (gidx <= 3) WAIT_VM(8); else if (gidx == 4) WAIT_VM(7); else if (gidx == 5) WAIT_VM(5); else if (gidx == 6) WAIT_VM(3); else WAIT_VM(1);
      bar();
      if (gidx == 0) STAMP(1);
      SB;
#pragma unroll
      for (int i = 0; i < 2; ++i) { kf[i] = lds_b128(kp + i * 544); pf[i] = lds_b128(pp + i * 1024); }
      SB;
      if (gidx + 5 < 8) issue(gidx + 5);
      if (gidx == 3) dma16(wsr, r8 * 4096 + sp, WS_BT + ct * 64 * 4096 + rt * 128, lw + L_BT);
      SB;
    }
#pragma unroll
    for (int i = 0; i < 2; ++i) {
      if (gidx > 0) {
        PIN(nd[i]);
        const float r = __builtin_amdgcn_rcpf(nd[i][8] + 1e-6f);
#pragma unroll
        for (int e = 0; e < 8; ++e) nd[i][e] *= r;
        acc = mfma32(wf[(gidx - 1) & 1][i], pack8(nd[i], 0), acc);
      }
      if (gidx < 8) {
        const f32x16 zero = {};
        nd[i] = mfma32(kf[i], pf[i], zero);
        wf[gidx & 1][i] = lds_tr2(wp + i * 2048, wp + i * 2048 + 1024);
      }
      SB;
    }
  }
  STAMP(2);
  lgkm0();
  WAIT_VM(0);
  bar();
  {
    LAS f32x4* xw = (LAS f32x4*)(lds + L_PQ + wv * 2048) + lane;
    const LAS f32x4* xr = (const LAS f32x4*)(lds + L_PQ + (wv ^ 4) * 2048) + lane;
    f32x4 keep[2];
#pragma unroll
    for (int G = 0; G < 2; ++G) {
      const f32x4 lo = {acc[4 * G], acc[4 * G + 1], acc[4 * G + 2], acc[4 * G + 3]};
      const f32x4 hi = {acc[8 + 4 * G], acc[9 + 4 * G], acc[10 + 4 * G], acc[11 + 4 * G]};
      if (par) { xw[64 * G] = lo; keep[G] = hi; } else { xw[64 * G] = hi; keep[G] = lo; }
    }
    const lptr bb = lds + L_BT + (wc * 32 + par * 16 + 4 * h + q) * 128 + (((4 * wr + 2 * blk + (p4 >> 1)) ^ ((q >> 1) << 2)) << 4) + (p4 & 1) * 8;
    const u32x2 bv0 = __builtin_bit_cast(u32x2, lds_tr(bb)), bv1 = __builtin_bit_cast(u32x2, lds_tr(bb + 1024));
    lgkm0();
    bar();
    PIN(gate);
    u32x2 o[2];
#pragma unroll
    for (int G = 0; G < 2; ++G) {
      const f32x4 a = keep[G] + xr[64 * G];
      const unsigned b0 = G ? bv1.x : bv0.x, b1 = G ? bv1.y : bv0.y;
      const float bf[4] = {bf2f(b0), bfhi(b0), bf2f(b1), bfhi(b1)};
      float r[4];
#pragma unroll
      for (int e = 0; e < 4; ++e) r[e] = bf[e] + gate * (a[e] - bf[e]);
      o[G] = u32x2{pk_bf16(r[0], r[1]), pk_bf16(r[2], r[3])};
    }
    char* dst = (char*)out + (unsigned)(trow * 1024 + ct * 128 + wc * 64 + par * 32 + h * 16);
    auto sx = __builtin_amdgcn_permlane32_swap(o[0].x, o[1].x, false, false);
    auto sy = __builtin_amdgcn_permlane32_swap(o[0].y, o[1].y, false, false);
    const u32x4 v = {sx[0], sy[0], sx[1], sy[1]};
    *(u32x4*)dst = v;
  }
  STAMP(3);
  STAMP_FLUSH;
  if (blockIdx.x == 0 && wv == 0) {
    const u64* s1 = stamps - 32768;
    u64 mn = ~0ull; u64 sum = 0; unsigned cnt = 0; u64 mx = 0;
    for (int i = lane; i < 2048; i += 64) {
      const u64 a = s1[i * 16]; mn = a < mn ? a : mn;
      const int role = ((i >> 3) >> 3) >= 16;
      const u64 v = s1[i * 16 + PROBE_S];
      if (role == PROBE_ROLE && v) { sum += v; cnt++; mx = v > mx ? v : mx; }
    }
    for (int o = 32; o; o >>= 1) { u64 t = __shfl_xor(mn, o); mn = t < mn ? t : mn; sum += __shfl_xor(sum, o); cnt += __shfl_xor(cnt, o); t = __shfl_xor(mx, o); mx = t > mx ? t : mx; }
    if (PROBE_K == 1) mn = s1[65536 + PROBE_S];
    { const u64 now = __builtin_amdgcn_s_memrealtime(); if (now - mn > 20000) mn = now; }
    while (__builtin_amdgcn_s_memrealtime() - mn < 6000) __builtin_amdgcn_s_sleep(1);
  }
  PIN(cpf);
  CODE_END("k2");
}

#define STARG(o) , (u64*)(ws + (8u << 20)) + (o)

__global__ void kstamp(u64* p) { *p = __builtin_amdgcn_s_memrealtime(); }
__global__ void __launch_bounds__(512) kbig(u64* p) { extern __shared__ char l_[]; const u64 t = __builtin_amdgcn_s_memrealtime(); if (threadIdx.x == 0 && blockIdx.x == 0) p[8] = t; }
extern "C" void kernel_launch(void* const* d_in, const int* in_sizes, int n_in, void* d_out, int out_size, void* d_ws,
                              size_t ws_size, hipStream_t stream) {
  const u16* x = (const u16*)d_in[0];
  const u16* wqkv = (const u16*)d_in[1];
  const u16* wo = (const u16*)d_in[2];
  const u16* lar = (const u16*)d_in[4];
  const u16* aim = (const u16*)d_in[5];
  const u16* cre = (const u16*)d_in[6];
  const u16* cim = (const u16*)d_in[7];
  const u16* ldt = (const u16*)d_in[8];
  const u16* dsk = (const u16*)d_in[9];
  const u16* gw = (const u16*)d_in[10];
  char* ws = (char*)d_ws;
  (void)hipFuncSetAttribute((const void*)k1, hipFuncAttributeMaxDynamicSharedMemorySize, 163840);
  (void)hipFuncSetAttribute((const void*)k2, hipFuncAttributeMaxDynamicSharedMemorySize, K2_LDS);
  (void)hipFuncSetAttribute((const void*)kbig, hipFuncAttributeMaxDynamicSharedMemorySize, 163840);
  kbig<<<256, 512, 163840, stream>>>((u64*)(ws + (8u << 20)) + 65536);
  k1<<<256, 512, 163840, stream>>>(x, wqkv, gw, lar, aim, cre, cim, ldt, dsk, ws STARG(0));
  kr<<<17, 256, 0, stream>>>(ws);
  k2<<<256, 512, K2_LDS, stream>>>(wo, ws, (u16*)d_out STARG(32768));
}
```

```cpp
#include <hip/hip_runtime.h>
#include <stdint.h>

typedef unsigned short u16;
typedef unsigned long long u64;
typedef __attribute__((ext_vector_type(8))) __bf16 bf16x8;
typedef __attribute__((ext_vector_type(2))) __bf16 bf16x2;
typedef __attribute__((ext_vector_type(8))) _Float16 f16x8;
typedef __attribute__((ext_vector_type(2))) _Float16 f16x2;
typedef __attribute__((ext_vector_type(4))) short s16x4;
typedef __attribute__((ext_vector_type(16))) float f32x16;
typedef __attribute__((ext_vector_type(4))) float f32x4;
typedef __attribute__((ext_vector_type(2))) float f32x2;
typedef __attribute__((ext_vector_type(4))) unsigned u32x4;
typedef __attribute__((ext_vector_type(2))) unsigned u32x2;
#define LAS __attribute__((address_space(3)))
#define CAS __attribute__((address_space(4)))
#define DI __device__ __forceinline__

constexpr unsigned WS_PQF = 0;
constexpr unsigned WS_BT  = 2u << 20;
constexpr unsigned WS_KVP = 4u << 20;
constexpr unsigned WS_KVZ = 5u << 20;
constexpr unsigned WS_G   = (5u << 20) + 65536;
constexpr unsigned WS_USED = 6u << 20;

constexpr float LOG2E = 1.4426950408889634f;

#define STAMP_DECL
#define STAMP(i)
#define STAMP_FLUSH
#define SB __builtin_amdgcn_sched_barrier(0)
#define PIN(x) asm volatile("" : "+v"(x))
#define PINS(x) asm volatile("" : "+s"(x))

typedef LAS char* lptr;
DI float bf2f(unsigned v) { return __uint_as_float(v << 16); }
DI float bfhi(unsigned v) { return __uint_as_float(v & 0xffff0000u); }
DI unsigned pk_bf16(float a, float b) { f32x2 v = {a, b}; return __builtin_bit_cast(unsigned, __builtin_convertvector(v, bf16x2)); }
DI float rbf(float a) { return __uint_as_float(pk_bf16(a, a) << 16); }
DI f16x2 pk_f16(f32x2 v) { return __builtin_convertvector(v, f16x2); }
DI f32x2 cmul(f32x2 a, f32x2 b) { f32x2 t = a.xx * b; f32x2 bs = {-b.y, b.x}; return __builtin_elementwise_fma(a.yy, bs, t); }
DI void cmul2(f32x2& r0, f32x2 a0, f32x2 b0, f32x2& r1, f32x2 a1, f32x2 b1) {
  f32x2 t0, t1;
  asm("v_pk_mul_f32 %0, %2, %3 op_sel_hi:[0,1]\n\tv_pk_mul_f32 %1, %4, %5 op_sel_hi:[0,1]\n\t"
      "v_pk_fma_f32 %0, %2, %3, %0 op_sel:[1,1,0] op_sel_hi:[1,0,1] neg_lo:[0,1,0]\n\t"
      "v_pk_fma_f32 %1, %4, %5, %1 op_sel:[1,1,0] op_sel_hi:[1,0,1] neg_lo:[0,1,0]"
      : "=&v"(t0), "=&v"(t1) : "v"(a0), "v"(b0), "v"(a1), "v"(b1));
  r0 = t0; r1 = t1;
}
DI f16x2 pk_f16c(f32x2 v) { const f32x2 c = {v.x, -v.y}; return __builtin_convertvector(c, f16x2); }
DI f16x2 cmulh(f16x2 a, f16x2 b) { f16x2 t = a.xx * b; f16x2 bs = {-b.y, b.x}; return __builtin_elementwise_fma(a.yy, bs, t); }
DI u32x4 mk_rsrc(const void* p, unsigned bytes) { const u64 a = (u64)p; return u32x4{(unsigned)a, (unsigned)(a >> 32), bytes, 0x00020000u}; }
DI void dma16(u32x4 rs, unsigned voff, unsigned soff, lptr l) {
  asm volatile("s_mov_b32 m0, %3\n\ts_nop 0\n\tbuffer_load_dwordx4 %0, %1, %2 offen lds"
               :: "v"(voff), "s"(rs), "s"(soff), "s"((unsigned)(size_t)l) : "memory");
}
DI int sig(int w, int n) { return 32 * ((n >> 3) & 1) + ((n >> 2) & 1) + 16 * w + 8 * (n >> 4) + 2 * (n & 3); }
DI bf16x8 lds_b128(lptr p) { return *(const LAS bf16x8*)p; }
DI s16x4 lds_tr(lptr p) { return __builtin_amdgcn_ds_read_tr16_b64_v4i16((LAS s16x4*)p); }
DI bf16x8 lds_tr2(lptr p0, lptr p1) { s16x4 a = lds_tr(p0), b = lds_tr(p1); return __builtin_bit_cast(bf16x8, __builtin_shufflevector(a, b, 0, 1, 2, 3, 4, 5, 6, 7)); }
DI f32x16 mfma32(bf16x8 a, bf16x8 b, f32x16 c) { return __builtin_amdgcn_mfma_f32_32x32x16_bf16(a, b, c, 0, 0, 0); }
DI f32x4 mfma16(bf16x8 a, bf16x8 b, f32x4 c) { return __builtin_amdgcn_mfma_f32_16x16x32_bf16(a, b, c, 0, 0, 0); }
DI f32x4 mfma16h(f16x8 a, f16x8 b, f32x4 c) { return __builtin_amdgcn_mfma_f32_16x16x32_f16(a, b, c, 0, 0, 0); }
DI void bar() { asm volatile("" ::: "memory"); __builtin_amdgcn_s_barrier(); asm volatile("" ::: "memory"); }
DI void lgkm0() { asm volatile("s_waitcnt lgkmcnt(0)" ::: "memory"); }
#define WAIT_VM(n) asm volatile("s_waitcnt vmcnt(" #n ")" ::: "memory")
DI float phi(float tp) {
  const float e = __builtin_amdgcn_fmed3f(__builtin_amdgcn_exp2f(tp * LOG2E), 0.f, 1.f);
  const int ti = __float_as_int(tp);
  return __int_as_float(ti > 0x358637bd ? ti : 0x358637bd) + e;
}
DI bf16x8 pack8(const f32x16& a, int o) {
  u32x4 p = {pk_bf16(a[o], a[o + 1]), pk_bf16(a[o + 2], a[o + 3]), pk_bf16(a[o + 4], a[o + 5]), pk_bf16(a[o + 6], a[o + 7])};
  return __builtin_bit_cast(bf16x8, p);
}
template <int CTRL, int RM = 0xf> DI int dppi(int v) { return __builtin_amdgcn_update_dpp(0, v, CTRL, RM, 0xf, true); }
template <int CTRL, int RM = 0xf> DI float dppf(float v) { return __int_as_float(dppi<CTRL, RM>(__float_as_int(v))); }
DI unsigned umax(unsigned a, unsigned b) { return a > b ? a : b; }
DI unsigned wave_umax(unsigned v) {
  v = umax(v, dppi<0x111>(v)); v = umax(v, dppi<0x112>(v)); v = umax(v, dppi<0x114>(v)); v = umax(v, dppi<0x118>(v));
  v = umax(v, dppi<0x142, 0xa>(v)); v = umax(v, dppi<0x143, 0xc>(v));
  return __builtin_amdgcn_readlane(v, 63);
}
DI float wave_sum(float v) {
  v += dppf<0x111>(v); v += dppf<0x112>(v); v += dppf<0x114>(v); v += dppf<0x118>(v);
  v += dppf<0x142, 0xa>(v); v += dppf<0x143, 0xc>(v);
  return __int_as_float(__builtin_amdgcn_readlane(__float_as_int(v), 63));
}
#define CODE_PREFETCH(NAME, dummy) do { u64 pc_; unsigned len_; \
  asm volatile("s_getpc_b64 %0\n.L" NAME "_beg:\n\ts_mov_b32 %1, .L" NAME "_end-.L" NAME "_beg" : "=s"(pc_), "=s"(len_)); \
  unsigned o_ = threadIdx.x * 64; o_ = o_ < len_ ? o_ : 0; \
  asm volatile("global_load_dword %0, %1, %2" : "=v"(dummy) : "v"(o_), "s"(pc_)); } while (0)
#define CODE_END(NAME) asm volatile(".L" NAME "_end:")
typedef __amdgpu_buffer_rsrc_t rsrc_t;
DI rsrc_t make_rsrc(void* p, unsigned bytes) { return __builtin_amdgcn_make_buffer_rsrc(p, 0, bytes, 0x00020000); }
constexpr int WT = 16;
DI void st128(rsrc_t r, unsigned off, u32x4 v) { __builtin_amdgcn_raw_buffer_store_b128(v, r, off, 0, WT); }
DI void st64(rsrc_t r, unsigned off, u32x2 v) { __builtin_amdgcn_raw_buffer_store_b64(v, r, off, 0, WT); }
DI void st32(rsrc_t r, unsigned off, unsigned v) { __builtin_amdgcn_raw_buffer_store_b32(v, r, off, 0, WT); }

constexpr int SLOT = 32768, NSLOT = 4;
constexpr int GA_EX = NSLOT * SLOT;
constexpr int GA_GW = GA_EX + 8 * 544 * 4;

DI void gemm_role(lptr lds, const u16* __restrict__ x, const u16* __restrict__ w, const u16* __restrict__ gw, char* ws,
                  int rb, int cg, int rot, u64* stamps) {
  STAMP_DECL;
  const int tid = threadIdx.x, lane = tid & 63;
  const int wv = __builtin_amdgcn_readfirstlane(tid >> 6), rg = wv & 3, hp = wv >> 2;
  const u32x4 xr = mk_rsrc(x, 2048 * 1024), wr_ = mk_rsrc(w, 512 * 3072);
  const unsigned xrow = tid >> 3, xo = rb * 131072 + xrow * 1024 + (((tid & 7) ^ ((xrow >> 1) & 7)) << 4);
  const unsigned wrow = tid >> 4, wp = (tid & 15) ^ ((wrow & 3) << 2);
  const unsigned wo_ = cg * 128 + wrow * 3072 + 1024 + (wp >> 3) * 1024 + ((wp & 7) << 4);
  const unsigned qo = cg * 128 + xrow * 3072 + (((tid & 7) ^ (((xrow >> 1) & 1) << 2)) << 4);
  const lptr lw = lds + wv * 1024;
  auto issue = [&](int c, int i) {
    const lptr slot = lw + (c % NSLOT) * SLOT;
    if (c < 8) {
      const unsigned kc = (c + rot) & 7;
      if (i == 0) dma16(xr, xo, kc * 128, slot);
      if (i == 1) dma16(wr_, wo_, kc * (64 * 3072), slot + 16384);
      if (i == 2) dma16(xr, xo, kc * 128 + 65536, slot + 8192);
      if (i == 3) dma16(wr_, wo_, kc * (64 * 3072) + 32 * 3072, slot + 24576);
    } else {
      dma16(wr_, qo, (((c - 8) * 4 + i + rot) & 7) * (64 * 3072), slot + i * 8192);
    }
  };
  STAMP(0);
  const bool do_g = (wv == cg) && (cg < 4);
  if (do_g) dma16(mk_rsrc(gw, 1024), lane * 16, 0, lds + GA_GW);
#pragma unroll
  for (int c = 0; c < 3; ++c) {
#pragma unroll
    for (int i = 0; i < 4; ++i) issue(c, i);
    if (c < 2) bar();
  }
  const int h = lane >> 5, row = (rg >> 1) * 64 + sig(rg & 1, lane & 31);
  const int gi = lane >> 4, blk = gi & 1, q = (lane & 15) >> 2, p4 = lane & 3;
  lptr ap[4], bp[2];
#pragma unroll
  for (int s = 0; s < 4; ++s) ap[s] = lds + row * 128 + (((2 * s + h) ^ ((row >> 1) & 7)) << 4);
#pragma unroll
  for (int ct = 0; ct < 2; ++ct) bp[ct] = lds + 16384 + (8 * h + q) * 256 + (((4 * (2 * ct + hp) + 2 * blk + (p4 >> 1)) ^ (q << 2)) << 4) + (p4 & 1) * 8;
  f32x16 ak, av, ag = {};
#pragma unroll
  for (int i = 0; i < 16; ++i) { ak[i] = 1e-6f; av[i] = 0.f; }
  bf16x8 xf[32], Bf[2][2];
  STAMP(1);
#pragma unroll
  for (int n = 0; n <= 32; ++n) {
    const int c = n >> 2, s = n & 3, nb = n & 1, so = (c & 1) * SLOT;
    if (s == 0 && n < 32) {
      if (c > 0) {
        lgkm0();
        if ((c & 1) == 0) {
          const int dlt = (c == 4) ? -2 * SLOT : 2 * SLOT;
#pragma unroll
          for (int i = 0; i < 4; ++i) { ap[i] += dlt; PIN(ap[i]); }
#pragma unroll
          for (int i = 0; i < 2; ++i) { bp[i] += dlt; PIN(bp[i]); }
        }
      }
      WAIT_VM(8);
      bar();
      if (c == 0) STAMP(2);
      SB;
    }
    if (n > 0) { ak = mfma32(xf[n - 1], Bf[nb ^ 1][0], ak); SB; }
    if (n < 32) { xf[n] = lds_b128(ap[s] + so); Bf[nb][0] = lds_tr2(bp[0] + so + s * 4096, bp[0] + so + s * 4096 + 1024); SB; }
    if (n > 0) { av = mfma32(xf[n - 1], Bf[nb ^ 1][1], av); SB; }
    if (n < 32) { Bf[nb][1] = lds_tr2(bp[1] + so + s * 4096, bp[1] + so + s * 4096 + 1024); SB; }
    if (c + 3 < 10) { issue(c + 3, s); SB; }
    if (n > 0 && s == 0 && do_g) {
      const lptr gp = lds + GA_GW + ((c - 1 + rot) & 7) * 128 + h * 16;
#pragma unroll
      for (int i = 0; i < 4; ++i) ag = mfma32(lds_b128(gp + i * 32), xf[n - 4 + i], ag);
      SB;
    }
  }
  STAMP(3);
  const rsrc_t wsr = make_rsrc(ws, WS_USED);
  {
    bf16x8 ones;
#pragma unroll
    for (int i = 0; i < 8; ++i) ones[i] = (__bf16)1.0f;
    const int dp = lane & 31, d = lane & 15;
    const int pos = ((d >> 2) & 1) * 8 + (d & 3) + 4 * (d >> 3);
    const bool lo = dp < 16;
#pragma unroll
    for (int i = 0; i < 16; ++i) ak[i] = phi(ak[i]);
    f32x16 kv = {}, z = {};
#pragma unroll
    for (int s = 0; s < 2; ++s) {
      const bf16x8 pk = pack8(ak, 8 * s), vv = pack8(av, 8 * s);
      kv = mfma32(vv, pk, kv);
      z = mfma32(ones, pk, z);
    }
    LAS float* e0 = (LAS float*)(lds + GA_EX) + (rg * 4 + 2 * hp + (dp >> 4)) * 272 + pos + 64 * h;
#pragma unroll
    for (int i = 0; i < 8; ++i) {
      float a = kv[i], b = kv[8 + i];
      PIN(a); PIN(b);
      e0[((i & 3) + 8 * (i >> 2)) * 16] = lo ? a : b;
    }
    if (h == 0) e0[256] = z[0];
  }
  STAMP(4);
  f32x16 aq;
#pragma unroll
  for (int i = 0; i < 16; ++i) aq[i] = 1e-6f;
  const lptr qp = lds + (8 * h + q) * 128 + (((4 * hp + 2 * blk + (p4 >> 1)) ^ ((q >> 1) << 2)) << 4) + (p4 & 1) * 8;
  bf16x8 Aq[4];
  constexpr int PD = 3;
#pragma unroll
  for (int n = 0; n < 32 + PD; ++n) {
    if (n == 0) { lgkm0(); WAIT_VM(4); bar(); SB; }
    if (n == 16) { lgkm0(); WAIT_VM(1); bar(); SB; }
    if (n >= PD) { aq = mfma32(Aq[(n - PD) & 3], xf[n - PD], aq); SB; }
    if (n < 32) { Aq[n & 3] = lds_tr2(qp + n * 2048, qp + n * 2048 + 512); SB; }
    if (n == PD) {
      const LAS f32x4* ex = (const LAS f32x4*)(lds + GA_EX) + (tid < 272 ? tid : 0);
      const f32x4 sum = (ex[0] + ex[272]) + (ex[544] + ex[816]);
      st128(wsr, tid < 272 ? WS_KVP + ((rb * 32 + cg * 4) * 68 + tid) * 16 : 0xfffffff0u, __builtin_bit_cast(u32x4, sum));
      SB;
    }
  }
  STAMP(5);
  {
    const unsigned o = WS_PQF + (((rb * 4 + rg) * 32 + cg * 4 + 2 * hp) * 64 + lane) * 16;
#pragma unroll
    for (int i = 0; i < 16; ++i) aq[i] = phi(aq[i]);
    st128(wsr, o, __builtin_bit_cast(u32x4, pack8(aq, 0)));
    st128(wsr, o + 1024, __builtin_bit_cast(u32x4, pack8(aq, 8)));
    if (do_g && h == 0) {
      const float g = __builtin_amdgcn_rcpf(1.f + __builtin_amdgcn_exp2f(-ag[0] * LOG2E));
      st32(wsr, WS_G + (rb * 128 + row) * 4, __float_as_uint(g));
    }
  }
  STAMP(6);
  STAMP_FLUSH;
}

constexpr int SC_ROW = 2064;
constexpr int KC_CP = 4224, KC_CH = 2 * KC_CP;
constexpr int EX_BASE = 4 * KC_CH;
constexpr int U_BASE = 57 * SC_ROW, U_PAD = 224, U_CP = 4576, U_CH = 2 * U_CP;
constexpr int T_BASE = U_BASE + 4 * U_CH, T_CH = 1536;
static_assert(T_BASE + 4 * T_CH <= 163840 && U_BASE % 16 == 0, "lds");

DI f32x2 cexp_p(float l2, float rv) {
  const float m = __builtin_amdgcn_exp2f(l2), f = __builtin_amdgcn_fractf(rv);
  const f32x2 r = {m * __builtin_amdgcn_cosf(f), m * __builtin_amdgcn_sinf(f)};
  return r;
}
DI bf16x8 lds_2b64(lptr p) {
  const u32x2 a = *(const volatile LAS u32x2*)p, b = *(const volatile LAS u32x2*)(p + 8);
  const u32x4 v = {a.x, a.y, b.x, b.y};
  return __builtin_bit_cast(bf16x8, v);
}
DI unsigned perm(unsigned hi, unsigned lo, unsigned sel) { return __builtin_amdgcn_perm(hi, lo, sel); }
constexpr unsigned LL = 0x01000100, LH = 0x03020302, HL = 0x05040504, SWL = 0x01000302, SWH = 0x05040706, HL_LH = 0x03020504;
template <int E0, int E1, int E2, int E3>
DI void put4(unsigned base, f16x2 a, f16x2 b, f16x2 c, f16x2 d) {
  constexpr int O = E0 < 28 ? 0 : 28;
  static_assert((E1 < 28) == (E0 < 28) && (E2 < 28) == (E0 < 28) && (E3 < 28) == (E0 < 28), "one base");
  asm volatile("s_mov_b32 m0, %4\n\ts_nop 0\n\tds_write_addtid_b32 %0 offset:%5\n\tds_write_addtid_b32 %1 offset:%6\n\t"
               "ds_write_addtid_b32 %2 offset:%7\n\tds_write_addtid_b32 %3 offset:%8"
               :: "v"(a), "v"(b), "v"(c), "v"(d), "s"(base + O * SC_ROW), "i"((E0 - O) * SC_ROW), "i"((E1 - O) * SC_ROW),
                  "i"((E2 - O) * SC_ROW), "i"((E3 - O) * SC_ROW) : "memory");
}

DI void conv_role(lptr lds, const u16* __restrict__ x, const u16* __restrict__ lar, const u16* __restrict__ aim,
                  const u16* __restrict__ cre, const u16* __restrict__ cim, const u16* __restrict__ ldt,
                  const u16* __restrict__ dsk, char* ws, int ch0, int jrot, u64* stamps) {
  STAMP_DECL;
  const int tid = threadIdx.x, lane = tid & 63;
  const int wv = __builtin_amdgcn_readfirstlane(tid >> 6), cl = wv & 3, half = wv >> 2;
  const int d = ch0 + cl;
  STAMP(0);
  const unsigned r_lar = ((const unsigned*)lar)[d * 64 + lane], r_aim = ((const unsigned*)aim)[d * 64 + lane];
  const unsigned r_cr = ((const unsigned*)cre)[d * 64 + lane], r_ci = ((const unsigned*)cim)[d * 64 + lane];
  const unsigned s_ldt = ((const CAS unsigned*)ldt)[d >> 1], s_dsk = ((const CAS unsigned*)dsk)[d >> 1];
  bar();
  const int tt = (tid + jrot * 64) & 511;
  const char* xg = (const char*)x + ch0 * 2;
  const unsigned xoff = tt * 4096;
  u32x2 ur[4], up = {0u, 0u};
  ur[0] = *(const u32x2*)(xg + xoff); ur[1] = *(const u32x2*)(xg + xoff + 1024);
  if (lane == 0 && tt > 0) up = *(const u32x2*)(xg + xoff - 1024);
  {
    const u32x4 zero = {0, 0, 0, 0};
    if (lane < 28) *(LAS u32x4*)(lds + U_BASE + cl * U_CH + half * U_CP + lane * 16) = zero;
  }
  const int j = lane & 15, g = lane >> 4;
  const unsigned scb = (unsigned)(size_t)lds + cl * 512 + half * 256;
  const lptr fb = lds + cl * 512 + half * 256 + j * SC_ROW + g * 16;
  unsigned selA = lane < 32 ? LL : lane == 32 ? HL : SWH, selB = lane < 32 ? HL_LH : SWH, selC = lane < 32 ? LL : SWH, selE = lane < 32 ? LL : SWL;
  if (half == 0) { const bool odd = !(lane & 1); selA = odd ? LH : LL; selB = odd ? HL : LH; selC = odd ? HL : SWL; }
  PIN(selA); PIN(selB); PIN(selC); PIN(selE);
  const unsigned shl = half ? 0 : 16;
  const float f_ldt = (d & 1) ? bfhi(s_ldt) : bf2f(s_ldt), f_dsk = (d & 1) ? bfhi(s_dsk) : bf2f(s_dsk);
  const float dt = __int_as_float(__builtin_amdgcn_readfirstlane(__float_as_int(rbf(__expf(f_ldt)))));
  SB;
  STAMP(1);
  const float l2 = -rbf(__builtin_amdgcn_exp2f(bfhi(r_lar << shl) * LOG2E)) * dt * LOG2E;
  const float rv = bfhi(r_aim << shl) * dt * 0.15915494309189535f;
  unsigned cm;
  {
    typedef __attribute__((ext_vector_type(2))) unsigned short u16x2;
    const u16x2 m2 = __builtin_elementwise_max(__builtin_bit_cast(u16x2, r_cr & 0x7fff7fffu), __builtin_bit_cast(u16x2, r_ci & 0x7fff7fffu));
    const unsigned m = __builtin_bit_cast(unsigned, m2);
    cm = umax(wave_umax(umax(m << 16, m)), 0x0da24260u);
  }
  const float cmax = __uint_as_float(cm), cinv = __builtin_amdgcn_rcpf(cmax);
  f32x4 kt[3];
  {
    f16x8 rf[4], t0[4];
    const f16x2 one = {(_Float16)1.f, (_Float16)0.f};
    const f32x2 E1 = cexp_p(l2, rv), E8 = cexp_p(l2 * 8.f, rv * 8.f), E128 = cexp_p(l2 * 128.f, rv * 128.f);
    f32x2 E2, E3, E4, E16, E24, E32, E64, E96, E256, E384, E512, H1, H2, H3, H4, dummy;
    const f32x2 H0 = {bfhi(r_cr << shl) * cinv, bfhi(r_ci << shl) * cinv};
    cmul2(E16, E8, E8, E256, E128, E128);
    cmul2(E32, E16, E16, E512, E256, E256);
    cmul2(E24, E16, E8, E2, E1, E1);
    cmul2(E64, E32, E32, H1, H0, E512);
    cmul2(E3, E2, E1, E4, E2, E2);
    cmul2(E96, E64, E32, H2, H1, E512);
    cmul2(E384, E256, E128, H3, H2, E512);
    ur[2] = *(const u32x2*)(xg + xoff + 2048); ur[3] = *(const u32x2*)(xg + xoff + 3072);
    {
      const f16x2 P1 = pk_f16c(E8), P2 = pk_f16c(E16), P3 = pk_f16c(E24), Q1 = pk_f16c(E32), Q2 = pk_f16c(E64), Q3 = pk_f16c(E96);
      put4<0, 1, 2, 3>(scb, one, P1, P2, P3);
      put4<4, 5, 6, 7>(scb, Q1, cmulh(Q1, P1), cmulh(Q1, P2), cmulh(Q1, P3));
      put4<8, 9, 10, 11>(scb, Q2, cmulh(Q2, P1), cmulh(Q2, P2), cmulh(Q2, P3));
      put4<12, 13, 14, 15>(scb, Q3, cmulh(Q3, P1), cmulh(Q3, P2), cmulh(Q3, P3));
    }
    H4 = cmul(H3, E512); (void)dummy;
    const f16x2 e1 = pk_f16(E1), e2 = pk_f16(E2), e3 = pk_f16(E3), e4 = pk_f16(E4);
    const f16x2 e5 = cmulh(e4, e1), e6 = cmulh(e4, e2), e7 = cmulh(e4, e3);
    const f16x2 S1 = pk_f16(E128), S2 = pk_f16(E256), S3 = pk_f16(E384);
    const f16x2 h0 = pk_f16(H0), h1 = pk_f16(H1), h2 = pk_f16(H2), h3 = pk_f16(H3);
    {
      const f16x2 G1 = cmulh(h0, S1);
      put4<16, 17, 18, 19>(scb, h0, cmulh(h0, e1), cmulh(h0, e2), cmulh(h0, e3));
      put4<20, 21, 22, 23>(scb, cmulh(h0, e4), cmulh(h0, e5), cmulh(h0, e6), cmulh(h0, e7));
      put4<24, 25, 26, 27>(scb, G1, cmulh(G1, e1), cmulh(G1, e2), cmulh(G1, e3));
      put4<28, 29, 30, 31>(scb, cmulh(G1, e4), cmulh(G1, e5), cmulh(G1, e6), cmulh(G1, e7));
    }
#pragma unroll
    for (int s = 0; s < 4; ++s) { rf[s] = *(const LAS f16x8*)(fb + s * 64); t0[s] = *(const LAS f16x8*)(fb + 16 * SC_ROW + s * 64); }
    SB;
    {
      const f16x2 G2 = cmulh(h0, S2), G3 = cmulh(h0, S3), G5 = cmulh(h1, S1), G6 = cmulh(h1, S2), G7 = cmulh(h1, S3);
      put4<32, 33, 34, 35>(scb, G2, cmulh(G2, e2), cmulh(G2, e4), cmulh(G2, e6));
      kt[0] = mfma16h(t0[0], rf[0], f32x4{0.f, 0.f, 0.f, 0.f});
      kt[0] = mfma16h(t0[1], rf[1], kt[0]);
      put4<36, 37, 38, 39>(scb, G3, cmulh(G3, e2), cmulh(G3, e4), cmulh(G3, e6));
      kt[0] = mfma16h(t0[2], rf[2], kt[0]);
      put4<40, 41, 42, 43>(scb, h1, cmulh(h1, e4), G5, cmulh(G5, e4));
      kt[0] = mfma16h(t0[3], rf[3], kt[0]);
      put4<44, 45, 46, 47>(scb, G6, cmulh(G6, e4), G7, cmulh(G7, e4));
    }
#pragma unroll
    for (int s = 0; s < 4; ++s) t0[s] = *(const LAS f16x8*)(fb + 32 * SC_ROW + s * 64);
    SB;
    put4<48, 49, 50, 51>(scb, h2, cmulh(h2, S1), cmulh(h2, S2), cmulh(h2, S3));
    put4<52, 53, 54, 55>(scb, h3, cmulh(h3, S1), cmulh(h3, S2), cmulh(h3, S3));
    *(LAS f16x2*)(lds + cl * 512 + half * 256 + lane * 4 + 56 * SC_ROW) = pk_f16(H4);
    f16x8 t2[4];
#pragma unroll
    for (int s = 0; s < 4; ++s) t2[s] = *(const LAS f16x8*)(fb + 48 * SC_ROW + s * 64);
    SB;
    kt[1] = f32x4{0.f, 0.f, 0.f, 0.f}; kt[2] = kt[1];
#pragma unroll
    for (int s = 0; s < 4; ++s) kt[1] = mfma16h(t0[s], rf[s], kt[1]);
#pragma unroll
    for (int s = 0; s < 4; ++s) kt[2] = mfma16h(t2[s], rf[s], kt[2]);
  }
  STAMP(2);
  {
    const u32x2 pv = {(unsigned)__builtin_amdgcn_update_dpp((int)up.x, (int)ur[3].x, 0x138, 0xf, 0xf, false),
                      (unsigned)__builtin_amdgcn_update_dpp((int)up.y, (int)ur[3].y, 0x138, 0xf, 0xf, false)};
    const lptr ub = lds + U_BASE + (U_PAD + 4 * tt) * 2;
#pragma unroll
    for (int c = 0; c < 4; ++c) {
      const unsigned sel = (c & 1) ? 0x07060302 : 0x05040100;
      const unsigned p = pv[c >> 1], r0 = ur[0][c >> 1], r1 = ur[1][c >> 1], r2 = ur[2][c >> 1], r3 = ur[3][c >> 1];
      *(LAS u32x2*)(ub + c * U_CH) = u32x2{perm(r1, r0, sel), perm(r3, r2, sel)};
      *(LAS u32x2*)(ub + c * U_CH + U_CP) = u32x2{perm(r0, p, sel), perm(r2, r1, sel)};
    }
  }
  const lptr Tb = lds + T_BASE + cl * T_CH;
  {
    LAS f32x4* xw = (LAS f32x4*)(lds + cl * 512 + half * 256 + g * SC_ROW) + j;
    const LAS f32x4* xr = (const LAS f32x4*)(lds + cl * 512 + (half ^ 1) * 256 + g * SC_ROW) + j;
    if (half) xw[0] = kt[0]; else { xw[0] = kt[1]; xw[4 * SC_ROW / 16] = kt[2]; }
    const float ks = 2.f * dt * cmax;
    const lptr p1 = half ? Tb + (256 + 64 * g + (g < 2 ? 4 : 2) * j) * 2 : Tb + (128 * (g >> 1) + 4 * (g & 1) + 8 * j) * 2;
    lptr p1b = p1 + (g < 2 ? 4 : 64);
    PIN(p1b);
    lgkm0();
    STAMP(3);
    bar();
    if (half == 0) {
      f32x4 a0 = (kt[0] + xr[0]) * ks;
      if (lane == 0) a0[0] += f_dsk;
      *(LAS u32x2*)p1 = u32x2{pk_bf16(a0[0], a0[1]), pk_bf16(a0[2], a0[3])};
    } else {
      const f32x4 a1 = (kt[1] + xr[0]) * ks, a2 = (kt[2] + xr[4 * SC_ROW / 16]) * ks;
      *(LAS unsigned*)p1 = pk_bf16(a1[0], a1[1]);
      *(LAS unsigned*)p1b = pk_bf16(a1[2], a1[3]);
      const unsigned q0 = pk_bf16(a2[0], a2[1]), q1 = pk_bf16(a2[2], a2[3]);
      LAS u16* p2 = (LAS u16*)Tb + 512 + 64 * g + j;
      p2[0] = (u16)q0; p2[16] = (u16)(q0 >> 16); p2[32] = (u16)q1; p2[48] = (u16)(q1 >> 16);
    }
  }
  lgkm0();
  STAMP(4);
  bar();
  {
    const lptr kc = lds + cl * KC_CH + lane * 16;
    auto emit = [&](int Y0, unsigned P0, unsigned P1, unsigned P2, unsigned P3, unsigned P4) {
      *(LAS u32x4*)(kc + Y0 * 16) = u32x4{P0, P1, P2, P3};
      *(LAS u32x4*)(kc + KC_CP + Y0 * 16) = u32x4{P1, P2, P3, P4};
    };
    if (half == 0) {
      const LAS unsigned* p = (const LAS unsigned*)(Tb + ((575 - lane) >> 1) * 4);
      const unsigned D0 = p[32], D1 = p[33], F0 = p[0], F1 = p[1], Z = *(const LAS unsigned*)Tb;
      SB;
      const unsigned e1 = perm(D1, D0, selB), e0 = perm(D1, D0, selA), f1 = perm(F1, F0, selB), f0 = perm(F1, F0, selA);
      emit(0, e1, e1, perm(D1, D0, selC), e0, e0);
      emit(64, f1, f1, perm(F1, F0, selC), f0, f0);
      if (lane < 4) emit(256, lane == 0 ? perm(0, Z, SWL) : 0u, 0u, 0u, 0u, 0u);
    } else {
      const LAS unsigned* p = (const LAS unsigned*)(Tb + (255 - lane) * 4);
      const LAS unsigned* r = (const LAS unsigned*)(Tb + (lane < 32 ? 760 - 8 * lane : 1008 - 16 * lane));
      const unsigned F0 = p[0], F1 = p[1], D0 = r[0], D1 = r[1], D2 = r[2], D3 = r[3], D4 = r[4];
      SB;
      const unsigned e2 = perm(F1, F0, HL);
      emit(128, e2, e2, perm(F1, F0, LH), perm(F1, F0, SWL), perm(F1, F0, LL));
      emit(192, perm(D4, D2, selA), perm(lane < 32 ? D2 : D3, D1, selB), perm(D2, D1, selC), perm(D1, D0, selB), perm(D1, D0, selE));
    }
  }
  lgkm0();
  STAMP(5);
  bar();
  const int a_ = j;
  const lptr ab = lds + cl * KC_CH + ((a_ & 1) ? KC_CP + 4088 : 4096) - 8 * (a_ >> 1) + 16 * g - 3584 - 192 - half * 256;
  const lptr bb = lds + U_BASE + cl * U_CH + ((a_ & 1) ? 0 : U_CP) + (U_PAD + 32 * (a_ >> 1) + 8 * g) * 2 - 192 - half * 256;
  f32x4 acc[8];
#pragma unroll
  for (int i = 0; i < 8; ++i) acc[i] = f32x4{0.f, 0.f, 0.f, 0.f};
  LAS f32x4* exw = (LAS f32x4*)(lds + EX_BASE + wv * 4096) + lane;
  f32x4 keep[4];
  {
    bf16x8 A[2][8], B[2][8];
#pragma unroll
    for (int r = 0; r < 4; ++r) {
      const int nb = r & 1;
#pragma unroll
      for (int mu = 0; mu < 8; ++mu) {
        B[nb][mu] = lds_b128(bb + 512 * mu + 64 * (3 - r));
        A[nb][mu] = lds_2b64(ab + 512 * (7 - mu) + 64 * (3 - r));
        SB;
        if (r > 0) {
#pragma unroll
          for (int n = 0; n < 8; ++n) if (mu + n < 8) acc[mu + n] = mfma16(A[nb ^ 1][mu], B[nb ^ 1][n], acc[mu + n]);
          SB;
        }
      }
    }
    auto tiles = [&](int t0) {
#pragma unroll
      for (int mu = 0; mu < 8; ++mu)
#pragma unroll
        for (int n = 0; n < 8; ++n) if (mu + n >= t0 && mu + n < t0 + 4) acc[mu + n] = mfma16(A[1][mu], B[1][n], acc[mu + n]);
    };
    if (half) {
      tiles(0); SB; tiles(4);
#pragma unroll
      for (int i = 0; i < 4; ++i) { exw[64 * i] = acc[i]; keep[i] = acc[4 + i]; }
    } else {
      tiles(4); SB; tiles(0);
#pragma unroll
      for (int i = 0; i < 4; ++i) { exw[64 * i] = acc[4 + i]; keep[i] = acc[i]; }
    }
  }
  STAMP(6);
  {
    const LAS f32x4* exr = (const LAS f32x4*)(lds + EX_BASE + (wv ^ 4) * 4096) + lane;
    const rsrc_t wsr = make_rsrc(ws, WS_USED);
    const unsigned bto = WS_BT + d * 4096 + half * 2048 + (a_ >> 2) * 128 + g * 32 + (a_ & 3) * 8;
    lgkm0();
    STAMP(7);
    bar();
#pragma unroll
    for (int i = 0; i < 4; ++i) {
      const f32x4 v = keep[i] + exr[64 * i];
      st64(wsr, bto + 512 * i, u32x2{pk_bf16(v[0], v[1]), pk_bf16(v[2], v[3])});
    }
  }
  STAMP(8);
  STAMP_FLUSH;
}

__global__ void __launch_bounds__(512, 2) k1(const u16* __restrict__ x, const u16* __restrict__ wqkv, const u16* __restrict__ gw,
                                             const u16* __restrict__ lar, const u16* __restrict__ aim, const u16* __restrict__ cre,
                                             const u16* __restrict__ cim, const u16* __restrict__ ldt, const u16* __restrict__ dsk,
                                             char* ws
) {
  u64* stamps = nullptr;
  extern __shared__ __attribute__((aligned(16))) char lds_[];
  const lptr lds = (lptr)lds_;
  const int b = blockIdx.x, xcd = b & 7, idx = b >> 3;
  unsigned cpf;
  CODE_PREFETCH("k1", cpf);
  if (idx < 16) {
    conv_role(lds, x, lar, aim, cre, cim, ldt, dsk, ws, xcd * 64 + idx * 4, idx, stamps);
  } else {
    const int rl = idx & 3, cl = (idx >> 2) & 3;
    gemm_role(lds, x, wqkv, gw, ws, (xcd >> 1) * 4 + rl, (xcd & 1) * 4 + cl, 2 * ((rl + cl) & 3), stamps);
  }
  PIN(cpf);
  CODE_END("k1");
}

__global__ void __launch_bounds__(256) kr(char* ws) {
  const int i = blockIdx.x * 256 + threadIdx.x;
  if (i < 4352) {
    const f32x2* p = (const f32x2*)(ws + WS_KVP) + i;
    f32x2 s = {0.f, 0.f};
#pragma unroll
    for (int r = 0; r < 16; ++r) s += p[r * 4352];
    st32(make_rsrc(ws, WS_USED), WS_KVZ + i * 4, pk_bf16(s.x, s.y));
  }
}

constexpr int L_PQ = 0, L_WO = 65536, L_KVZ = 131072, L_BT = L_KVZ + 17408, K2_LDS = L_BT + 8192;

__global__ void __launch_bounds__(512, 2) k2(const u16* __restrict__ wo, const char* __restrict__ ws, u16* __restrict__ out
) {
  STAMP_DECL;
  extern __shared__ __attribute__((aligned(16))) char lds_[];
  const lptr lds = (lptr)lds_;
  const int b = blockIdx.x, xcd = b & 7, idx = b >> 3;
  const int rl = idx & 7, cl = idx >> 3, rt = (xcd >> 1) * 8 + rl, ct = (xcd & 1) * 4 + cl, rot = (rl + 2 * cl) & 7;
  const int tid = threadIdx.x, lane = tid & 63;
  const int wv = __builtin_amdgcn_readfirstlane(tid >> 6), wr = wv & 1, wc = (wv >> 1) & 1, par = wv >> 2;
  const unsigned r8 = tid >> 3, sp = ((tid & 7) ^ (((r8 >> 1) & 1) << 2)) << 4;
  const lptr lw = lds + wv * 1024;
  STAMP(0);
  unsigned cpf;
  CODE_PREFETCH("k2", cpf);
  const int h = lane >> 5, t = lane & 31;
  float gate;
  const int trow = rt * 64 + sig(wr, t);
  asm volatile("global_load_dword %0, %1, %2" : "=v"(gate) : "v"(trow * 4), "s"(ws + WS_G));
  const u32x4 wsr = mk_rsrc(ws, WS_USED), wor = mk_rsrc(wo, 512 * 1024);
  dma16(wsr, tid * 16, WS_KVZ, lw + L_KVZ);
  dma16(wsr, tid * 16, WS_KVZ + 8192, lw + L_KVZ + 8192);
  if (wv == 0) dma16(wsr, tid * 16, WS_KVZ + 16384, lw + L_KVZ + 16384);
  const unsigned pqo = WS_PQF + rt * 65536 + (tid >> 8) * 32768 + (tid & 255) * 16, woo = ct * 128 + r8 * 1024 + sp;
  const lptr lpq = lds + L_PQ + (wv >> 2) * 32768 + (wv & 3) * 1024;
  auto issue = [&](int gidx) {
    const int gr = (gidx + rot) & 7;
    dma16(wsr, pqo, gr * 4096, lpq + gr * 4096);
    dma16(wor, woo, gr * 65536, lw + L_WO + gr * 8192);
  };
  issue(0); issue(1);
  bar();
  issue(2); issue(3); issue(4);
  const int gi = lane >> 4, blk = gi & 1, q = (lane & 15) >> 2, p4 = lane & 3;
  const lptr kvb = lds + L_KVZ + (t < 16 ? t : 16) * 32 + h * 16 + par * 1088;
  const lptr pqb = lds + L_PQ + wr * 32768 + lane * 16 + par * 2048;
  const lptr wob = lds + L_WO + (4 * h + q) * 128 + (((4 * wc + 2 * blk + (p4 >> 1)) ^ ((q >> 1) << 2)) << 4) + (p4 & 1) * 8 + par * 4096;
  f32x16 acc = {};
  f32x16 nd[2];
  bf16x8 wf[2][2], kf[2], pf[2];
#pragma unroll
  for (int gidx = 0; gidx <= 8; ++gidx) {
    lptr wp;
    if (gidx < 8) {
      const int hd0 = ((gidx + rot) & 7) * 4;
      const lptr kp = kvb + hd0 * 544, pp = pqb + hd0 * 1024;
      wp = wob + hd0 * 2048;
      if (gidx <= 3) WAIT_VM(8); else if (gidx == 4) WAIT_VM(7); else if (gidx == 5) WAIT_VM(5); else if (gidx == 6) WAIT_VM(3); else WAIT_VM(1);
      bar();
      if (gidx == 0) STAMP(1);
      SB;
#pragma unroll
      for (int i = 0; i < 2; ++i) { kf[i] = lds_b128(kp + i * 544); pf[i] = lds_b128(pp + i * 1024); }
      SB;
      if (gidx + 5 < 8) issue(gidx + 5);
      if (gidx == 3) dma16(wsr, r8 * 4096 + sp, WS_BT + ct * 64 * 4096 + rt * 128, lw + L_BT);
      SB;
    }
#pragma unroll
    for (int i = 0; i < 2; ++i) {
      if (gidx > 0) {
        PIN(nd[i]);
        const float r = __builtin_amdgcn_rcpf(nd[i][8] + 1e-6f);
#pragma unroll
        for (int e = 0; e < 8; ++e) nd[i][e] *= r;
        acc = mfma32(wf[(gidx - 1) & 1][i], pack8(nd[i], 0), acc);
      }
      if (gidx < 8) {
        const f32x16 zero = {};
        nd[i] = mfma32(kf[i], pf[i], zero);
        wf[gidx & 1][i] = lds_tr2(wp + i * 2048, wp + i * 2048 + 1024);
      }
      SB;
    }
  }
  STAMP(2);
  lgkm0();
  WAIT_VM(0);
  bar();
  {
    LAS f32x4* xw = (LAS f32x4*)(lds + L_PQ + wv * 2048) + lane;
    const LAS f32x4* xr = (const LAS f32x4*)(lds + L_PQ + (wv ^ 4) * 2048) + lane;
    f32x4 keep[2];
#pragma unroll
    for (int G = 0; G < 2; ++G) {
      const f32x4 lo = {acc[4 * G], acc[4 * G + 1], acc[4 * G + 2], acc[4 * G + 3]};
      const f32x4 hi = {acc[8 + 4 * G], acc[9 + 4 * G], acc[10 + 4 * G], acc[11 + 4 * G]};
      if (par) { xw[64 * G] = lo; keep[G] = hi; } else { xw[64 * G] = hi; keep[G] = lo; }
    }
    const lptr bb = lds + L_BT + (wc * 32 + par * 16 + 4 * h + q) * 128 + (((4 * wr + 2 * blk + (p4 >> 1)) ^ ((q >> 1) << 2)) << 4) + (p4 & 1) * 8;
    const u32x2 bv0 = __builtin_bit_cast(u32x2, lds_tr(bb)), bv1 = __builtin_bit_cast(u32x2, lds_tr(bb + 1024));
    lgkm0();
    bar();
    PIN(gate);
    u32x2 o[2];
#pragma unroll
    for (int G = 0; G < 2; ++G) {
      const f32x4 a = keep[G] + xr[64 * G];
      const unsigned b0 = G ? bv1.x : bv0.x, b1 = G ? bv1.y : bv0.y;
      const float bf[4] = {bf2f(b0), bfhi(b0), bf2f(b1), bfhi(b1)};
      float r[4];
#pragma unroll
      for (int e = 0; e < 4; ++e) r[e] = bf[e] + gate * (a[e] - bf[e]);
      o[G] = u32x2{pk_bf16(r[0], r[1]), pk_bf16(r[2], r[3])};
    }
    char* dst = (char*)out + (unsigned)(trow * 1024 + ct * 128 + wc * 64 + par * 32 + h * 16);
    auto sx = __builtin_amdgcn_permlane32_swap(o[0].x, o[1].x, false, false);
    auto sy = __builtin_amdgcn_permlane32_swap(o[0].y, o[1].y, false, false);
    const u32x4 v = {sx[0], sy[0], sx[1], sy[1]};
    *(u32x4*)dst = v;
  }
  STAMP(3);
  STAMP_FLUSH;
  PIN(cpf);
  CODE_END("k2");
}

#define STARG(o)

extern "C" void kernel_launch(void* const* d_in, const int* in_sizes, int n_in, void* d_out, int out_size, void* d_ws,
                              size_t ws_size, hipStream_t stream) {
  const u16* x = (const u16*)d_in[0];
  const u16* wqkv = (const u16*)d_in[1];
  const u16* wo = (const u16*)d_in[2];
  const u16* lar = (const u16*)d_in[4];
  const u16* aim = (const u16*)d_in[5];
  const u16* cre = (const u16*)d_in[6];
  const u16* cim = (const u16*)d_in[7];
  const u16* ldt = (const u16*)d_in[8];
  const u16* dsk = (const u16*)d_in[9];
  const u16* gw = (const u16*)d_in[10];
  char* ws = (char*)d_ws;
  (void)hipFuncSetAttribute((const void*)k1, hipFuncAttributeMaxDynamicSharedMemorySize, 163840);
  (void)hipFuncSetAttribute((const void*)k2, hipFuncAttributeMaxDynamicSharedMemorySize, K2_LDS);
  k1<<<256, 512, 163840, stream>>>(x, wqkv, gw, lar, aim, cre, cim, ldt, dsk, ws STARG(0));
  kr<<<17, 256, 0, stream>>>(ws);
  k2<<<256, 512, K2_LDS, stream>>>(wo, ws, (u16*)d_out STARG(32768));
}
```

```cpp
#include <hip/hip_runtime.h>
#include <stdint.h>

typedef unsigned short u16;
typedef unsigned long long u64;
typedef __attribute__((ext_vector_type(8))) __bf16 bf16x8;
typedef __attribute__((ext_vector_type(2))) __bf16 bf16x2;
typedef __attribute__((ext_vector_type(8))) _Float16 f16x8;
typedef __attribute__((ext_vector_type(2))) _Float16 f16x2;
typedef __attribute__((ext_vector_type(4))) short s16x4;
typedef __attribute__((ext_vector_type(16))) float f32x16;
typedef __attribute__((ext_vector_type(4))) float f32x4;
typedef __attribute__((ext_vector_type(2))) float f32x2;
typedef __attribute__((ext_vector_type(4))) unsigned u32x4;
typedef __attribute__((ext_vector_type(2))) unsigned u32x2;
#define LAS __attribute__((address_space(3)))
#define CAS __attribute__((address_space(4)))
#define DI __device__ __forceinline__

constexpr unsigned WS_PQF = 0;
constexpr unsigned WS_BT  = 2u << 20;
constexpr unsigned WS_KVP = 4u << 20;
constexpr unsigned WS_KVZ = 6u << 20;
constexpr unsigned WS_G   = (6u << 20) + 65536;
constexpr unsigned WS_FLAG = (6u << 20) + 131072;
constexpr unsigned WS_EPOCH = WS_FLAG + 65536;
constexpr unsigned WS_USED = 7u << 20;
constexpr unsigned SPIN_CAP = 200000;

constexpr float LOG2E = 1.4426950408889634f;

#define STAMP_DECL
#define STAMP_PARM
#define STAMP_ARG
#define STAMP(i)
#define STAMP_FLUSH
#define SB __builtin_amdgcn_sched_barrier(0)
#define PIN(x) asm volatile("" : "+v"(x))
#define PINS(x) asm volatile("" : "+s"(x))

typedef LAS char* lptr;
DI float bf2f(unsigned v) { return __uint_as_float(v << 16); }
DI float bfhi(unsigned v) { return __uint_as_float(v & 0xffff0000u); }
DI unsigned pk_bf16(float a, float b) { f32x2 v = {a, b}; return __builtin_bit_cast(unsigned, __builtin_convertvector(v, bf16x2)); }
DI float rbf(float a) { return __uint_as_float(pk_bf16(a, a) << 16); }
DI f16x2 pk_f16(f32x2 v) { return __builtin_convertvector(v, f16x2); }
DI f32x2 cmul(f32x2 a, f32x2 b) { f32x2 t = a.xx * b; f32x2 bs = {-b.y, b.x}; return __builtin_elementwise_fma(a.yy, bs, t); }
DI void cmul2(f32x2& r0, f32x2 a0, f32x2 b0, f32x2& r1, f32x2 a1, f32x2 b1) {
  f32x2 t0, t1;
  asm("v_pk_mul_f32 %0, %2, %3 op_sel_hi:[0,1]\n\tv_pk_mul_f32 %1, %4, %5 op_sel_hi:[0,1]\n\t"
      "v_pk_fma_f32 %0, %2, %3, %0 op_sel:[1,1,0] op_sel_hi:[1,0,1] neg_lo:[0,1,0]\n\t"
      "v_pk_fma_f32 %1, %4, %5, %1 op_sel:[1,1,0] op_sel_hi:[1,0,1] neg_lo:[0,1,0]"
      : "=&v"(t0), "=&v"(t1) : "v"(a0), "v"(b0), "v"(a1), "v"(b1));
  r0 = t0; r1 = t1;
}
DI f16x2 pk_f16c(f32x2 v) { const f32x2 c = {v.x, -v.y}; return __builtin_convertvector(c, f16x2); }
DI f16x2 cmulh(f16x2 a, f16x2 b) { f16x2 t = a.xx * b; f16x2 bs = {-b.y, b.x}; return __builtin_elementwise_fma(a.yy, bs, t); }
DI u32x4 mk_rsrc(const void* p, unsigned bytes) { const u64 a = (u64)p; return u32x4{(unsigned)a, (unsigned)(a >> 32), bytes, 0x00020000u}; }
DI void dma16(u32x4 rs, unsigned voff, unsigned soff, lptr l) {
  asm volatile("s_mov_b32 m0, %3\n\ts_nop 0\n\tbuffer_load_dwordx4 %0, %1, %2 offen lds"
               :: "v"(voff), "s"(rs), "s"(soff), "s"((unsigned)(size_t)l) : "memory");
}
DI void dma16c(u32x4 rs, unsigned voff, unsigned soff, lptr l) {
  asm volatile("s_mov_b32 m0, %3\n\ts_nop 0\n\tbuffer_load_dwordx4 %0, %1, %2 offen sc1 lds"
               :: "v"(voff), "s"(rs), "s"(soff), "s"((unsigned)(size_t)l) : "memory");
}
DI u32x4 ld128c(u32x4 rs, unsigned voff) { u32x4 v; asm volatile("buffer_load_dwordx4 %0, %1, %2, 0 offen sc1" : "=v"(v) : "v"(voff), "s"(rs) : "memory"); return v; }
DI u32x2 poll64(u32x4 rs, unsigned voff) {
  u32x2 v; asm volatile("buffer_load_dwordx2 %0, %1, %2, 0 offen sc1\n\ts_waitcnt vmcnt(0)" : "=&v"(v) : "v"(voff), "s"(rs) : "memory"); return v;
}
DI unsigned ld32c(u32x4 rs, unsigned voff) { unsigned v; asm volatile("buffer_load_dword %0, %1, %2, 0 offen sc1" : "=v"(v) : "v"(voff), "s"(rs) : "memory"); return v; }
DI int sig(int w, int n) { return 32 * ((n >> 3) & 1) + ((n >> 2) & 1) + 16 * w + 8 * (n >> 4) + 2 * (n & 3); }
DI bf16x8 lds_b128(lptr p) { return *(const LAS bf16x8*)p; }
DI s16x4 lds_tr(lptr p) { return __builtin_amdgcn_ds_read_tr16_b64_v4i16((LAS s16x4*)p); }
DI bf16x8 lds_tr2(lptr p0, lptr p1) { s16x4 a = lds_tr(p0), b = lds_tr(p1); return __builtin_bit_cast(bf16x8, __builtin_shufflevector(a, b, 0, 1, 2, 3, 4, 5, 6, 7)); }
DI f32x16 mfma32(bf16x8 a, bf16x8 b, f32x16 c) { return __builtin_amdgcn_mfma_f32_32x32x16_bf16(a, b, c, 0, 0, 0); }
DI f32x4 mfma16(bf16x8 a, bf16x8 b, f32x4 c) { return __builtin_amdgcn_mfma_f32_16x16x32_bf16(a, b, c, 0, 0, 0); }
DI f32x4 mfma16h(f16x8 a, f16x8 b, f32x4 c) { return __builtin_amdgcn_mfma_f32_16x16x32_f16(a, b, c, 0, 0, 0); }
DI void bar() { asm volatile("" ::: "memory"); __builtin_amdgcn_s_barrier(); asm volatile("" ::: "memory"); }
DI void lgkm0() { asm volatile("s_waitcnt lgkmcnt(0)" ::: "memory"); }
#define WAIT_VM(n) asm volatile("s_waitcnt vmcnt(" #n ")" ::: "memory")
DI float phi(float tp) {
  const float e = __builtin_amdgcn_fmed3f(__builtin_amdgcn_exp2f(tp * LOG2E), 0.f, 1.f);
  const int ti = __float_as_int(tp);
  return __int_as_float(ti > 0x358637bd ? ti : 0x358637bd) + e;
}
DI bf16x8 pack8(const f32x16& a, int o) {
  u32x4 p = {pk_bf16(a[o], a[o + 1]), pk_bf16(a[o + 2], a[o + 3]), pk_bf16(a[o + 4], a[o + 5]), pk_bf16(a[o + 6], a[o + 7])};
  return __builtin_bit_cast(bf16x8, p);
}
template <int CTRL, int RM = 0xf> DI int dppi(int v) { return __builtin_amdgcn_update_dpp(0, v, CTRL, RM, 0xf, true); }
template <int CTRL, int RM = 0xf> DI float dppf(float v) { return __int_as_float(dppi<CTRL, RM>(__float_as_int(v))); }
DI unsigned umax(unsigned a, unsigned b) { return a > b ? a : b; }
DI unsigned wave_umax(unsigned v) {
  v = umax(v, dppi<0x111>(v)); v = umax(v, dppi<0x112>(v)); v = umax(v, dppi<0x114>(v)); v = umax(v, dppi<0x118>(v));
  v = umax(v, dppi<0x142, 0xa>(v)); v = umax(v, dppi<0x143, 0xc>(v));
  return __builtin_amdgcn_readlane(v, 63);
}
DI float wave_sum(float v) {
  v += dppf<0x111>(v); v += dppf<0x112>(v); v += dppf<0x114>(v); v += dppf<0x118>(v);
  v += dppf<0x142, 0xa>(v); v += dppf<0x143, 0xc>(v);
  return __int_as_float(__builtin_amdgcn_readlane(__float_as_int(v), 63));
}
DI void dma4(u32x4 rs, unsigned voff, lptr l) {
  asm volatile("s_mov_b32 m0, %2\n\ts_nop 0\n\tbuffer_load_dword %0, %1, 0 offen sc1 lds" :: "v"(voff), "s"(rs), "s"((unsigned)(size_t)l) : "memory");
}
#define CODE_PREFETCH(NAME, dump) do { u64 pc_; unsigned len_; \
  asm volatile("s_getpc_b64 %0\n.L" NAME "_beg:\n\ts_mov_b32 %1, .L" NAME "_end-.L" NAME "_beg" : "=s"(pc_), "=s"(len_)); \
  asm volatile("s_mov_b32 m0, %2\n\ts_nop 0\n\tbuffer_load_dword %0, %1, 0 offen lds" \
               :: "v"(threadIdx.x * 64), "s"(mk_rsrc((const void*)pc_, len_)), "s"((unsigned)(size_t)(dump)) : "memory"); } while (0)
#define CODE_END(NAME) asm volatile(".L" NAME "_end:")
typedef __amdgpu_buffer_rsrc_t rsrc_t;
DI rsrc_t make_rsrc(void* p, unsigned bytes) { return __builtin_amdgcn_make_buffer_rsrc(p, 0, bytes, 0x00020000); }
constexpr int WT = 16;
DI void st128(rsrc_t r, unsigned off, u32x4 v) { __builtin_amdgcn_raw_buffer_store_b128(v, r, off, 0, WT); }
DI void st64(rsrc_t r, unsigned off, u32x2 v) { __builtin_amdgcn_raw_buffer_store_b64(v, r, off, 0, WT); }
DI void st32(rsrc_t r, unsigned off, unsigned v) { __builtin_amdgcn_raw_buffer_store_b32(v, r, off, 0, WT); }

constexpr int L_PQ = 0, L_WO = 66560, L_KVZ = L_WO + 65536, L_BT = L_KVZ + 17408;
struct Out { u32x4 wor; unsigned woo; int wv; lptr lds; };
DI void wo_issue(const Out& o, int gr) { dma16(o.wor, o.woo, gr * 65536, o.lds + L_WO + o.wv * 1024 + gr * 8192); }
constexpr int L_DUMP = 163840 - 512, L_EPOCH = 163840 - 256;
DI u32x2 next_tag(lptr lds, int wv) {
  const u32x2 e = *(const volatile LAS u32x2*)(lds + L_EPOCH + wv * 8);
  const u64 t = (((u64)(unsigned)__builtin_amdgcn_readfirstlane(e.y) << 32) | (unsigned)__builtin_amdgcn_readfirstlane(e.x)) + 1;
  return u32x2{(unsigned)t, (unsigned)(t >> 32)};
}

constexpr int SLOT = 32768, NSLOT = 4;
constexpr int GA_EX = L_KVZ;
constexpr int GA_GW = GA_EX + 8 * 544 * 4;

DI u32x2 gemm_role(lptr lds, const u16* __restrict__ x, const u16* __restrict__ w, const u16* __restrict__ gw, char* ws,
                   int rb, int cg, int rot, const Out& wo STAMP_PARM) {
  const int tid = threadIdx.x, lane = tid & 63;
  const int wv = __builtin_amdgcn_readfirstlane(tid >> 6), rg = wv & 3, hp = wv >> 2;
  const u32x4 xr = mk_rsrc(x, 2048 * 1024), wr_ = mk_rsrc(w, 512 * 3072);
  const unsigned xrow = tid >> 3, xo = rb * 131072 + xrow * 1024 + (((tid & 7) ^ ((xrow >> 1) & 7)) << 4);
  const unsigned wrow = tid >> 4, wp = (tid & 15) ^ ((wrow & 3) << 2);
  const unsigned wo_ = cg * 128 + wrow * 3072 + 1024 + (wp >> 3) * 1024 + ((wp & 7) << 4);
  const unsigned qo = cg * 128 + xrow * 3072 + (((tid & 7) ^ (((xrow >> 1) & 1) << 2)) << 4);
  const lptr lw = lds + wv * 1024;
  auto issue = [&](int c, int i) {
    const lptr slot = lw + (c % NSLOT) * SLOT;
    if (c < 8) {
      const unsigned kc = (c + rot) & 7;
      if (i == 0) dma16(xr, xo, kc * 128, slot);
      if (i == 1) dma16(wr_, wo_, kc * (64 * 3072), slot + 16384);
      if (i == 2) dma16(xr, xo, kc * 128 + 65536, slot + 8192);
      if (i == 3) dma16(wr_, wo_, kc * (64 * 3072) + 32 * 3072, slot + 24576);
    } else {
      dma16(wr_, qo, (((c - 8) * 4 + i + rot) & 7) * (64 * 3072), slot + i * 8192);
    }
  };
  STAMP(0);
  const bool do_g = (wv == cg) && (cg < 4);
  if (do_g) dma16(mk_rsrc(gw, 1024), lane * 16, 0, lds + GA_GW);
#pragma unroll
  for (int c = 0; c < 3; ++c) {
#pragma unroll
    for (int i = 0; i < 4; ++i) issue(c, i);
    if (c < 2) bar();
  }
  const int h = lane >> 5, row = (rg >> 1) * 64 + sig(rg & 1, lane & 31);
  const int gi = lane >> 4, blk = gi & 1, q = (lane & 15) >> 2, p4 = lane & 3;
  lptr ap[4], bp[2];
#pragma unroll
  for (int s = 0; s < 4; ++s) ap[s] = lds + row * 128 + (((2 * s + h) ^ ((row >> 1) & 7)) << 4);
#pragma unroll
  for (int ct = 0; ct < 2; ++ct) bp[ct] = lds + 16384 + (8 * h + q) * 256 + (((4 * (2 * ct + hp) + 2 * blk + (p4 >> 1)) ^ (q << 2)) << 4) + (p4 & 1) * 8;
  f32x16 ak, av, ag = {};
#pragma unroll
  for (int i = 0; i < 16; ++i) { ak[i] = 1e-6f; av[i] = 0.f; }
  bf16x8 xf[32], Bf[2][2];
  STAMP(1);
#pragma unroll
  for (int n = 0; n <= 32; ++n) {
    const int c = n >> 2, s = n & 3, nb = n & 1, so = (c & 1) * SLOT;
    if (s == 0 && n < 32) {
      if (c > 0) {
        lgkm0();
        if ((c & 1) == 0) {
          const int dlt = (c == 4) ? -2 * SLOT : 2 * SLOT;
#pragma unroll
          for (int i = 0; i < 4; ++i) { ap[i] += dlt; PIN(ap[i]); }
#pragma unroll
          for (int i = 0; i < 2; ++i) { bp[i] += dlt; PIN(bp[i]); }
        }
      }
      WAIT_VM(8);
      bar();
      if (c == 0) STAMP(2);
      SB;
    }
    if (n > 0) { ak = mfma32(xf[n - 1], Bf[nb ^ 1][0], ak); SB; }
    if (n < 32) { xf[n] = lds_b128(ap[s] + so); Bf[nb][0] = lds_tr2(bp[0] + so + s * 4096, bp[0] + so + s * 4096 + 1024); SB; }
    if (n > 0) { av = mfma32(xf[n - 1], Bf[nb ^ 1][1], av); SB; }
    if (n < 32) { Bf[nb][1] = lds_tr2(bp[1] + so + s * 4096, bp[1] + so + s * 4096 + 1024); SB; }
    if (c + 3 < 10) { issue(c + 3, s); SB; }
    if (n > 0 && s == 0 && do_g) {
      const lptr gp = lds + GA_GW + ((c - 1 + rot) & 7) * 128 + h * 16;
#pragma unroll
      for (int i = 0; i < 4; ++i) ag = mfma32(lds_b128(gp + i * 32), xf[n - 4 + i], ag);
      SB;
    }
  }
  STAMP(3);
  const rsrc_t wsr = make_rsrc(ws, WS_USED);
  const u32x4 wsl = mk_rsrc(ws, WS_USED);
  const u32x2 tag = next_tag(lds, wv);
  {
    bf16x8 ones;
#pragma unroll
    for (int i = 0; i < 8; ++i) ones[i] = (__bf16)1.0f;
    const int dp = lane & 31, d = lane & 15;
    const int pos = ((d >> 2) & 1) * 8 + (d & 3) + 4 * (d >> 3);
    const bool lo = dp < 16;
#pragma unroll
    for (int i = 0; i < 16; ++i) { ak[i] = phi(ak[i]); if ((i & 3) == 3 && i < 12) { PIN(ak[i]); wo_issue(wo, i >> 2); } }
    f32x16 kv = {}, z = {};
#pragma unroll
    for (int s = 0; s < 2; ++s) {
      const bf16x8 pk = pack8(ak, 8 * s), vv = pack8(av, 8 * s);
      kv = mfma32(vv, pk, kv);
      z = mfma32(ones, pk, z);
    }
    LAS float* e0 = (LAS float*)(lds + GA_EX) + (rg * 4 + 2 * hp + (dp >> 4)) * 272 + pos + 64 * h;
#pragma unroll
    for (int i = 0; i < 8; ++i) {
      float a = kv[i], b = kv[8 + i];
      PIN(a); PIN(b);
      e0[((i & 3) + 8 * (i >> 2)) * 16] = lo ? a : b;
    }
    if (h == 0) e0[256] = z[0];
  }
  STAMP(4);
  f32x16 aq;
#pragma unroll
  for (int i = 0; i < 16; ++i) aq[i] = 1e-6f;
  const lptr qp = lds + (8 * h + q) * 128 + (((4 * hp + 2 * blk + (p4 >> 1)) ^ ((q >> 1) << 2)) << 4) + (p4 & 1) * 8;
  bf16x8 Aq[4];
  constexpr int PD = 3;
  const unsigned v0 = (rb >> 2) * 272 + (rb & 3) * 64 + 8 * wv + 2 * (lane >> 4), vz = (rb >> 2) * 272 + 256 + 4 * (rb & 3) + 2 * ((lane >> 4) & 1);
  const unsigned gsrc = WS_KVP + ((lane & 15) * 8 + cg) * 8704;
  const unsigned gko = gsrc + v0 * 8, gzo = wv == 0 ? gsrc + vz * 8 : 0xffffffe0u;
  u32x4 gk, gz;
#pragma unroll
  for (int n = 0; n < 32 + PD; ++n) {
    if (n == 0) { lgkm0(); WAIT_VM(7); bar(); SB; }
    if (n == 16) { lgkm0(); WAIT_VM(5); bar(); SB; }
    if (n >= PD) { aq = mfma32(Aq[(n - PD) & 3], xf[n - PD], aq); SB; }
    if (n < 32) { Aq[n & 3] = lds_tr2(qp + n * 2048, qp + n * 2048 + 512); SB; }
    if (n == PD) {
      const LAS f32x4* ex = (const LAS f32x4*)(lds + GA_EX) + (tid < 272 ? tid : 0);
      const u32x4 sum = __builtin_bit_cast(u32x4, (ex[0] + ex[272]) + (ex[544] + ex[816]));
      const unsigned o = tid < 272 ? WS_KVP + (rb * 8 + cg) * 8704 + tid * 32 : 0xffffffe0u;
      st128(wsr, o, u32x4{sum.x, tag.x, sum.y, tag.x});
      st128(wsr, o + 16, u32x4{sum.z, tag.x, sum.w, tag.x});
      SB;
    }
    if (n == 27) { gk = ld128c(wsl, gko); gz = ld128c(wsl, gzo); SB; }
  }
  PIN(aq); PIN(ag);
  STAMP(5);
  {
    WAIT_VM(0);
    PIN(gk); PIN(gz);
#pragma unroll 1
    for (unsigned spin = 0; spin < SPIN_CAP; ++spin) {
      const bool ok = gk.y == tag.x && gk.w == tag.x && (wv != 0 || (gz.y == tag.x && gz.w == tag.x));
      if (__all(ok)) break;
      __builtin_amdgcn_s_sleep(4);
      asm volatile("buffer_load_dwordx4 %0, %2, %4, 0 offen sc1\n\tbuffer_load_dwordx4 %1, %3, %4, 0 offen sc1\n\ts_waitcnt vmcnt(0)"
                   : "=&v"(gk), "=&v"(gz) : "v"(gko), "v"(gzo), "s"(wsl) : "memory");
    }
    auto rowsum = [&](unsigned u) { float v = __uint_as_float(u); v += dppf<0x111>(v); v += dppf<0x112>(v); v += dppf<0x114>(v); v += dppf<0x118>(v); return v; };
    const bool last = (lane & 15) == 15;
    st32(wsr, last ? WS_KVZ + (cg * 1088 + v0) * 2 : 0xfffffff0u, pk_bf16(rowsum(gk.x), rowsum(gk.z)));
    if (wv == 0) st32(wsr, last && lane < 32 ? WS_KVZ + (cg * 1088 + vz) * 2 : 0xfffffff0u, pk_bf16(rowsum(gz.x), rowsum(gz.z)));
  }
  STAMP(6);
  {
    const unsigned o = WS_PQF + (((rb * 4 + rg) * 32 + cg * 4 + 2 * hp) * 64 + lane) * 16;
#pragma unroll
    for (int i = 0; i < 8; ++i) aq[i] = phi(aq[i]);
    st128(wsr, o, __builtin_bit_cast(u32x4, pack8(aq, 0)));
#pragma unroll
    for (int i = 8; i < 16; ++i) aq[i] = phi(aq[i]);
    st128(wsr, o + 1024, __builtin_bit_cast(u32x4, pack8(aq, 8)));
    if (do_g && h == 0) {
      const float g = __builtin_amdgcn_rcpf(1.f + __builtin_amdgcn_exp2f(-ag[0] * LOG2E));
      st32(wsr, WS_G + (rb * 128 + row) * 4, __float_as_uint(g));
    }
  }
#pragma unroll
  for (int i = 3; i < 8; ++i) wo_issue(wo, i);
  STAMP(7);
  WAIT_VM(5);
  STAMP(8);
  bar();
  STAMP(13);
  if (tid < 32) st64(wsr, WS_FLAG + tid * 2048 + (cg * 16 + rb) * 8, tag);
  return tag;
}

constexpr int SC_ROW = 2064;
constexpr int KC_CP = 4224, KC_CH = 2 * KC_CP;
constexpr int EX_BASE = 4 * KC_CH;
constexpr int U_BASE = 57 * SC_ROW, U_PAD = 224, U_CP = 4576, U_CH = 2 * U_CP;
constexpr int T_BASE = U_BASE + 4 * U_CH, T_CH = 1536;
static_assert(T_BASE + 4 * T_CH <= 163840 && U_BASE % 16 == 0, "lds");

DI f32x2 cexp_p(float l2, float rv) {
  const float m = __builtin_amdgcn_exp2f(l2), f = __builtin_amdgcn_fractf(rv);
  const f32x2 r = {m * __builtin_amdgcn_cosf(f), m * __builtin_amdgcn_sinf(f)};
  return r;
}
DI bf16x8 lds_2b64(lptr p) {
  const u32x2 a = *(const volatile LAS u32x2*)p, b = *(const volatile LAS u32x2*)(p + 8);
  const u32x4 v = {a.x, a.y, b.x, b.y};
  return __builtin_bit_cast(bf16x8, v);
}
DI unsigned perm(unsigned hi, unsigned lo, unsigned sel) { return __builtin_amdgcn_perm(hi, lo, sel); }
constexpr unsigned LL = 0x01000100, LH = 0x03020302, HL = 0x05040504, SWL = 0x01000302, SWH = 0x05040706, HL_LH = 0x03020504;
template <int E0, int E1, int E2, int E3>
DI void put4(unsigned base, f16x2 a, f16x2 b, f16x2 c, f16x2 d) {
  constexpr int O = E0 < 28 ? 0 : 28;
  static_assert((E1 < 28) == (E0 < 28) && (E2 < 28) == (E0 < 28) && (E3 < 28) == (E0 < 28), "one base");
  asm volatile("s_mov_b32 m0, %4\n\ts_nop 0\n\tds_write_addtid_b32 %0 offset:%5\n\tds_write_addtid_b32 %1 offset:%6\n\t"
               "ds_write_addtid_b32 %2 offset:%7\n\tds_write_addtid_b32 %3 offset:%8"
               :: "v"(a), "v"(b), "v"(c), "v"(d), "s"(base + O * SC_ROW), "i"((E0 - O) * SC_ROW), "i"((E1 - O) * SC_ROW),
                  "i"((E2 - O) * SC_ROW), "i"((E3 - O) * SC_ROW) : "memory");
}

DI u32x2 conv_role(lptr lds, const u16* __restrict__ x, const u16* __restrict__ lar, const u16* __restrict__ aim,
                   const u16* __restrict__ cre, const u16* __restrict__ cim, const u16* __restrict__ ldt,
                   const u16* __restrict__ dsk, char* ws, int ch0, int jrot, const Out& wo STAMP_PARM) {
  const int tid = threadIdx.x, lane = tid & 63;
  const int wv = __builtin_amdgcn_readfirstlane(tid >> 6), cl = wv & 3, half = wv >> 2;
  const int d = ch0 + cl;
  STAMP(0);
  const unsigned r_lar = ((const unsigned*)lar)[d * 64 + lane], r_aim = ((const unsigned*)aim)[d * 64 + lane];
  const unsigned r_cr = ((const unsigned*)cre)[d * 64 + lane], r_ci = ((const unsigned*)cim)[d * 64 + lane];
  const unsigned s_ldt = ((const CAS unsigned*)ldt)[d >> 1], s_dsk = ((const CAS unsigned*)dsk)[d >> 1];
  bar();
  const int tt = (tid + jrot * 64) & 511;
  const char* xg = (const char*)x + ch0 * 2;
  const unsigned xoff = tt * 4096;
  u32x2 ur[4];
  ur[0] = *(const u32x2*)(xg + xoff); ur[1] = *(const u32x2*)(xg + xoff + 1024);
  u32x2 up;
  {
    const u32x4 zero = {0, 0, 0, 0};
    if (lane < 28) *(LAS u32x4*)(lds + U_BASE + cl * U_CH + half * U_CP + lane * 16) = zero;
  }
  const int j = lane & 15, g = lane >> 4;
  const unsigned scb = (unsigned)(size_t)lds + cl * 512 + half * 256;
  const lptr fb = lds + cl * 512 + half * 256 + j * SC_ROW + g * 16;
  unsigned selA = lane < 32 ? LL : lane == 32 ? HL : SWH, selB = lane < 32 ? HL_LH : SWH, selC = lane < 32 ? LL : SWH, selE = lane < 32 ? LL : SWL;
  if (half == 0) { const bool odd = !(lane & 1); selA = odd ? LH : LL; selB = odd ? HL : LH; selC = odd ? HL : SWL; }
  PIN(selA); PIN(selB); PIN(selC); PIN(selE);
  const unsigned shl = half ? 0 : 16;
  const float f_ldt = (d & 1) ? bfhi(s_ldt) : bf2f(s_ldt), f_dsk = (d & 1) ? bfhi(s_dsk) : bf2f(s_dsk);
  const float dt = __int_as_float(__builtin_amdgcn_readfirstlane(__float_as_int(rbf(__expf(f_ldt)))));
  SB;
  const float l2 = -rbf(__builtin_amdgcn_exp2f(bfhi(r_lar << shl) * LOG2E)) * dt * LOG2E;
  const float rv = bfhi(r_aim << shl) * dt * 0.15915494309189535f;
  unsigned cm;
  {
    typedef __attribute__((ext_vector_type(2))) unsigned short u16x2;
    const u16x2 m2 = __builtin_elementwise_max(__builtin_bit_cast(u16x2, r_cr & 0x7fff7fffu), __builtin_bit_cast(u16x2, r_ci & 0x7fff7fffu));
    const unsigned m = __builtin_bit_cast(unsigned, m2);
    cm = umax(wave_umax(umax(m << 16, m)), 0x0da24260u);
  }
  const float cmax = __uint_as_float(cm), cinv = __builtin_amdgcn_rcpf(cmax);
  f32x4 kt[3];
  {
    f16x8 rf[4], t0[4];
    const f16x2 one = {(_Float16)1.f, (_Float16)0.f};
    const f32x2 E1 = cexp_p(l2, rv), E8 = cexp_p(l2 * 8.f, rv * 8.f), E128 = cexp_p(l2 * 128.f, rv * 128.f);
    f32x2 E2, E3, E4, E16, E24, E32, E64, E96, E256, E384, E512, H1, H2, H3, H4, dummy;
    const f32x2 H0 = {bfhi(r_cr << shl) * cinv, bfhi(r_ci << shl) * cinv};
    cmul2(E16, E8, E8, E256, E128, E128);
    cmul2(E32, E16, E16, E512, E256, E256);
    cmul2(E24, E16, E8, E2, E1, E1);
    cmul2(E64, E32, E32, H1, H0, E512);
    cmul2(E3, E2, E1, E4, E2, E2);
    cmul2(E96, E64, E32, H2, H1, E512);
    cmul2(E384, E256, E128, H3, H2, E512);
    {
      const f16x2 P1 = pk_f16c(E8), P2 = pk_f16c(E16), P3 = pk_f16c(E24), Q1 = pk_f16c(E32), Q2 = pk_f16c(E64), Q3 = pk_f16c(E96);
      put4<0, 1, 2, 3>(scb, one, P1, P2, P3);
      put4<4, 5, 6, 7>(scb, Q1, cmulh(Q1, P1), cmulh(Q1, P2), cmulh(Q1, P3));
      put4<8, 9, 10, 11>(scb, Q2, cmulh(Q2, P1), cmulh(Q2, P2), cmulh(Q2, P3));
      put4<12, 13, 14, 15>(scb, Q3, cmulh(Q3, P1), cmulh(Q3, P2), cmulh(Q3, P3));
    }
    ur[2] = *(const u32x2*)(xg + xoff + 2048); ur[3] = *(const u32x2*)(xg + xoff + 3072);
    up = __builtin_amdgcn_raw_buffer_load_b64(make_rsrc((void*)x, 2048 * 1024), lane == 0 && tt > 0 ? ch0 * 2 + xoff - 1024 : 0xfffffff0u, 0, 0);
    H4 = cmul(H3, E512); (void)dummy;
    const f16x2 e1 = pk_f16(E1), e2 = pk_f16(E2), e3 = pk_f16(E3), e4 = pk_f16(E4);
    const f16x2 e5 = cmulh(e4, e1), e6 = cmulh(e4, e2), e7 = cmulh(e4, e3);
    const f16x2 S1 = pk_f16(E128), S2 = pk_f16(E256), S3 = pk_f16(E384);
    const f16x2 h0 = pk_f16(H0), h1 = pk_f16(H1), h2 = pk_f16(H2), h3 = pk_f16(H3);
    {
      const f16x2 G1 = cmulh(h0, S1);
      put4<16, 17, 18, 19>(scb, h0, cmulh(h0, e1), cmulh(h0, e2), cmulh(h0, e3));
      put4<20, 21, 22, 23>(scb, cmulh(h0, e4), cmulh(h0, e5), cmulh(h0, e6), cmulh(h0, e7));
      put4<24, 25, 26, 27>(scb, G1, cmulh(G1, e1), cmulh(G1, e2), cmulh(G1, e3));
      put4<28, 29, 30, 31>(scb, cmulh(G1, e4), cmulh(G1, e5), cmulh(G1, e6), cmulh(G1, e7));
    }
#pragma unroll
    for (int s = 0; s < 4; ++s) { rf[s] = *(const LAS f16x8*)(fb + s * 64); t0[s] = *(const LAS f16x8*)(fb + 16 * SC_ROW + s * 64); }
    SB;
    {
      const f16x2 G2 = cmulh(h0, S2), G3 = cmulh(h0, S3), G5 = cmulh(h1, S1), G6 = cmulh(h1, S2), G7 = cmulh(h1, S3);
      put4<32, 33, 34, 35>(scb, G2, cmulh(G2, e2), cmulh(G2, e4), cmulh(G2, e6));
      kt[0] = mfma16h(t0[0], rf[0], f32x4{0.f, 0.f, 0.f, 0.f});
      kt[0] = mfma16h(t0[1], rf[1], kt[0]);
      put4<36, 37, 38, 39>(scb, G3, cmulh(G3, e2), cmulh(G3, e4), cmulh(G3, e6));
      kt[0] = mfma16h(t0[2], rf[2], kt[0]);
      put4<40, 41, 42, 43>(scb, h1, cmulh(h1, e4), G5, cmulh(G5, e4));
      kt[0] = mfma16h(t0[3], rf[3], kt[0]);
      put4<44, 45, 46, 47>(scb, G6, cmulh(G6, e4), G7, cmulh(G7, e4));
    }
#pragma unroll
    for (int s = 0; s < 4; ++s) t0[s] = *(const LAS f16x8*)(fb + 32 * SC_ROW + s * 64);
    SB;
    put4<48, 49, 50, 51>(scb, h2, cmulh(h2, S1), cmulh(h2, S2), cmulh(h2, S3));
    put4<52, 53, 54, 55>(scb, h3, cmulh(h3, S1), cmulh(h3, S2), cmulh(h3, S3));
    *(LAS f16x2*)(lds + cl * 512 + half * 256 + lane * 4 + 56 * SC_ROW) = pk_f16(H4);
    f16x8 t2[4];
#pragma unroll
    for (int s = 0; s < 4; ++s) t2[s] = *(const LAS f16x8*)(fb + 48 * SC_ROW + s * 64);
    SB;
    kt[1] = f32x4{0.f, 0.f, 0.f, 0.f}; kt[2] = kt[1];
#pragma unroll
    for (int s = 0; s < 4; ++s) kt[1] = mfma16h(t0[s], rf[s], kt[1]);
#pragma unroll
    for (int s = 0; s < 4; ++s) kt[2] = mfma16h(t2[s], rf[s], kt[2]);
  }
  STAMP(2);
  {
    const u32x2 pv = {(unsigned)__builtin_amdgcn_update_dpp((int)up.x, (int)ur[3].x, 0x138, 0xf, 0xf, false),
                      (unsigned)__builtin_amdgcn_update_dpp((int)up.y, (int)ur[3].y, 0x138, 0xf, 0xf, false)};
    const lptr ub = lds + U_BASE + (U_PAD + 4 * tt) * 2;
#pragma unroll
    for (int c = 0; c < 4; ++c) {
      const unsigned sel = (c & 1) ? 0x07060302 : 0x05040100;
      const unsigned p = pv[c >> 1], r0 = ur[0][c >> 1], r1 = ur[1][c >> 1], r2 = ur[2][c >> 1], r3 = ur[3][c >> 1];
      *(LAS u32x2*)(ub + c * U_CH) = u32x2{perm(r1, r0, sel), perm(r3, r2, sel)};
      *(LAS u32x2*)(ub + c * U_CH + U_CP) = u32x2{perm(r0, p, sel), perm(r2, r1, sel)};
    }
  }
  const lptr Tb = lds + T_BASE + cl * T_CH;
  {
    LAS f32x4* xw = (LAS f32x4*)(lds + cl * 512 + half * 256 + g * SC_ROW) + j;
    const LAS f32x4* xr = (const LAS f32x4*)(lds + cl * 512 + (half ^ 1) * 256 + g * SC_ROW) + j;
    if (half) xw[0] = kt[0]; else { xw[0] = kt[1]; xw[4 * SC_ROW / 16] = kt[2]; }
    const float ks = 2.f * dt * cmax;
    const lptr p1 = half ? Tb + (256 + 64 * g + (g < 2 ? 4 : 2) * j) * 2 : Tb + (128 * (g >> 1) + 4 * (g & 1) + 8 * j) * 2;
    lptr p1b = p1 + (g < 2 ? 4 : 64);
    PIN(p1b);
    lgkm0();
    STAMP(3);
    bar();
    if (half == 0) {
      f32x4 a0 = (kt[0] + xr[0]) * ks;
      if (lane == 0) a0[0] += f_dsk;
      *(LAS u32x2*)p1 = u32x2{pk_bf16(a0[0], a0[1]), pk_bf16(a0[2], a0[3])};
    } else {
      const f32x4 a1 = (kt[1] + xr[0]) * ks, a2 = (kt[2] + xr[4 * SC_ROW / 16]) * ks;
      *(LAS unsigned*)p1 = pk_bf16(a1[0], a1[1]);
      *(LAS unsigned*)p1b = pk_bf16(a1[2], a1[3]);
      const unsigned q0 = pk_bf16(a2[0], a2[1]), q1 = pk_bf16(a2[2], a2[3]);
      LAS u16* p2 = (LAS u16*)Tb + 512 + 64 * g + j;
      p2[0] = (u16)q0; p2[16] = (u16)(q0 >> 16); p2[32] = (u16)q1; p2[48] = (u16)(q1 >> 16);
    }
  }
  lgkm0();
  STAMP(4);
  bar();
  {
    const lptr kc = lds + cl * KC_CH + lane * 16;
    auto emit = [&](int Y0, unsigned P0, unsigned P1, unsigned P2, unsigned P3, unsigned P4) {
      *(LAS u32x4*)(kc + Y0 * 16) = u32x4{P0, P1, P2, P3};
      *(LAS u32x4*)(kc + KC_CP + Y0 * 16) = u32x4{P1, P2, P3, P4};
    };
    if (half == 0) {
      const LAS unsigned* p = (const LAS unsigned*)(Tb + ((575 - lane) >> 1) * 4);
      const unsigned D0 = p[32], D1 = p[33], F0 = p[0], F1 = p[1], Z = *(const LAS unsigned*)Tb;
      SB;
      const unsigned e1 = perm(D1, D0, selB), e0 = perm(D1, D0, selA), f1 = perm(F1, F0, selB), f0 = perm(F1, F0, selA);
      emit(0, e1, e1, perm(D1, D0, selC), e0, e0);
      emit(64, f1, f1, perm(F1, F0, selC), f0, f0);
      if (lane < 4) emit(256, lane == 0 ? perm(0, Z, SWL) : 0u, 0u, 0u, 0u, 0u);
    } else {
      const LAS unsigned* p = (const LAS unsigned*)(Tb + (255 - lane) * 4);
      const LAS unsigned* r = (const LAS unsigned*)(Tb + (lane < 32 ? 760 - 8 * lane : 1008 - 16 * lane));
      const unsigned F0 = p[0], F1 = p[1], D0 = r[0], D1 = r[1], D2 = r[2], D3 = r[3], D4 = r[4];
      SB;
      const unsigned e2 = perm(F1, F0, HL);
      emit(128, e2, e2, perm(F1, F0, LH), perm(F1, F0, SWL), perm(F1, F0, LL));
      emit(192, perm(D4, D2, selA), perm(lane < 32 ? D2 : D3, D1, selB), perm(D2, D1, selC), perm(D1, D0, selB), perm(D1, D0, selE));
    }
  }
  lgkm0();
  STAMP(5);
  bar();
  const int a_ = j;
  const lptr ab = lds + cl * KC_CH + ((a_ & 1) ? KC_CP + 4088 : 4096) - 8 * (a_ >> 1) + 16 * g - 3584 - 192 - half * 256;
  const lptr bb = lds + U_BASE + cl * U_CH + ((a_ & 1) ? 0 : U_CP) + (U_PAD + 32 * (a_ >> 1) + 8 * g) * 2 - 192 - half * 256;
  f32x4 acc[8];
#pragma unroll
  for (int i = 0; i < 8; ++i) acc[i] = f32x4{0.f, 0.f, 0.f, 0.f};
  LAS f32x4* exw = (LAS f32x4*)(lds + EX_BASE + wv * 4096) + lane;
  f32x4 keep[4];
  {
    u32x2 Al[2][8], Ah[2][8];
    bf16x8 B[2][8];
    auto rd = [&](int r, int k) {
      const int nb = r & 1, mu = k / 3, w = k % 3;
      if (w == 0) B[nb][mu] = lds_b128(bb + 512 * mu + 64 * (3 - r));
      if (w == 1) Al[nb][mu] = *(const volatile LAS u32x2*)(ab + 512 * (7 - mu) + 64 * (3 - r));
      if (w == 2) Ah[nb][mu] = *(const volatile LAS u32x2*)(ab + 512 * (7 - mu) + 64 * (3 - r) + 8);
    };
    auto AF = [&](int nb, int mu) { return __builtin_bit_cast(bf16x8, u32x4{Al[nb][mu].x, Al[nb][mu].y, Ah[nb][mu].x, Ah[nb][mu].y}); };
#pragma unroll
    for (int k = 0; k < 24; ++k) rd(0, k);
    SB;
#pragma unroll
    for (int r = 1; r < 4; ++r) {
      const int cb = (r - 1) & 1;
      int cnt = 0;
#pragma unroll
      for (int m = 0; m < 8; ++m)
#pragma unroll
        for (int n = 0; n <= m; ++n)
#pragma unroll
          for (int sw = 0; sw < 2; ++sw)
            if (m + n < 8 && (sw == 0 || n < m)) {
              const int mu = sw ? n : m, nu = sw ? m : n;
              acc[m + n] = mfma16(AF(cb, mu), B[cb][nu], acc[m + n]);
              SB;
              if (cnt % 3 != 2) rd(r, cnt - cnt / 3);
              else if (cnt == 14 || cnt == 29) wo_issue(wo, 2 * (r - 1) + (cnt == 29));
              SB;
              ++cnt;
            }
    }
    auto tiles = [&](int t0) {
#pragma unroll
      for (int mu = 0; mu < 8; ++mu)
#pragma unroll
        for (int n = 0; n < 8; ++n) if (mu + n >= t0 && mu + n < t0 + 4) acc[mu + n] = mfma16(AF(1, mu), B[1][n], acc[mu + n]);
    };
    if (half) {
      tiles(0); SB; tiles(4);
#pragma unroll
      for (int i = 0; i < 4; ++i) { exw[64 * i] = acc[i]; keep[i] = acc[4 + i]; }
    } else {
      tiles(4); SB; tiles(0);
#pragma unroll
      for (int i = 0; i < 4; ++i) { exw[64 * i] = acc[4 + i]; keep[i] = acc[i]; }
    }
  }
  STAMP(6);
  {
    const LAS f32x4* exr = (const LAS f32x4*)(lds + EX_BASE + (wv ^ 4) * 4096) + lane;
    const rsrc_t wsr = make_rsrc(ws, WS_USED);
    const u32x2 tag = next_tag(lds, wv);
    const unsigned bto = WS_BT + d * 4096 + half * 2048 + (a_ >> 2) * 128 + g * 32 + (a_ & 3) * 8;
    lgkm0();
    STAMP(7);
    bar();
#pragma unroll
    for (int i = 0; i < 4; ++i) {
      const f32x4 v = keep[i] + exr[64 * i];
      st64(wsr, bto + 512 * i, u32x2{pk_bf16(v[0], v[1]), pk_bf16(v[2], v[3])});
    }
    wo_issue(wo, 6); wo_issue(wo, 7);
    STAMP(8);
    lgkm0();
    WAIT_VM(2);
    STAMP(1);
    bar();
    STAMP(13);
    if (tid < 32) st64(wsr, WS_FLAG + tid * 2048 + 1024 + (ch0 >> 2) * 8, tag);
    return tag;
  }
}

DI void out_phase(lptr lds, char* ws, u16* __restrict__ out, int rt, int ct, u32x2 tag STAMP_PARM) {
  const int tid = threadIdx.x, lane = tid & 63;
  const int wv = __builtin_amdgcn_readfirstlane(tid >> 6), wr = wv & 1, wc = (wv >> 1) & 1, par = wv >> 2;
  const u32x4 wsl = mk_rsrc(ws, WS_USED);
  const int h = lane >> 5, t = lane & 31, trow = rt * 64 + sig(wr, t);
  const unsigned fo = WS_FLAG + rt * 2048 + 8 * (lane < 16 ? wv * 16 + lane : lane < 18 ? 128 + ct * 16 + 2 * wv + (lane - 16)
                                     : lane == 18 ? ((rt & 1) * 2 + wr) * 16 + (rt >> 1) : wv * 16);
  const unsigned bo = WS_BT + (ct * 64 + wv * 8 + (lane >> 3)) * 4096 + rt * 128 + (((lane & 7) ^ (((lane >> 4) & 1) << 2)) << 4);
  bool b_done = false;
#pragma unroll 1
  for (unsigned spin = 0; spin < SPIN_CAP; ++spin) {
    const u32x2 f = poll64(wsl, fo);
    const u64 bad = __ballot(f.x != tag.x || f.y != tag.y);
    b_done = (bad & 0x30000) == 0;
    if ((bad & ~0x30000ull) == 0) break;
    __builtin_amdgcn_s_sleep(4);
  }
  STAMP(9);
#pragma unroll
  for (int i = 0; i < 2; ++i) dma16c(wsl, lane * 16, WS_KVZ + wv * 2176 + i * 1024, lds + L_KVZ + wv * 2176 + i * 1024);
  if (lane < 8) dma16c(wsl, lane * 16, WS_KVZ + wv * 2176 + 2048, lds + L_KVZ + wv * 2176 + 2048);
#pragma unroll
  for (int i = 0; i < 8; ++i)
    dma16c(wsl, lane * 16, WS_PQF + rt * 65536 + (i & 1) * 32768 + wv * 4096 + (i >> 1) * 1024, lds + L_PQ + (i & 1) * 32768 + wv * 4096 + (i >> 1) * 1024);
  unsigned gate = ld32c(wsl, WS_G + trow * 4);
  if (b_done) dma16c(wsl, bo, 0, lds + L_BT + wv * 1024);
  const int gi = lane >> 4, blk = gi & 1, q = (lane & 15) >> 2, p4 = lane & 3;
  const lptr kvb = lds + L_KVZ + (t < 16 ? t : 16) * 32 + h * 16 + par * 1088;
  const lptr pqb = lds + L_PQ + wr * 32768 + lane * 16 + par * 2048;
  const lptr wob = lds + L_WO + (4 * h + q) * 128 + (((4 * wc + 2 * blk + (p4 >> 1)) ^ ((q >> 1) << 2)) << 4) + (p4 & 1) * 8 + par * 4096;
  f32x16 acc = {};
  f32x16 nd[2][2];
  bf16x8 wf[2][2], kf[2][2], pf[2][2];
  const bool keeper = blockIdx.x == 0 && wv == 0;
  const unsigned cfo = keeper ? WS_FLAG + rt * 2048 + 1024 + lane * 16 : 0xffffffe0u;
  u32x4 cf;
#pragma unroll
  for (int g = 0; g < 10; ++g) {
    if (g < 8) {
      if (wv == g) WAIT_VM(0);
      bar();
      if (g == 0) STAMP(10);
      SB;
    }
#pragma unroll
    for (int i = 0; i < 2; ++i) {
      if (g >= 1 && g < 9) { const f32x16 zero = {}; nd[(g - 1) & 1][i] = mfma32(kf[(g - 1) & 1][i], pf[(g - 1) & 1][i], zero); SB; }
      if (g < 8) { kf[g & 1][i] = lds_b128(kvb + g * 2176 + i * 544); pf[g & 1][i] = lds_b128(pqb + g * 4096 + i * 1024); SB; }
      if (g >= 2) {
        f32x16& v = nd[g & 1][i];
        PIN(v);
        const float r = __builtin_amdgcn_rcpf(v[8] + 1e-6f);
        float o[8];
#pragma unroll
        for (int e = 0; e < 8; ++e) { o[e] = v[e] * r; PIN(o[e]); }
        const u32x4 p = {pk_bf16(o[0], o[1]), pk_bf16(o[2], o[3]), pk_bf16(o[4], o[5]), pk_bf16(o[6], o[7])};
        acc = mfma32(wf[g & 1][i], __builtin_bit_cast(bf16x8, p), acc);
        SB;
      }
      if (g >= 1 && g < 9) { wf[(g - 1) & 1][i] = lds_tr2(wob + (g - 1) * 8192 + i * 2048, wob + (g - 1) * 8192 + i * 2048 + 1024); SB; }
    }
    if (g == 7) { cf = ld128c(wsl, cfo); SB; }
  }
  PIN(acc);
  STAMP(11);
  if (!b_done) {
#pragma unroll 1
    for (unsigned spin = 0; spin < SPIN_CAP; ++spin) {
      const u32x2 f = poll64(wsl, fo);
      if (__ballot(f.x != tag.x || f.y != tag.y) == 0) break;
      __builtin_amdgcn_s_sleep(4);
    }
    dma16c(wsl, bo, 0, lds + L_BT + wv * 1024);
  }
  {
    LAS f32x4* xw = (LAS f32x4*)(lds + L_PQ + wv * 2048) + lane;
    const LAS f32x4* xr = (const LAS f32x4*)(lds + L_PQ + (wv ^ 4) * 2048) + lane;
    f32x4 keep[2];
#pragma unroll
    for (int G = 0; G < 2; ++G) {
      const f32x4 lo = {acc[4 * G], acc[4 * G + 1], acc[4 * G + 2], acc[4 * G + 3]};
      const f32x4 hi = {acc[8 + 4 * G], acc[9 + 4 * G], acc[10 + 4 * G], acc[11 + 4 * G]};
      if (par) { xw[64 * G] = lo; keep[G] = hi; } else { xw[64 * G] = hi; keep[G] = lo; }
    }
    const lptr bb = lds + L_BT + (wc * 32 + par * 16 + 4 * h + q) * 128 + (((4 * wr + 2 * blk + (p4 >> 1)) ^ ((q >> 1) << 2)) << 4) + (p4 & 1) * 8;
    lgkm0();
    WAIT_VM(0);
    PIN(gate); PIN(cf);
    bar();
    const u32x2 bv0 = __builtin_bit_cast(u32x2, lds_tr(bb)), bv1 = __builtin_bit_cast(u32x2, lds_tr(bb + 1024));
    const float gt = __uint_as_float(gate);
    u32x2 o[2];
#pragma unroll
    for (int G = 0; G < 2; ++G) {
      const f32x4 a = keep[G] + xr[64 * G];
      const unsigned b0 = G ? bv1.x : bv0.x, b1 = G ? bv1.y : bv0.y;
      const float bf[4] = {bf2f(b0), bfhi(b0), bf2f(b1), bfhi(b1)};
      float r[4];
#pragma unroll
      for (int e = 0; e < 4; ++e) r[e] = bf[e] + gt * (a[e] - bf[e]);
      o[G] = u32x2{pk_bf16(r[0], r[1]), pk_bf16(r[2], r[3])};
    }
    char* dst = (char*)out + (unsigned)(trow * 1024 + ct * 128 + wc * 64 + par * 32 + h * 16);
    auto sx = __builtin_amdgcn_permlane32_swap(o[0].x, o[1].x, false, false);
    auto sy = __builtin_amdgcn_permlane32_swap(o[0].y, o[1].y, false, false);
    const u32x4 v = {sx[0], sy[0], sx[1], sy[1]};
    *(u32x4*)dst = v;
  }
  if (keeper) {
#pragma unroll 1
    for (unsigned spin = 0; spin < SPIN_CAP; ++spin) {
      if (__all(cf.x == tag.x && cf.y == tag.y && cf.z == tag.x && cf.w == tag.y)) break;
      __builtin_amdgcn_s_sleep(4);
      asm volatile("buffer_load_dwordx4 %0, %1, %2, 0 offen sc1\n\ts_waitcnt vmcnt(0)" : "=&v"(cf) : "v"(cfo), "s"(wsl) : "memory");
    }
    if (lane == 0) st64(make_rsrc(ws, WS_USED), WS_EPOCH, tag);
  }
}

__global__ void __launch_bounds__(512, 2) fused(const u16* __restrict__ x, const u16* __restrict__ wqkv, const u16* __restrict__ gw,
                                                const u16* __restrict__ lar, const u16* __restrict__ aim, const u16* __restrict__ cre,
                                                const u16* __restrict__ cim, const u16* __restrict__ ldt, const u16* __restrict__ dsk,
                                                const u16* __restrict__ wo, char* ws, u16* __restrict__ out
) {
  STAMP_DECL;
  extern __shared__ __attribute__((aligned(16))) char lds_[];
  const lptr lds = (lptr)lds_;
  const int b = blockIdx.x, xcd = b & 7, idx = b >> 3, tid = threadIdx.x;
  CODE_PREFETCH("fused", lds + L_DUMP);
  const int wv = __builtin_amdgcn_readfirstlane(tid >> 6);
  if ((tid & 63) < 2) dma4(mk_rsrc(ws, WS_USED), WS_EPOCH + (tid & 63) * 4, lds + L_EPOCH + wv * 8);
  const int rt = (xcd >> 1) * 8 + (idx & 7), ct = (xcd & 1) * 4 + (idx >> 3);
  const Out o = {mk_rsrc(wo, 512 * 1024), (unsigned)(ct * 128 + (tid >> 3) * 1024 + (((tid & 7) ^ (((tid >> 4) & 1) << 2)) << 4)),
                 wv, lds};
  u32x2 tag;
  if (idx < 16) {
    tag = conv_role(lds, x, lar, aim, cre, cim, ldt, dsk, ws, xcd * 64 + idx * 4, idx, o STAMP_ARG);
  } else {
    const int rl = idx & 3, cl = (idx >> 2) & 3;
    tag = gemm_role(lds, x, wqkv, gw, ws, (xcd >> 1) * 4 + rl, (xcd & 1) * 4 + cl, 2 * ((rl + cl) & 3), o STAMP_ARG);
  }
  out_phase(lds, ws, out, rt, ct, tag STAMP_ARG);
  STAMP(12);
  STAMP_FLUSH;
  CODE_END("fused");
}

#define STARG(o)

extern "C" void kernel_launch(void* const* d_in, const int* in_sizes, int n_in, void* d_out, int out_size, void* d_ws,
                              size_t ws_size, hipStream_t stream) {
  const u16* x = (const u16*)d_in[0];
  const u16* wqkv = (const u16*)d_in[1];
  const u16* wo = (const u16*)d_in[2];
  const u16* lar = (const u16*)d_in[4];
  const u16* aim = (const u16*)d_in[5];
  const u16* cre = (const u16*)d_in[6];
  const u16* cim = (const u16*)d_in[7];
  const u16* ldt = (const u16*)d_in[8];
  const u16* dsk = (const u16*)d_in[9];
  const u16* gw = (const u16*)d_in[10];
  char* ws = (char*)d_ws;
  (void)hipFuncSetAttribute((const void*)fused, hipFuncAttributeMaxDynamicSharedMemorySize, 163840);
  fused<<<256, 512, 163840, stream>>>(x, wqkv, gw, lar, aim, cre, cim, ldt, dsk, wo, ws, (u16*)d_out STARG(0));
}
```

```cpp
#include <hip/hip_runtime.h>
#include <stdint.h>

typedef unsigned short u16;
typedef unsigned long long u64;
typedef __attribute__((ext_vector_type(8))) __bf16 bf16x8;
typedef __attribute__((ext_vector_type(2))) __bf16 bf16x2;
typedef __attribute__((ext_vector_type(8))) _Float16 f16x8;
typedef __attribute__((ext_vector_type(2))) _Float16 f16x2;
typedef __attribute__((ext_vector_type(4))) short s16x4;
typedef __attribute__((ext_vector_type(16))) float f32x16;
typedef __attribute__((ext_vector_type(4))) float f32x4;
typedef __attribute__((ext_vector_type(2))) float f32x2;
typedef __attribute__((ext_vector_type(4))) unsigned u32x4;
typedef __attribute__((ext_vector_type(2))) unsigned u32x2;
#define LAS __attribute__((address_space(3)))
#define CAS __attribute__((address_space(4)))
#define DI __device__ __forceinline__

constexpr unsigned WS_PQF = 0;
constexpr unsigned WS_BT  = 2u << 20;
constexpr unsigned WS_KVP = 4u << 20;
constexpr unsigned WS_KVZ = 6u << 20;
constexpr unsigned WS_G   = (6u << 20) + 65536;
constexpr unsigned WS_FLAG = (6u << 20) + 131072;
constexpr unsigned WS_EPOCH = WS_FLAG + 65536;
constexpr unsigned WS_PF = WS_FLAG + 131072;
constexpr unsigned WS_USED = 7u << 20;
constexpr unsigned SPIN_CAP = 200000;

constexpr float LOG2E = 1.4426950408889634f;

#define STAMP_DECL
#define STAMP_PARM
#define STAMP_ARG
#define STAMP(i)
#define STAMP_VAL(i, v)
#define STAMP_FLUSH
#define SB __builtin_amdgcn_sched_barrier(0)
#define PIN(x) asm volatile("" : "+v"(x))
#define PINS(x) asm volatile("" : "+s"(x))

typedef LAS char* lptr;
DI float bf2f(unsigned v) { return __uint_as_float(v << 16); }
DI float bfhi(unsigned v) { return __uint_as_float(v & 0xffff0000u); }
DI unsigned pk_bf16(float a, float b) { f32x2 v = {a, b}; return __builtin_bit_cast(unsigned, __builtin_convertvector(v, bf16x2)); }
DI float rbf(float a) { return __uint_as_float(pk_bf16(a, a) << 16); }
DI f16x2 pk_f16(f32x2 v) { return __builtin_convertvector(v, f16x2); }
DI f32x2 cmul(f32x2 a, f32x2 b) { f32x2 t = a.xx * b; f32x2 bs = {-b.y, b.x}; return __builtin_elementwise_fma(a.yy, bs, t); }
DI void cmul2(f32x2& r0, f32x2 a0, f32x2 b0, f32x2& r1, f32x2 a1, f32x2 b1) {
  f32x2 t0, t1;
  asm("v_pk_mul_f32 %0, %2, %3 op_sel_hi:[0,1]\n\tv_pk_mul_f32 %1, %4, %5 op_sel_hi:[0,1]\n\t"
      "v_pk_fma_f32 %0, %2, %3, %0 op_sel:[1,1,0] op_sel_hi:[1,0,1] neg_lo:[0,1,0]\n\t"
      "v_pk_fma_f32 %1, %4, %5, %1 op_sel:[1,1,0] op_sel_hi:[1,0,1] neg_lo:[0,1,0]"
      : "=&v"(t0), "=&v"(t1) : "v"(a0), "v"(b0), "v"(a1), "v"(b1));
  r0 = t0; r1 = t1;
}
DI f16x2 pk_f16c(f32x2 v) { const f32x2 c = {v.x, -v.y}; return __builtin_convertvector(c, f16x2); }
DI f16x2 cmulh(f16x2 a, f16x2 b) { f16x2 t = a.xx * b; f16x2 bs = {-b.y, b.x}; return __builtin_elementwise_fma(a.yy, bs, t); }
DI u32x4 mk_rsrc(const void* p, unsigned bytes) { const u64 a = (u64)p; return u32x4{(unsigned)a, (unsigned)(a >> 32), bytes, 0x00020000u}; }
DI void dma16(u32x4 rs, unsigned voff, unsigned soff, lptr l) {
  asm volatile("s_mov_b32 m0, %3\n\ts_nop 0\n\tbuffer_load_dwordx4 %0, %1, %2 offen lds"
               :: "v"(voff), "s"(rs), "s"(soff), "s"((unsigned)(size_t)l) : "memory");
}
DI void dma16c(u32x4 rs, unsigned voff, unsigned soff, lptr l) {
  asm volatile("s_mov_b32 m0, %3\n\ts_nop 0\n\tbuffer_load_dwordx4 %0, %1, %2 offen sc1 lds"
               :: "v"(voff), "s"(rs), "s"(soff), "s"((unsigned)(size_t)l) : "memory");
}
DI u32x4 ld128c(u32x4 rs, unsigned voff) { u32x4 v; asm volatile("buffer_load_dwordx4 %0, %1, %2, 0 offen sc1" : "=v"(v) : "v"(voff), "s"(rs) : "memory"); return v; }
DI u32x2 poll64(u32x4 rs, unsigned voff) {
  u32x2 v; asm volatile("buffer_load_dwordx2 %0, %1, %2, 0 offen sc1\n\ts_waitcnt vmcnt(0)" : "=&v"(v) : "v"(voff), "s"(rs) : "memory"); return v;
}
DI u32x2 ld64c(u32x4 rs, unsigned voff) { u32x2 v; asm volatile("buffer_load_dwordx2 %0, %1, %2, 0 offen sc1" : "=v"(v) : "v"(voff), "s"(rs) : "memory"); return v; }
DI unsigned ld32c(u32x4 rs, unsigned voff) { unsigned v; asm volatile("buffer_load_dword %0, %1, %2, 0 offen sc1" : "=v"(v) : "v"(voff), "s"(rs) : "memory"); return v; }
DI int sig(int w, int n) { return 32 * (n >> 4) + ((n >> 3) & 1) + 16 * w + 8 * ((n >> 2) & 1) + 2 * (n & 3); }
DI bf16x8 lds_b128(lptr p) { return *(const LAS bf16x8*)p; }
DI s16x4 lds_tr(lptr p) { return __builtin_amdgcn_ds_read_tr16_b64_v4i16((LAS s16x4*)p); }
DI bf16x8 lds_tr2(lptr p0, lptr p1) { s16x4 a = lds_tr(p0), b = lds_tr(p1); return __builtin_bit_cast(bf16x8, __builtin_shufflevector(a, b, 0, 1, 2, 3, 4, 5, 6, 7)); }
DI f32x16 mfma32(bf16x8 a, bf16x8 b, f32x16 c) { return __builtin_amdgcn_mfma_f32_32x32x16_bf16(a, b, c, 0, 0, 0); }
DI f32x4 mfma16(bf16x8 a, bf16x8 b, f32x4 c) { return __builtin_amdgcn_mfma_f32_16x16x32_bf16(a, b, c, 0, 0, 0); }
DI f32x4 mfma16h(f16x8 a, f16x8 b, f32x4 c) { return __builtin_amdgcn_mfma_f32_16x16x32_f16(a, b, c, 0, 0, 0); }
DI void bar() { asm volatile("" ::: "memory"); __builtin_amdgcn_s_barrier(); asm volatile("" ::: "memory"); }
DI void lgkm0() { asm volatile("s_waitcnt lgkmcnt(0)" ::: "memory"); }
#define WAIT_VM(n) asm volatile("s_waitcnt vmcnt(" #n ")" ::: "memory")
DI float phi(float tp) {
  const float e = __builtin_amdgcn_fmed3f(__builtin_amdgcn_exp2f(tp * LOG2E), 0.f, 1.f);
  const int ti = __float_as_int(tp);
  return __int_as_float(ti > 0x358637bd ? ti : 0x358637bd) + e;
}
DI bf16x8 pack8(const f32x16& a, int o) {
  u32x4 p = {pk_bf16(a[o], a[o + 1]), pk_bf16(a[o + 2], a[o + 3]), pk_bf16(a[o + 4], a[o + 5]), pk_bf16(a[o + 6], a[o + 7])};
  return __builtin_bit_cast(bf16x8, p);
}
template <int CTRL, int RM = 0xf> DI int dppi(int v) { return __builtin_amdgcn_update_dpp(0, v, CTRL, RM, 0xf, true); }
template <int CTRL, int RM = 0xf> DI float dppf(float v) { return __int_as_float(dppi<CTRL, RM>(__float_as_int(v))); }
DI unsigned umax(unsigned a, unsigned b) { return a > b ? a : b; }
DI unsigned umin(unsigned a, unsigned b) { return a < b ? a : b; }
DI unsigned umax4(unsigned m, u32x4 v) { return umax(umax(umax(m, v.x), v.y), umax(v.z, v.w)); }
DI unsigned wave_umax(unsigned v) {
  v = umax(v, dppi<0x111>(v)); v = umax(v, dppi<0x112>(v)); v = umax(v, dppi<0x114>(v)); v = umax(v, dppi<0x118>(v));
  v = umax(v, dppi<0x142, 0xa>(v)); v = umax(v, dppi<0x143, 0xc>(v));
  return __builtin_amdgcn_readlane(v, 63);
}
DI float wave_sum(float v) {
  v += dppf<0x111>(v); v += dppf<0x112>(v); v += dppf<0x114>(v); v += dppf<0x118>(v);
  v += dppf<0x142, 0xa>(v); v += dppf<0x143, 0xc>(v);
  return __int_as_float(__builtin_amdgcn_readlane(__float_as_int(v), 63));
}
DI void dma4(u32x4 rs, unsigned voff, lptr l) {
  asm volatile("s_mov_b32 m0, %2\n\ts_nop 0\n\tbuffer_load_dword %0, %1, 0 offen sc1 lds" :: "v"(voff), "s"(rs), "s"((unsigned)(size_t)l) : "memory");
}
#define CODE_PREFETCH(NAME, dump, off) do { u64 pc_; unsigned len_; \
  asm volatile("s_getpc_b64 %0\n.L" NAME "_beg:\n\ts_mov_b32 %1, .L" NAME "_end-.L" NAME "_beg" : "=s"(pc_), "=s"(len_)); \
  asm volatile("s_mov_b32 m0, %2\n\ts_nop 0\n\tbuffer_load_dword %0, %1, 0 offen lds" \
               :: "v"(off), "s"(mk_rsrc((const void*)pc_, len_)), "s"((unsigned)(size_t)(dump)) : "memory"); } while (0)
#define CODE_END(NAME) asm volatile(".L" NAME "_end:")
typedef __amdgpu_buffer_rsrc_t rsrc_t;
DI rsrc_t make_rsrc(void* p, unsigned bytes) { return __builtin_amdgcn_make_buffer_rsrc(p, 0, bytes, 0x00020000); }
constexpr int WT = 16;
DI void st128(rsrc_t r, unsigned off, u32x4 v) { __builtin_amdgcn_raw_buffer_store_b128(v, r, off, 0, WT); }
DI void st64(rsrc_t r, unsigned off, u32x2 v) { __builtin_amdgcn_raw_buffer_store_b64(v, r, off, 0, WT); }
DI void st32(rsrc_t r, unsigned off, unsigned v) { __builtin_amdgcn_raw_buffer_store_b32(v, r, off, 0, WT); }

constexpr int L_PQ = 0, L_WO = 66560, L_KVZ = L_WO + 65536, L_BT = L_KVZ + 17408;
struct Out { u32x4 wor; unsigned woo; int wv; lptr lds; };
constexpr unsigned EMPTY = 0xffffffffu;
DI void wo_issue(const Out& o, int gr) { dma16(o.wor, o.woo, gr * 65536, o.lds + L_WO + o.wv * 1024 + gr * 8192); }
constexpr int L_DUMP = 163840 - 512, L_EPOCH = 163840 - 256;
DI u32x2 next_tag(lptr lds, int wv) {
  const u32x2 e = *(const volatile LAS u32x2*)(lds + L_EPOCH + wv * 8);
  const u64 t = (((u64)(unsigned)__builtin_amdgcn_readfirstlane(e.y) << 32) | (unsigned)__builtin_amdgcn_readfirstlane(e.x)) + 1;
  return u32x2{(unsigned)t, (unsigned)(t >> 32)};
}

constexpr int SLOT = 32768, NSLOT = 4;
constexpr int GA_EX = L_KVZ;
constexpr int GA_GW = GA_EX + 8 * 544 * 4;

DI u32x2 gemm_role(lptr lds, const u16* __restrict__ x, const u16* __restrict__ w, const u16* __restrict__ gw, char* ws,
                   int rb, int cg, int rot, const Out& wo STAMP_PARM) {
  const int tid = threadIdx.x, lane = tid & 63;
  const int wv = __builtin_amdgcn_readfirstlane(tid >> 6), rg = wv & 3, hp = wv >> 2;
  const u32x4 xr = mk_rsrc(x, 2048 * 1024), wr_ = mk_rsrc(w, 512 * 3072);
  const unsigned xrow = tid >> 3, xo = rb * 131072 + xrow * 1024 + (((tid & 7) ^ ((xrow >> 1) & 7)) << 4);
  const unsigned wrow = tid >> 4, wp = (tid & 15) ^ ((wrow & 3) << 2);
  const unsigned wo_ = cg * 128 + wrow * 3072 + 1024 + (wp >> 3) * 1024 + ((wp & 7) << 4);
  const unsigned qo = cg * 128 + xrow * 3072 + (((tid & 7) ^ (((xrow >> 1) & 1) << 2)) << 4);
  const lptr lw = lds + wv * 1024;
  auto issue = [&](int c, int i) {
    const lptr slot = lw + (c % NSLOT) * SLOT;
    if (c < 8) {
      const unsigned kc = (c + rot) & 7;
      if (i == 0) dma16(xr, xo, kc * 128, slot);
      if (i == 1) dma16(wr_, wo_, kc * (64 * 3072), slot + 16384);
      if (i == 2) dma16(xr, xo, kc * 128 + 65536, slot + 8192);
      if (i == 3) dma16(wr_, wo_, kc * (64 * 3072) + 32 * 3072, slot + 24576);
    } else {
      dma16(wr_, qo, (((c - 8) * 4 + i + rot) & 7) * (64 * 3072), slot + i * 8192);
    }
  };
  STAMP(0);
  const int gsel = hp == 0 ? ((cg - rot) & 7) : -1;
  if (hp == 0 && lane < 8) dma16(mk_rsrc(gw, 1024), lane * 16, cg * 128, lds + GA_GW + wv * 128);
#pragma unroll
  for (int c = 0; c < 3; ++c) {
#pragma unroll
    for (int i = 0; i < 4; ++i) issue(c, i);
    if (c < 2) bar();
  }
  const rsrc_t wsr = make_rsrc(ws, WS_USED);
  const unsigned pqo = WS_PQF + (((rb * 4 + rg) * 32 + cg * 4 + 2 * hp) * 64 + lane) * 16;
  const int h = lane >> 5, row = (rg >> 1) * 64 + sig(rg & 1, lane & 31);
  const int gi = lane >> 4, blk = gi & 1, q = (lane & 15) >> 2, p4 = lane & 3;
  lptr ap[4], bp[2];
#pragma unroll
  for (int s = 0; s < 4; ++s) ap[s] = lds + row * 128 + (((2 * s + h) ^ ((row >> 1) & 7)) << 4);
#pragma unroll
  for (int ct = 0; ct < 2; ++ct) bp[ct] = lds + 16384 + (8 * h + q) * 256 + (((4 * (2 * ct + hp) + 2 * blk + (p4 >> 1)) ^ (q << 2)) << 4) + (p4 & 1) * 8;
  f32x16 ak, av;
  float g0 = 0.f;
#pragma unroll
  for (int i = 0; i < 16; ++i) { ak[i] = 1e-6f; av[i] = 0.f; }
  bf16x8 xf[32], Bf[3][2];
  u32x2 tag;
  auto gate_steps = [&](int c) {
    if (gsel == c) {
      const lptr gp = lds + GA_GW + wv * 128 + h * 16;
      f32x16 ag = {};
#pragma unroll
      for (int i = 0; i < 4; ++i) ag = mfma32(lds_b128(gp + i * 32), xf[4 * c + i], ag);
      g0 = ag[0];
    }
    PIN(g0);
  };
  lptr qp = lds + (8 * h + q) * 128 + (((4 * hp + 2 * blk + (p4 >> 1)) ^ ((q >> 1) << 2)) << 4) + (p4 & 1) * 8;
  const int dp = lane & 31, e_ = lane & 15;
  lptr exw = lds + GA_EX + rg * 4352 + (2 * hp + (dp >> 4)) * 1024 + e_ * 64 + (((2 * h) ^ (e_ >> 2)) << 4);
  lptr ezw = lds + GA_EX + rg * 4352 + 4096 + (2 * hp + (dp >> 4)) * 64 + (((e_ >> 2) & 1) * 8 + (e_ & 3) + 4 * (e_ >> 3)) * 4;
  lptr exr = lds + GA_EX + (tid < 272 ? tid : 0) * 16;
  unsigned kpo = tid < 272 ? WS_KVP + (rb * 8 + cg) * 8704 + tid * 16 : 0xffff0000u;
  PIN(qp); PIN(exw); PIN(ezw); PIN(exr); PIN(kpo);
  f32x16 aq;
  STAMP(1);
#pragma unroll
  for (int n = 0; n < 32; ++n) {
    const int c = n >> 2, s = n & 3, so = (c & 1) * SLOT;
    if (s == 0) {
      if (c > 0) {
        lgkm0();
        if ((c & 1) == 0) {
          const int dlt = (c == 4) ? -2 * SLOT : 2 * SLOT;
#pragma unroll
          for (int i = 0; i < 4; ++i) { ap[i] += dlt; PIN(ap[i]); }
#pragma unroll
          for (int i = 0; i < 2; ++i) { bp[i] += dlt; PIN(bp[i]); }
        }
      }
      WAIT_VM(8);
      bar();
      if (c == 0) STAMP(2);
      SB;
    }
    if (n >= 2) { ak = mfma32(xf[n - 2], Bf[(n - 2) % 3][0], ak); SB; }
    xf[n] = lds_b128(ap[s] + so); Bf[n % 3][0] = lds_tr2(bp[0] + so + s * 4096, bp[0] + so + s * 4096 + 1024); SB;
    if (n >= 2) { av = mfma32(xf[n - 2], Bf[(n - 2) % 3][1], av); SB; }
    Bf[n % 3][1] = lds_tr2(bp[1] + so + s * 4096, bp[1] + so + s * 4096 + 1024); SB;
    if (c + 3 < 10) { issue(c + 3, s); SB; } else if (s < 3) { wo_issue(wo, s); SB; }
    if (n == 14) { tag = next_tag(lds, wv); SB; }
    if (n == 30) {
#pragma unroll
      for (int i = 0; i < 16; ++i) aq[i] = 1e-6f;
      PIN(aq); SB;
    }
    if (n > 0 && s == 0) { gate_steps(c - 1); SB; }
  }
  STAMP(3);
  f32x16 kv = {}, z = {};
  constexpr int PD = 5;
  bf16x8 Aq[PD + 1], ones;
#pragma unroll
  for (int i = 0; i < 8; ++i) ones[i] = (__bf16)1.0f;
  u32x4 pk[2], vv[2];
  auto rdq = [&](int m) { Aq[m % (PD + 1)] = lds_tr2(qp + m * 2048, qp + m * 2048 + 512); };
#pragma unroll
  for (int m = -2; m < 32; ++m) {
    if (m == -2) { lgkm0(); WAIT_VM(7); bar(); SB; rdq(0); rdq(1); SB; }
    if (m == 11) { lgkm0(); WAIT_VM(6); STAMP(4); bar(); SB; }
    if (m < 0) { ak = mfma32(xf[32 + m], Bf[(32 + m) % 3][0], ak); av = mfma32(xf[32 + m], Bf[(32 + m) % 3][1], av); SB; }
    else { aq = mfma32(Aq[m % (PD + 1)], xf[m], aq); SB; }
    if (m == -2) { rdq(2); SB; }
    if (m + PD < 32) { rdq(m + PD); SB; }
    if (m == 0) { vv[0] = __builtin_bit_cast(u32x4, pack8(av, 0)); vv[1] = __builtin_bit_cast(u32x4, pack8(av, 8)); PIN(vv[0]); PIN(vv[1]); SB; }
    if (m >= 1 && m < 9) {
      const int j = m - 1;
      unsigned p = pk_bf16(phi(ak[2 * j]), phi(ak[2 * j + 1]));
      PIN(p);
      pk[j >> 2][j & 3] = p;
      SB;
      if ((j & 3) == 3) {
        kv = mfma32(__builtin_bit_cast(bf16x8, pk[j >> 2]), __builtin_bit_cast(bf16x8, vv[j >> 2]), kv);
        z = mfma32(ones, __builtin_bit_cast(bf16x8, pk[j >> 2]), z);
        SB;
      }
      if (j == 1 || j == 4 || j == 6) { wo_issue(wo, 3 + j / 2 - (j == 1 ? 0 : 1)); SB; }
    }
    if (m == 10) {
      float sel[8];
#pragma unroll
      for (int i = 0; i < 8; ++i) { float a = kv[i], b = kv[8 + i]; PIN(a); PIN(b); sel[i] = dp < 16 ? a : b; }
      *(LAS f32x4*)exw = f32x4{sel[0], sel[1], sel[2], sel[3]};
      *(LAS f32x4*)((lptr)((unsigned)(size_t)exw ^ 16u)) = f32x4{sel[4], sel[5], sel[6], sel[7]};
      *(LAS float*)ezw = z[0];
      SB;
    }
    if (m == 11) {
      const LAS f32x4* ex = (const LAS f32x4*)exr;
      const u32x4 sum = __builtin_bit_cast(u32x4, (ex[0] + ex[272]) + (ex[544] + ex[816]));
      st128(wsr, kpo, u32x4{sum.x, tag.x, sum.y, tag.x});
      st128(wsr, kpo + 4352, u32x4{sum.z, tag.x, sum.w, tag.x});
      SB;
    }
    if (m == 13) { PIN(aq); gate_steps(7); SB; }
    if (m == 16 || m == 19) { wo_issue(wo, m == 16 ? 6 : 7); SB; }
  }
  PIN(aq);
  STAMP(5);
  {
    if (hp == 0) st32(wsr, h == 0 ? WS_G + cg * 8192 + (rb * 4 + rg) * 128 + lane * 4 : 0xfffffff0u, __float_as_uint(g0));
#pragma unroll
    for (int i = 0; i < 8; ++i) aq[i] = phi(aq[i]);
    st128(wsr, pqo, __builtin_bit_cast(u32x4, pack8(aq, 0)));
#pragma unroll
    for (int i = 8; i < 16; ++i) aq[i] = phi(aq[i]);
    st128(wsr, pqo + 1024, __builtin_bit_cast(u32x4, pack8(aq, 8)));
    STAMP(6);
    WAIT_VM(0);
  }
  STAMP(7);
  bar();
  STAMP(13);
  if (tid == 511) st64(wsr, WS_PF + (rb * 8 + cg) * 128, tag);
  return tag;
}

constexpr int SC_ROW = 2064;
constexpr int KC_CP = 4224, KC_CH = 2 * KC_CP;
constexpr int EX_BASE = 4 * KC_CH;
constexpr int U_BASE = 57 * SC_ROW, U_PAD = 224, U_CP = 4576, U_CH = 2 * U_CP;
constexpr int T_BASE = U_BASE + 4 * U_CH, T_CH = 1536;
static_assert(T_BASE + 4 * T_CH <= 163840 && U_BASE % 16 == 0, "lds");

DI f32x2 cexp_p(float l2, float rv) {
  const float m = __builtin_amdgcn_exp2f(l2), f = __builtin_amdgcn_fractf(rv);
  const f32x2 r = {m * __builtin_amdgcn_cosf(f), m * __builtin_amdgcn_sinf(f)};
  return r;
}
DI bf16x8 lds_2b64(lptr p) {
  const u32x2 a = *(const volatile LAS u32x2*)p, b = *(const volatile LAS u32x2*)(p + 8);
  const u32x4 v = {a.x, a.y, b.x, b.y};
  return __builtin_bit_cast(bf16x8, v);
}
DI unsigned perm(unsigned hi, unsigned lo, unsigned sel) { return __builtin_amdgcn_perm(hi, lo, sel); }
constexpr unsigned LL = 0x01000100, LH = 0x03020302, HL = 0x05040504, SWL = 0x01000302, SWH = 0x05040706, HL_LH = 0x03020504;
template <int E0, int E1, int E2, int E3>
DI void put4(unsigned base, f16x2 a, f16x2 b, f16x2 c, f16x2 d) {
  constexpr int O = E0 < 28 ? 0 : 28;
  static_assert((E1 < 28) == (E0 < 28) && (E2 < 28) == (E0 < 28) && (E3 < 28) == (E0 < 28), "one base");
  asm volatile("s_mov_b32 m0, %4\n\ts_nop 0\n\tds_write_addtid_b32 %0 offset:%5\n\tds_write_addtid_b32 %1 offset:%6\n\t"
               "ds_write_addtid_b32 %2 offset:%7\n\tds_write_addtid_b32 %3 offset:%8"
               :: "v"(a), "v"(b), "v"(c), "v"(d), "s"(base + O * SC_ROW), "i"((E0 - O) * SC_ROW), "i"((E1 - O) * SC_ROW),
                  "i"((E2 - O) * SC_ROW), "i"((E3 - O) * SC_ROW) : "memory");
}

DI u32x2 conv_role(lptr lds, const u16* __restrict__ x, const u16* __restrict__ lar, const u16* __restrict__ aim,
                   const u16* __restrict__ cre, const u16* __restrict__ cim, const u16* __restrict__ ldt,
                   const u16* __restrict__ dsk, char* ws, int ch0, int jrot, const Out& wo STAMP_PARM) {
  const int tid = threadIdx.x, lane = tid & 63;
  const int wv = __builtin_amdgcn_readfirstlane(tid >> 6), cl = wv & 3, half = wv >> 2;
  const int d = ch0 + cl;
  STAMP(0);
  const unsigned r_lar = ((const unsigned*)lar)[d * 64 + lane], r_aim = ((const unsigned*)aim)[d * 64 + lane];
  const unsigned r_cr = ((const unsigned*)cre)[d * 64 + lane], r_ci = ((const unsigned*)cim)[d * 64 + lane];
  const unsigned s_ldt = ((const CAS unsigned*)ldt)[d >> 1], s_dsk = ((const CAS unsigned*)dsk)[d >> 1];
  const int j = lane & 15, g = lane >> 4;
  const rsrc_t wsr = make_rsrc(ws, WS_USED);
  bar();
  const int cj = ch0 >> 2, cg = cj >> 4, rb = cj & 15;
  const unsigned kvo = wv == 4 && lane < 9 ? WS_KVZ + cj * 256 + lane * 16 : 0xffffffe0u;
  const int tt = (tid + jrot * 64) & 511;
  const char* xg = (const char*)x + ch0 * 2;
  const unsigned xoff = tt * 4096;
  u32x2 ur[4];
  ur[0] = *(const u32x2*)(xg + xoff); ur[1] = *(const u32x2*)(xg + xoff + 1024);
  u32x2 up;
  {
    const u32x4 zero = {0, 0, 0, 0};
    if (lane < 28) *(LAS u32x4*)(lds + U_BASE + cl * U_CH + half * U_CP + lane * 16) = zero;
  }
  const unsigned scb = (unsigned)(size_t)lds + cl * 512 + half * 256;
  const lptr fb = lds + cl * 512 + half * 256 + j * SC_ROW + g * 16;
  unsigned selA = lane < 32 ? LL : lane == 32 ? HL : SWH, selB = lane < 32 ? HL_LH : SWH, selC = lane < 32 ? LL : SWH, selE = lane < 32 ? LL : SWL;
  if (half == 0) { const bool odd = !(lane & 1); selA = odd ? LH : LL; selB = odd ? HL : LH; selC = odd ? HL : SWL; }
  PIN(selA); PIN(selB); PIN(selC); PIN(selE);
  const unsigned shl = half ? 0 : 16;
  const float f_ldt = (d & 1) ? bfhi(s_ldt) : bf2f(s_ldt), f_dsk = (d & 1) ? bfhi(s_dsk) : bf2f(s_dsk);
  const float dt = __int_as_float(__builtin_amdgcn_readfirstlane(__float_as_int(rbf(__expf(f_ldt)))));
  SB;
  const float l2 = -rbf(__builtin_amdgcn_exp2f(bfhi(r_lar << shl) * LOG2E)) * dt * LOG2E;
  const float rv = bfhi(r_aim << shl) * dt * 0.15915494309189535f;
  unsigned cm;
  {
    typedef __attribute__((ext_vector_type(2))) unsigned short u16x2;
    const u16x2 m2 = __builtin_elementwise_max(__builtin_bit_cast(u16x2, r_cr & 0x7fff7fffu), __builtin_bit_cast(u16x2, r_ci & 0x7fff7fffu));
    const unsigned m = __builtin_bit_cast(unsigned, m2);
    cm = umax(wave_umax(umax(m << 16, m)), 0x0da24260u);
  }
  const float cmax = __uint_as_float(cm), cinv = __builtin_amdgcn_rcpf(cmax);
  f32x4 kt[3];
  {
    f16x8 rf[4], t0[4];
    const f16x2 one = {(_Float16)1.f, (_Float16)0.f};
    const f32x2 E1 = cexp_p(l2, rv), E8 = cexp_p(l2 * 8.f, rv * 8.f), E128 = cexp_p(l2 * 128.f, rv * 128.f);
    f32x2 E2, E3, E4, E16, E24, E32, E64, E96, E256, E384, E512, H1, H2, H3, H4, dummy;
    const f32x2 H0 = {bfhi(r_cr << shl) * cinv, bfhi(r_ci << shl) * cinv};
    cmul2(E16, E8, E8, E256, E128, E128);
    cmul2(E32, E16, E16, E512, E256, E256);
    cmul2(E24, E16, E8, E2, E1, E1);
    cmul2(E64, E32, E32, H1, H0, E512);
    cmul2(E3, E2, E1, E4, E2, E2);
    cmul2(E96, E64, E32, H2, H1, E512);
    cmul2(E384, E256, E128, H3, H2, E512);
    {
      const f16x2 P1 = pk_f16c(E8), P2 = pk_f16c(E16), P3 = pk_f16c(E24), Q1 = pk_f16c(E32), Q2 = pk_f16c(E64), Q3 = pk_f16c(E96);
      put4<0, 1, 2, 3>(scb, one, P1, P2, P3);
      put4<4, 5, 6, 7>(scb, Q1, cmulh(Q1, P1), cmulh(Q1, P2), cmulh(Q1, P3));
      put4<8, 9, 10, 11>(scb, Q2, cmulh(Q2, P1), cmulh(Q2, P2), cmulh(Q2, P3));
      put4<12, 13, 14, 15>(scb, Q3, cmulh(Q3, P1), cmulh(Q3, P2), cmulh(Q3, P3));
    }
    SB;
    ur[2] = *(const u32x2*)(xg + xoff + 2048);
    SB;
    H4 = cmul(H3, E512); (void)dummy;
    const f16x2 e1 = pk_f16(E1), e2 = pk_f16(E2), e3 = pk_f16(E3), e4 = pk_f16(E4);
    const f16x2 e5 = cmulh(e4, e1), e6 = cmulh(e4, e2), e7 = cmulh(e4, e3);
    const f16x2 S1 = pk_f16(E128), S2 = pk_f16(E256), S3 = pk_f16(E384);
    const f16x2 h0 = pk_f16(H0), h1 = pk_f16(H1), h2 = pk_f16(H2), h3 = pk_f16(H3);
    {
      const f16x2 G1 = cmulh(h0, S1);
      put4<16, 17, 18, 19>(scb, h0, cmulh(h0, e1), cmulh(h0, e2), cmulh(h0, e3));
      put4<20, 21, 22, 23>(scb, cmulh(h0, e4), cmulh(h0, e5), cmulh(h0, e6), cmulh(h0, e7));
      put4<24, 25, 26, 27>(scb, G1, cmulh(G1, e1), cmulh(G1, e2), cmulh(G1, e3));
      put4<28, 29, 30, 31>(scb, cmulh(G1, e4), cmulh(G1, e5), cmulh(G1, e6), cmulh(G1, e7));
    }
#pragma unroll
    for (int s = 0; s < 4; ++s) { rf[s] = *(const LAS f16x8*)(fb + s * 64); t0[s] = *(const LAS f16x8*)(fb + 16 * SC_ROW + s * 64); }
    SB;
    ur[3] = *(const u32x2*)(xg + xoff + 3072);
    up = __builtin_amdgcn_raw_buffer_load_b64(make_rsrc((void*)x, 2048 * 1024), lane == 0 && tt > 0 ? ch0 * 2 + xoff - 1024 : 0xfffffff0u, 0, 0);
    SB;
    {
      const f16x2 G2 = cmulh(h0, S2), G3 = cmulh(h0, S3), G5 = cmulh(h1, S1), G6 = cmulh(h1, S2), G7 = cmulh(h1, S3);
      put4<32, 33, 34, 35>(scb, G2, cmulh(G2, e2), cmulh(G2, e4), cmulh(G2, e6));
      kt[0] = mfma16h(t0[0], rf[0], f32x4{0.f, 0.f, 0.f, 0.f});
      kt[0] = mfma16h(t0[1], rf[1], kt[0]);
      put4<36, 37, 38, 39>(scb, G3, cmulh(G3, e2), cmulh(G3, e4), cmulh(G3, e6));
      kt[0] = mfma16h(t0[2], rf[2], kt[0]);
      put4<40, 41, 42, 43>(scb, h1, cmulh(h1, e4), G5, cmulh(G5, e4));
      kt[0] = mfma16h(t0[3], rf[3], kt[0]);
      put4<44, 45, 46, 47>(scb, G6, cmulh(G6, e4), G7, cmulh(G7, e4));
    }
#pragma unroll
    for (int s = 0; s < 4; ++s) t0[s] = *(const LAS f16x8*)(fb + 32 * SC_ROW + s * 64);
    SB;
    put4<48, 49, 50, 51>(scb, h2, cmulh(h2, S1), cmulh(h2, S2), cmulh(h2, S3));
    put4<52, 53, 54, 55>(scb, h3, cmulh(h3, S1), cmulh(h3, S2), cmulh(h3, S3));
    *(LAS f16x2*)(lds + cl * 512 + half * 256 + lane * 4 + 56 * SC_ROW) = pk_f16(H4);
    f16x8 t2[4];
#pragma unroll
    for (int s = 0; s < 4; ++s) t2[s] = *(const LAS f16x8*)(fb + 48 * SC_ROW + s * 64);
    SB;
    kt[1] = f32x4{0.f, 0.f, 0.f, 0.f}; kt[2] = kt[1];
#pragma unroll
    for (int s = 0; s < 4; ++s) kt[1] = mfma16h(t0[s], rf[s], kt[1]);
#pragma unroll
    for (int s = 0; s < 4; ++s) kt[2] = mfma16h(t2[s], rf[s], kt[2]);
  }
  const lptr Tb = lds + T_BASE + cl * T_CH;
  {
    LAS f32x4* xw = (LAS f32x4*)(lds + cl * 512 + half * 256 + g * SC_ROW) + j;
    const LAS f32x4* xr = (const LAS f32x4*)(lds + cl * 512 + (half ^ 1) * 256 + g * SC_ROW) + j;
    if (half) xw[0] = kt[0]; else { xw[0] = kt[1]; xw[4 * SC_ROW / 16] = kt[2]; }
    const float ks = 2.f * dt * cmax;
    const lptr p1 = half ? Tb + (256 + 64 * g + (g < 2 ? 4 : 2) * j) * 2 : Tb + (128 * (g >> 1) + 4 * (g & 1) + 8 * j) * 2;
    lptr p1b = p1 + (g < 2 ? 4 : 64);
    PIN(p1b);
    lgkm0();
    STAMP(3);
    bar();
    if (half == 0) {
      f32x4 a0 = (kt[0] + xr[0]) * ks;
      if (lane == 0) a0[0] += f_dsk;
      *(LAS u32x2*)p1 = u32x2{pk_bf16(a0[0], a0[1]), pk_bf16(a0[2], a0[3])};
    } else {
      const f32x4 a1 = (kt[1] + xr[0]) * ks, a2 = (kt[2] + xr[4 * SC_ROW / 16]) * ks;
      *(LAS unsigned*)p1 = pk_bf16(a1[0], a1[1]);
      *(LAS unsigned*)p1b = pk_bf16(a1[2], a1[3]);
      const unsigned q0 = pk_bf16(a2[0], a2[1]), q1 = pk_bf16(a2[2], a2[3]);
      LAS u16* p2 = (LAS u16*)Tb + 512 + 64 * g + j;
      p2[0] = (u16)q0; p2[16] = (u16)(q0 >> 16); p2[32] = (u16)q1; p2[48] = (u16)(q1 >> 16);
    }
  }
  lgkm0();
  STAMP(4);
  bar();
  {
    const lptr kc = lds + cl * KC_CH + lane * 16;
    auto emit = [&](int Y0, unsigned P0, unsigned P1, unsigned P2, unsigned P3, unsigned P4) {
      *(LAS u32x4*)(kc + Y0 * 16) = u32x4{P0, P1, P2, P3};
      *(LAS u32x4*)(kc + KC_CP + Y0 * 16) = u32x4{P1, P2, P3, P4};
    };
    if (half == 0) {
      const LAS unsigned* p = (const LAS unsigned*)(Tb + ((575 - lane) >> 1) * 4);
      const unsigned D0 = p[32], D1 = p[33], F0 = p[0], F1 = p[1], Z = *(const LAS unsigned*)Tb;
      SB;
      const unsigned e1 = perm(D1, D0, selB), e0 = perm(D1, D0, selA), f1 = perm(F1, F0, selB), f0 = perm(F1, F0, selA);
      emit(0, e1, e1, perm(D1, D0, selC), e0, e0);
      emit(64, f1, f1, perm(F1, F0, selC), f0, f0);
      if (lane < 4) emit(256, lane == 0 ? perm(0, Z, SWL) : 0u, 0u, 0u, 0u, 0u);
    } else {
      const LAS unsigned* p = (const LAS unsigned*)(Tb + (255 - lane) * 4);
      const LAS unsigned* r = (const LAS unsigned*)(Tb + (lane < 32 ? 760 - 8 * lane : 1008 - 16 * lane));
      const unsigned F0 = p[0], F1 = p[1], D0 = r[0], D1 = r[1], D2 = r[2], D3 = r[3], D4 = r[4];
      SB;
      const unsigned e2 = perm(F1, F0, HL);
      emit(128, e2, e2, perm(F1, F0, LH), perm(F1, F0, SWL), perm(F1, F0, LL));
      emit(192, perm(D4, D2, selA), perm(lane < 32 ? D2 : D3, D1, selB), perm(D2, D1, selC), perm(D1, D0, selB), perm(D1, D0, selE));
    }
  }
  SB;
  {
    const u32x2 pv = {(unsigned)__builtin_amdgcn_update_dpp((int)up.x, (int)ur[3].x, 0x138, 0xf, 0xf, false),
                      (unsigned)__builtin_amdgcn_update_dpp((int)up.y, (int)ur[3].y, 0x138, 0xf, 0xf, false)};
    const lptr ub = lds + U_BASE + (U_PAD + 4 * tt) * 2;
#pragma unroll
    for (int c = 0; c < 4; ++c) {
      const unsigned sel = (c & 1) ? 0x07060302 : 0x05040100;
      const unsigned p = pv[c >> 1], r0 = ur[0][c >> 1], r1 = ur[1][c >> 1], r2 = ur[2][c >> 1], r3 = ur[3][c >> 1];
      *(LAS u32x2*)(ub + c * U_CH) = u32x2{perm(r1, r0, sel), perm(r3, r2, sel)};
      *(LAS u32x2*)(ub + c * U_CH + U_CP) = u32x2{perm(r0, p, sel), perm(r2, r1, sel)};
    }
  }
  const u32x2 tag = next_tag(lds, wv);
  lgkm0();
  STAMP(5);
  bar();
  const u32x4 wsl = mk_rsrc(ws, WS_USED);
  const int a_ = j;
  const lptr ab = lds + cl * KC_CH + ((a_ & 1) ? KC_CP + 4088 : 4096) - 8 * (a_ >> 1) + 16 * g - 3584 - 192 - half * 256;
  const lptr bb = lds + U_BASE + cl * U_CH + ((a_ & 1) ? 0 : U_CP) + (U_PAD + 32 * (a_ >> 1) + 8 * g) * 2 - 192 - half * 256;
  f32x4 acc[8];
#pragma unroll
  for (int i = 0; i < 8; ++i) acc[i] = f32x4{0.f, 0.f, 0.f, 0.f};
  LAS f32x4* exw = (LAS f32x4*)(lds + EX_BASE + wv * 4096) + lane;
  f32x4 keep[4];
  {
    u32x2 Al[2][8], Ah[2][8];
    bf16x8 B[2][8];
    auto rd = [&](int r, int k) {
      const int nb = r & 1, mu = k / 3, w = k % 3;
      if (w == 0) B[nb][mu] = lds_b128(bb + 512 * mu + 64 * (3 - r));
      if (w == 1) Al[nb][mu] = *(const volatile LAS u32x2*)(ab + 512 * (7 - mu) + 64 * (3 - r));
      if (w == 2) Ah[nb][mu] = *(const volatile LAS u32x2*)(ab + 512 * (7 - mu) + 64 * (3 - r) + 8);
    };
    auto AF = [&](int nb, int mu) { return __builtin_bit_cast(bf16x8, u32x4{Al[nb][mu].x, Al[nb][mu].y, Ah[nb][mu].x, Ah[nb][mu].y}); };
#pragma unroll
    for (int k = 0; k < 24; ++k) rd(0, k);
    SB;
#pragma unroll
    for (int r = 1; r < 4; ++r) {
      const int cb = (r - 1) & 1;
      int cnt = 0;

#pragma unroll
      for (int m = 0; m < 8; ++m)
#pragma unroll
        for (int n = 0; n <= m; ++n)
#pragma unroll
          for (int sw = 0; sw < 2; ++sw)
            if (m + n < 8 && (sw == 0 || n < m)) {
              const int mu = sw ? n : m, nu = sw ? m : n;
              acc[m + n] = mfma16(AF(cb, mu), B[cb][nu], acc[m + n]);
              SB;
              if (cnt % 3 != 2) rd(r, cnt - cnt / 3);
              else if (cnt == 14 || cnt == 29) wo_issue(wo, 2 * (r - 1) + (cnt == 29));
              else if ((cnt == 5 || cnt == 20) && r == 1) st128(wsr, WS_BT + ch0 * 4096 + (cnt == 5 ? 0 : 8192) + tid * 16, u32x4{EMPTY, EMPTY, EMPTY, EMPTY});
              else if (cnt == 2 && r == 1) st128(wsr, kvo, u32x4{EMPTY, EMPTY, EMPTY, EMPTY});
              else if (cnt == 2 && r == 3) { WAIT_VM(3); st64(wsr, lane == 0 ? WS_FLAG + (cj * 8 + wv) * 8 : 0xfffffff0u, tag); }
              SB;
              ++cnt;
            }
    }
    auto tiles = [&](int t0) {
#pragma unroll
      for (int mu = 0; mu < 8; ++mu)
#pragma unroll
        for (int n = 0; n < 8; ++n) if (mu + n >= t0 && mu + n < t0 + 4) acc[mu + n] = mfma16(AF(1, mu), B[1][n], acc[mu + n]);
    };
    if (half) {
      tiles(0); SB; tiles(4);
#pragma unroll
      for (int i = 0; i < 4; ++i) { exw[64 * i] = acc[i]; keep[i] = acc[4 + i]; }
    } else {
      tiles(4); SB; tiles(0);
#pragma unroll
      for (int i = 0; i < 4; ++i) { exw[64 * i] = acc[4 + i]; keep[i] = acc[i]; }
    }
  }
  STAMP(6);
  {
    const LAS f32x4* exr = (const LAS f32x4*)(lds + EX_BASE + (wv ^ 4) * 4096) + lane;
    const unsigned bto = WS_BT + d * 4096 + half * 2048 + (g & 1) * 512 + (a_ >> 2) * 128 + (g >> 1) * 64 + (a_ & 3) * 16;
    u32x4 bt[2], k[4];
    constexpr unsigned DONE = 0xffffffe0u;
    const unsigned src = WS_KVP + (j * 8 + cg) * 8704;
    const unsigned ko = src + (rb >> 2) * 1024 + ((rb & 3) * 4 + cl) * 64 + ((g ^ (rb & 3)) << 4);
    const bool zl = wv == 4 && g == 0;
    unsigned ka[4] = {half ? ko : DONE, half ? ko + 4352 : DONE, zl ? src + 4096 + rb * 16 : DONE, zl ? src + 4352 + 4096 + rb * 16 : DONE};
#pragma unroll
    for (int i = 0; i < 4; ++i) k[i] = ld128c(wsl, ka[i]);
    lgkm0();
    STAMP(7);
    bar();
#pragma unroll
    for (int i = 0; i < 4; i += 2) {
      const f32x4 v = keep[i] + exr[64 * i], w = keep[i + 1] + exr[64 * i + 64];
      const auto sx = __builtin_amdgcn_permlane16_swap(umin(pk_bf16(v[0], v[1]), EMPTY - 1), umin(pk_bf16(w[0], w[1]), EMPTY - 1), false, false);
      const auto sy = __builtin_amdgcn_permlane16_swap(umin(pk_bf16(v[2], v[3]), EMPTY - 1), umin(pk_bf16(w[2], w[3]), EMPTY - 1), false, false);
      bt[i >> 1] = u32x4{sx[0], sy[0], sx[1], sy[1]};
      PIN(bt[i >> 1]);
    }
    wo_issue(wo, 6); wo_issue(wo, 7);
    STAMP(2);
    unsigned spin = 0;
    WAIT_VM(2);
    PIN(k[0]); PIN(k[1]); PIN(k[2]); PIN(k[3]);
    const lptr slice = lds + T_BASE;
    if (half == 0) { st128(wsr, bto, bt[0]); st128(wsr, bto + 1024, bt[1]); }
    else {
      unsigned val[8] = {0, 0, 0, 0, 0, 0, 0, 0};
#pragma unroll 1
      for (; spin < SPIN_CAP; ++spin) {
#pragma unroll
        for (int i = 0; i < 4; ++i) {
          const bool got = k[i].y == tag.x && k[i].w == tag.x && ka[i] != DONE;
          val[2 * i] = got ? k[i].x : val[2 * i]; val[2 * i + 1] = got ? k[i].z : val[2 * i + 1];
          ka[i] = got ? DONE : ka[i];
        }
        if (__all((ka[0] & ka[1] & ka[2] & ka[3]) == DONE)) break;
        asm volatile("buffer_load_dwordx4 %0, %4, %8, 0 offen sc1\n\tbuffer_load_dwordx4 %1, %5, %8, 0 offen sc1\n\t"
                     "buffer_load_dwordx4 %2, %6, %8, 0 offen sc1\n\tbuffer_load_dwordx4 %3, %7, %8, 0 offen sc1\n\ts_waitcnt vmcnt(0)"
                     : "=&v"(k[0]), "=&v"(k[1]), "=&v"(k[2]), "=&v"(k[3])
                     : "v"(ka[0]), "v"(ka[1]), "v"(ka[2]), "v"(ka[3]), "s"(wsl) : "memory");
      }
      auto rowsum = [&](unsigned u) { float v = __uint_as_float(u); v += dppf<0x111>(v); v += dppf<0x112>(v); v += dppf<0x114>(v); v += dppf<0x118>(v); return v; };
      const u32x2 kvs = {umin(pk_bf16(rowsum(val[0]), rowsum(val[1])), EMPTY - 1), umin(pk_bf16(rowsum(val[2]), rowsum(val[3])), EMPTY - 1)};
      if (j == 15) *(LAS u32x2*)(slice + (cl * 4 + g) * 8) = kvs;
      if (wv == 4) {
        const u32x2 zs = {umin(pk_bf16(rowsum(val[4]), rowsum(val[5])), EMPTY - 1), umin(pk_bf16(rowsum(val[6]), rowsum(val[7])), EMPTY - 1)};
        if (lane == 15) *(LAS u32x4*)(slice + 128) = u32x4{zs.x, zs.y, 0, 0};
      }
    }
    lgkm0();
    bar();
    if (half) {
      if (wv == 4) st128(wsr, kvo, *(const LAS u32x4*)(slice + (lane < 9 ? lane : 0) * 16));
      st128(wsr, bto, bt[0]); st128(wsr, bto + 1024, bt[1]);
    }
    STAMP_VAL(3, st_[0] + spin);
    STAMP(13);
    return tag;
  }
}

DI void out_phase(lptr lds, char* ws, u16* __restrict__ out, int rt, int ct, u32x2 tag STAMP_PARM) {
  const int tid = threadIdx.x, lane = tid & 63;
  const int wv = __builtin_amdgcn_readfirstlane(tid >> 6), wr = wv & 1, wc = (wv >> 1) & 1, par = wv >> 2;
  const u32x4 wsl = mk_rsrc(ws, WS_USED);
  const int h = lane >> 5, t = lane & 31, trow = rt * 64 + sig(wr, t);
  const unsigned bo = WS_BT + (ct * 64 + wv * 8 + (lane >> 3)) * 4096 + rt * 128 + (((lane & 7) ^ (((lane >> 4) & 1) << 2)) << 4);
  const unsigned fo = lane < 8 ? WS_PF + ((rt >> 1) * 8 + lane) * 128 : lane < 24 ? WS_FLAG + 8 * ((ct * 16 + 2 * wv) * 8 + lane - 8)
                    : lane < 40 ? WS_FLAG + 8 * ((wv * 16 + lane - 24) * 8 + 4) : 0xfffffff0u;
  unsigned tries = 0;
  unsigned gp[4];
  const unsigned go = WS_G + h * 32768 + (rt * 2 + wr) * 128 + t * 4;
  bool b_arm;
#pragma unroll 1
  for (unsigned spin = 0; spin < SPIN_CAP; ++spin) {
    const u32x2 f = poll64(wsl, fo);
    const u64 bad = __ballot(f.x != tag.x || f.y != tag.y);
    ++tries;
    b_arm = (bad & 0xffff00ull) == 0;
    if ((bad & 0xffff0000ffull) == 0) break;
    __builtin_amdgcn_s_sleep(2);
  }
  STAMP(8);
  auto pq_piece = [&](int g) {
    dma16c(wsl, lane * 16, WS_PQF + rt * 65536 + (wv & 1) * 32768 + (wv >> 1) * 1024 + g * 4096, lds + L_PQ + (wv & 1) * 32768 + (wv >> 1) * 1024 + g * 4096);
  };
  {
    constexpr unsigned DONE = 0xffffffe0u;
    const unsigned ko = WS_KVZ + wv * 4096 + (lane >> 3) * 256 + (lane & 7) * 16;
    unsigned ka[3] = {ko, ko + 2048, lane < 16 ? WS_KVZ + wv * 4096 + lane * 256 + 128 : DONE};
    const lptr kl = lds + L_KVZ + wv * 2176 + (lane >> 5) * 544 + (lane & 31) * 16, kz = lds + L_KVZ + wv * 2176 + (lane >> 2) * 544 + 512 + (lane & 3) * 8;
    u32x4 k[3];
#pragma unroll
    for (int i = 0; i < 3; ++i) k[i] = ld128c(wsl, ka[i]);
    pq_piece(0); pq_piece(1);
    WAIT_VM(2);
    PIN(k[0]); PIN(k[1]); PIN(k[2]);
#pragma unroll 1
    for (unsigned spin = 0; spin < SPIN_CAP; ++spin) {
#pragma unroll
      for (int i = 0; i < 3; ++i) {
        if (umax4(0, k[i]) != EMPTY && ka[i] != DONE) {
          if (i < 2) *(LAS u32x4*)(kl + i * 1088) = k[i]; else *(LAS u32x2*)kz = u32x2{k[i].x, k[i].y};
          ka[i] = DONE;
        }
      }
      tries += 256;
      if (__all((ka[0] & ka[1] & ka[2]) == DONE)) break;
      __builtin_amdgcn_s_sleep(3);
      asm volatile("buffer_load_dwordx4 %0, %3, %6, 0 offen sc1\n\tbuffer_load_dwordx4 %1, %4, %6, 0 offen sc1\n\t"
                   "buffer_load_dwordx4 %2, %5, %6, 0 offen sc1\n\ts_waitcnt vmcnt(0)"
                   : "=&v"(k[0]), "=&v"(k[1]), "=&v"(k[2]) : "v"(ka[0]), "v"(ka[1]), "v"(ka[2]), "s"(wsl) : "memory");
    }
  }
  pq_piece(2); pq_piece(3);
  STAMP(9);
  STAMP_VAL(1, st_[0] + tries);
  const int gi = lane >> 4, blk = gi & 1, q = (lane & 15) >> 2, p4 = lane & 3;
  const lptr kvb = lds + L_KVZ + (t < 16 ? t : 16) * 32 + h * 16 + par * 1088;
  const lptr pqb = lds + L_PQ + wr * 32768 + lane * 16 + par * 2048;
  const lptr wob = lds + L_WO + (4 * h + q) * 128 + (((4 * wc + 2 * blk + (p4 >> 1)) ^ ((q >> 1) << 2)) << 4) + (p4 & 1) * 8 + par * 4096;
  f32x16 acc = {};
  f32x16 nd[2];
  bf16x8 wf[2], kf[3], pf[3];
  lgkm0();
#pragma unroll
  for (int n = 0; n < 19; ++n) {
    const int a = n - 2, b = n - 3;
    if (n == 0) { WAIT_VM(2); bar(); STAMP(10); SB; }
    if (n == 4) { WAIT_VM(4); bar(); SB; }
    if (n == 8) { WAIT_VM(2); bar(); SB; }
    if (n == 12) { WAIT_VM(5); bar(); SB; }
    if (a >= 0 && a < 16) { const f32x16 zero = {}; nd[a & 1] = mfma32(kf[a % 3], pf[a % 3], zero); SB; }
    if (n < 16) { kf[n % 3] = lds_b128(kvb + (n >> 1) * 2176 + (n & 1) * 544); pf[n % 3] = lds_b128(pqb + (n >> 1) * 4096 + (n & 1) * 1024); SB; }
    if (b >= 0) {
      f32x16& v = nd[b & 1];
      PIN(v);
      const float r = __builtin_amdgcn_rcpf(v[8] + 1e-6f);
      float o[8];
#pragma unroll
      for (int e = 0; e < 8; ++e) { o[e] = v[e] * r; PIN(o[e]); }
      const u32x4 p = {pk_bf16(o[0], o[1]), pk_bf16(o[2], o[3]), pk_bf16(o[4], o[5]), pk_bf16(o[6], o[7])};
      acc = mfma32(wf[b & 1], __builtin_bit_cast(bf16x8, p), acc);
      SB;
    }
    if (a >= 0 && a < 16) { wf[a & 1] = lds_tr2(wob + (a >> 1) * 8192 + (a & 1) * 2048, wob + (a >> 1) * 8192 + (a & 1) * 2048 + 1024); SB; }
    if (n < 4) { pq_piece(n + 4); SB; }
    if (n == 9) { dma16c(wsl, bo, 0, lds + L_BT + wv * 1024); SB; }
    if (n == 10 || n == 11) { gp[2 * n - 20] = ld32c(wsl, go + (2 * n - 20) * 8192); gp[2 * n - 19] = ld32c(wsl, go + (2 * n - 19) * 8192); SB; }
  }
  PIN(acc);
  STAMP(11);
  WAIT_VM(0);
  PIN(gp[0]); PIN(gp[1]); PIN(gp[2]); PIN(gp[3]);
  const unsigned gs = __float_as_uint((__uint_as_float(gp[0]) + __uint_as_float(gp[1])) + (__uint_as_float(gp[2]) + __uint_as_float(gp[3])));
  const auto gw2 = __builtin_amdgcn_permlane32_swap(gs, gs, false, false);
  float gt = __builtin_amdgcn_rcpf(1.f + __builtin_amdgcn_exp2f(-(__uint_as_float(gw2[0]) + __uint_as_float(gw2[1])) * LOG2E));
  PIN(gt);
#pragma unroll 1
  for (unsigned spin = 0; spin < SPIN_CAP; ++spin) {
    const unsigned m = umax4(0, *(const volatile LAS u32x4*)(lds + L_BT + wv * 1024 + lane * 16));
    if (b_arm && __all(m != EMPTY)) break;
    __builtin_amdgcn_s_sleep(2);
    if (!b_arm) { const u32x2 f = poll64(wsl, fo); b_arm = (__ballot(f.x != tag.x || f.y != tag.y) & 0xffff00ull) == 0; }
    dma16c(wsl, bo, 0, lds + L_BT + wv * 1024);
    WAIT_VM(0);
  }
  {
    LAS f32x4* xw = (LAS f32x4*)(lds + L_PQ + wv * 2048) + lane;
    const LAS f32x4* xr = (const LAS f32x4*)(lds + L_PQ + (wv ^ 4) * 2048) + lane;
    f32x4 keep[2];
#pragma unroll
    for (int G = 0; G < 2; ++G) {
      const f32x4 lo = {acc[4 * G], acc[4 * G + 1], acc[4 * G + 2], acc[4 * G + 3]};
      const f32x4 hi = {acc[8 + 4 * G], acc[9 + 4 * G], acc[10 + 4 * G], acc[11 + 4 * G]};
      if (par) { xw[64 * G] = lo; keep[G] = hi; } else { xw[64 * G] = hi; keep[G] = lo; }
    }
    const lptr bb = lds + L_BT + (wc * 32 + par * 16 + 4 * h + q) * 128 + (((4 * wr + 2 * blk + (p4 >> 1)) ^ ((q >> 1) << 2)) << 4) + (p4 & 1) * 8;
    lgkm0();
    bar();
    const u32x2 bv0 = __builtin_bit_cast(u32x2, lds_tr(bb)), bv1 = __builtin_bit_cast(u32x2, lds_tr(bb + 1024));
    u32x2 o[2];
#pragma unroll
    for (int G = 0; G < 2; ++G) {
      const f32x4 a = keep[G] + xr[64 * G];
      const unsigned b0 = G ? bv1.x : bv0.x, b1 = G ? bv1.y : bv0.y;
      const float bf[4] = {bf2f(b0), bfhi(b0), bf2f(b1), bfhi(b1)};
      float r[4];
#pragma unroll
      for (int e = 0; e < 4; ++e) r[e] = bf[e] + gt * (a[e] - bf[e]);
      o[G] = u32x2{pk_bf16(r[0], r[1]), pk_bf16(r[2], r[3])};
    }
    char* dst = (char*)out + (unsigned)(trow * 1024 + ct * 128 + wc * 64 + par * 32 + h * 16);
    auto sx = __builtin_amdgcn_permlane32_swap(o[0].x, o[1].x, false, false);
    auto sy = __builtin_amdgcn_permlane32_swap(o[0].y, o[1].y, false, false);
    const u32x4 v = {sx[0], sy[0], sx[1], sy[1]};
    *(u32x4*)dst = v;
  }
  if (blockIdx.x == 0 && tid == 0) st64(make_rsrc(ws, WS_USED), WS_EPOCH, tag);
}

__global__ void __launch_bounds__(512, 2) fused(const u16* __restrict__ x, const u16* __restrict__ wqkv, const u16* __restrict__ gw,
                                                const u16* __restrict__ lar, const u16* __restrict__ aim, const u16* __restrict__ cre,
                                                const u16* __restrict__ cim, const u16* __restrict__ ldt, const u16* __restrict__ dsk,
                                                const u16* __restrict__ wo, char* ws, u16* __restrict__ out
) {
  STAMP_DECL;
  extern __shared__ __attribute__((aligned(16))) char lds_[];
  const lptr lds = (lptr)lds_;
  const int b = blockIdx.x, xcd = b & 7, idx = b >> 3, tid = threadIdx.x, lane = tid & 63;
  const int wv = __builtin_amdgcn_readfirstlane(tid >> 6);
  if (wv == 7) CODE_PREFETCH("fused", lds + L_DUMP, ((idx & 7) * 64 + lane) * 64);
  if ((tid & 63) < 2) dma4(mk_rsrc(ws, WS_USED), WS_EPOCH + (tid & 63) * 4, lds + L_EPOCH + wv * 8);
  const int rt = (xcd >> 1) * 8 + (idx & 7), ct = (xcd & 1) * 4 + (idx >> 3);
  const Out o = {mk_rsrc(wo, 512 * 1024), (unsigned)(ct * 128 + (tid >> 3) * 1024 + (((tid & 7) ^ (((tid >> 4) & 1) << 2)) << 4)),
                 wv, lds};
  u32x2 tag;
  if (idx < 16) {
    tag = conv_role(lds, x, lar, aim, cre, cim, ldt, dsk, ws, xcd * 64 + idx * 4, idx, o STAMP_ARG);
  } else {
    const int rl = idx & 3, cl = (idx >> 2) & 3;
    tag = gemm_role(lds, x, wqkv, gw, ws, (xcd >> 1) * 4 + rl, (xcd & 1) * 4 + cl, 2 * ((rl + cl) & 3), o STAMP_ARG);
  }
  out_phase(lds, ws, out, rt, ct, tag STAMP_ARG);
  STAMP(12);
  STAMP_FLUSH;
  CODE_END("fused");
}

#define STARG(o)

extern "C" void kernel_launch(void* const* d_in, const int* in_sizes, int n_in, void* d_out, int out_size, void* d_ws,
                              size_t ws_size, hipStream_t stream) {
  const u16* x = (const u16*)d_in[0];
  const u16* wqkv = (const u16*)d_in[1];
  const u16* wo = (const u16*)d_in[2];
  const u16* lar = (const u16*)d_in[4];
  const u16* aim = (const u16*)d_in[5];
  const u16* cre = (const u16*)d_in[6];
  const u16* cim = (const u16*)d_in[7];
  const u16* ldt = (const u16*)d_in[8];
  const u16* dsk = (const u16*)d_in[9];
  const u16* gw = (const u16*)d_in[10];
  char* ws = (char*)d_ws;
  (void)hipFuncSetAttribute((const void*)fused, hipFuncAttributeMaxDynamicSharedMemorySize, 163840);
  fused<<<256, 512, 163840, stream>>>(x, wqkv, gw, lar, aim, cre, cim, ldt, dsk, wo, ws, (u16*)d_out STARG(0));
}
```

```cpp
#include <hip/hip_runtime.h>
#include <stdint.h>

typedef unsigned short u16;
typedef unsigned long long u64;
typedef __attribute__((ext_vector_type(8))) __bf16 bf16x8;
typedef __attribute__((ext_vector_type(2))) __bf16 bf16x2;
typedef __attribute__((ext_vector_type(8))) _Float16 f16x8;
typedef __attribute__((ext_vector_type(2))) _Float16 f16x2;
typedef __attribute__((ext_vector_type(4))) short s16x4;
typedef __attribute__((ext_vector_type(16))) float f32x16;
typedef __attribute__((ext_vector_type(4))) float f32x4;
typedef __attribute__((ext_vector_type(2))) float f32x2;
typedef __attribute__((ext_vector_type(4))) unsigned u32x4;
typedef __attribute__((ext_vector_type(2))) unsigned u32x2;
#define LAS __attribute__((address_space(3)))
#define CAS __attribute__((address_space(4)))
#define DI __device__ __forceinline__

constexpr unsigned WS_PQF = 0;
constexpr unsigned WS_BT  = 2u << 20;
constexpr unsigned WS_KVP = 4u << 20;
constexpr unsigned WS_KVZ = 6u << 20;
constexpr unsigned WS_G   = (6u << 20) + 65536;
constexpr unsigned WS_FLAG = (6u << 20) + 131072;
constexpr unsigned WS_EPOCH = WS_FLAG + 65536;
constexpr unsigned WS_PF = WS_FLAG + 131072;
constexpr unsigned WS_USED = 7u << 20;
constexpr unsigned SPIN_CAP = 200000;

constexpr float LOG2E = 1.4426950408889634f;

#define STAMP_DECL u64 st_[2] = {}
#define STAMP_PARM , u64* st_
#define STAMP_ARG , st_
#define STAMP(i) do { if ((i) == 99) { __builtin_amdgcn_sched_barrier(0); asm volatile("s_memrealtime %0\n\ts_waitcnt lgkmcnt(0)" : "=s"(st_[1]) :: "memory"); __builtin_amdgcn_sched_barrier(0); } } while (0)
#define STAMP_VAL(i, v)
#define STAMP_FLUSH do { const int role_ = idx >= 16; const u64 tg_ = ((2 == 2 || role_ == 2) ? st_[1] : st_[0]) + 2500; while (__builtin_amdgcn_s_memrealtime() < tg_) __builtin_amdgcn_s_sleep(1); } while (0)
#define SB __builtin_amdgcn_sched_barrier(0)
#define PIN(x) asm volatile("" : "+v"(x))
#define PINS(x) asm volatile("" : "+s"(x))

typedef LAS char* lptr;
DI float bf2f(unsigned v) { return __uint_as_float(v << 16); }
DI float bfhi(unsigned v) { return __uint_as_float(v & 0xffff0000u); }
DI unsigned pk_bf16(float a, float b) { f32x2 v = {a, b}; return __builtin_bit_cast(unsigned, __builtin_convertvector(v, bf16x2)); }
DI float rbf(float a) { return __uint_as_float(pk_bf16(a, a) << 16); }
DI f16x2 pk_f16(f32x2 v) { return __builtin_convertvector(v, f16x2); }
DI f32x2 cmul(f32x2 a, f32x2 b) { f32x2 t = a.xx * b; f32x2 bs = {-b.y, b.x}; return __builtin_elementwise_fma(a.yy, bs, t); }
DI void cmul2(f32x2& r0, f32x2 a0, f32x2 b0, f32x2& r1, f32x2 a1, f32x2 b1) {
  f32x2 t0, t1;
  asm("v_pk_mul_f32 %0, %2, %3 op_sel_hi:[0,1]\n\tv_pk_mul_f32 %1, %4, %5 op_sel_hi:[0,1]\n\t"
      "v_pk_fma_f32 %0, %2, %3, %0 op_sel:[1,1,0] op_sel_hi:[1,0,1] neg_lo:[0,1,0]\n\t"
      "v_pk_fma_f32 %1, %4, %5, %1 op_sel:[1,1,0] op_sel_hi:[1,0,1] neg_lo:[0,1,0]"
      : "=&v"(t0), "=&v"(t1) : "v"(a0), "v"(b0), "v"(a1), "v"(b1));
  r0 = t0; r1 = t1;
}
DI f16x2 pk_f16c(f32x2 v) { const f32x2 c = {v.x, -v.y}; return __builtin_convertvector(c, f16x2); }
DI f16x2 cmulh(f16x2 a, f16x2 b) { f16x2 t = a.xx * b; f16x2 bs = {-b.y, b.x}; return __builtin_elementwise_fma(a.yy, bs, t); }
DI u32x4 mk_rsrc(const void* p, unsigned bytes) { const u64 a = (u64)p; return u32x4{(unsigned)a, (unsigned)(a >> 32), bytes, 0x00020000u}; }
DI void dma16(u32x4 rs, unsigned voff, unsigned soff, lptr l) {
  asm volatile("s_mov_b32 m0, %3\n\ts_nop 0\n\tbuffer_load_dwordx4 %0, %1, %2 offen lds"
               :: "v"(voff), "s"(rs), "s"(soff), "s"((unsigned)(size_t)l) : "memory");
}
DI void dma16c(u32x4 rs, unsigned voff, unsigned soff, lptr l) {
  asm volatile("s_mov_b32 m0, %3\n\ts_nop 0\n\tbuffer_load_dwordx4 %0, %1, %2 offen sc1 lds"
               :: "v"(voff), "s"(rs), "s"(soff), "s"((unsigned)(size_t)l) : "memory");
}
DI u32x4 ld128c(u32x4 rs, unsigned voff) { u32x4 v; asm volatile("buffer_load_dwordx4 %0, %1, %2, 0 offen sc1" : "=v"(v) : "v"(voff), "s"(rs) : "memory"); return v; }
DI u32x2 poll64(u32x4 rs, unsigned voff) {
  u32x2 v; asm volatile("buffer_load_dwordx2 %0, %1, %2, 0 offen sc1\n\ts_waitcnt vmcnt(0)" : "=&v"(v) : "v"(voff), "s"(rs) : "memory"); return v;
}
DI u32x2 ld64c(u32x4 rs, unsigned voff) { u32x2 v; asm volatile("buffer_load_dwordx2 %0, %1, %2, 0 offen sc1" : "=v"(v) : "v"(voff), "s"(rs) : "memory"); return v; }
DI unsigned ld32c(u32x4 rs, unsigned voff) { unsigned v; asm volatile("buffer_load_dword %0, %1, %2, 0 offen sc1" : "=v"(v) : "v"(voff), "s"(rs) : "memory"); return v; }
DI int sig(int w, int n) { return 32 * (n >> 4) + ((n >> 3) & 1) + 16 * w + 8 * ((n >> 2) & 1) + 2 * (n & 3); }
DI bf16x8 lds_b128(lptr p) { return *(const LAS bf16x8*)p; }
DI s16x4 lds_tr(lptr p) { return __builtin_amdgcn_ds_read_tr16_b64_v4i16((LAS s16x4*)p); }
DI bf16x8 lds_tr2(lptr p0, lptr p1) { s16x4 a = lds_tr(p0), b = lds_tr(p1); return __builtin_bit_cast(bf16x8, __builtin_shufflevector(a, b, 0, 1, 2, 3, 4, 5, 6, 7)); }
DI f32x16 mfma32(bf16x8 a, bf16x8 b, f32x16 c) { return __builtin_amdgcn_mfma_f32_32x32x16_bf16(a, b, c, 0, 0, 0); }
DI f32x4 mfma16(bf16x8 a, bf16x8 b, f32x4 c) { return __builtin_amdgcn_mfma_f32_16x16x32_bf16(a, b, c, 0, 0, 0); }
DI f32x4 mfma16h(f16x8 a, f16x8 b, f32x4 c) { return __builtin_amdgcn_mfma_f32_16x16x32_f16(a, b, c, 0, 0, 0); }
DI void bar() { asm volatile("" ::: "memory"); __builtin_amdgcn_s_barrier(); asm volatile("" ::: "memory"); }
DI void lgkm0() { asm volatile("s_waitcnt lgkmcnt(0)" ::: "memory"); }
#define WAIT_VM(n) asm volatile("s_waitcnt vmcnt(" #n ")" ::: "memory")
DI float phi(float tp) {
  const float e = __builtin_amdgcn_fmed3f(__builtin_amdgcn_exp2f(tp * LOG2E), 0.f, 1.f);
  const int ti = __float_as_int(tp);
  return __int_as_float(ti > 0x358637bd ? ti : 0x358637bd) + e;
}
DI bf16x8 pack8(const f32x16& a, int o) {
  u32x4 p = {pk_bf16(a[o], a[o + 1]), pk_bf16(a[o + 2], a[o + 3]), pk_bf16(a[o + 4], a[o + 5]), pk_bf16(a[o + 6], a[o + 7])};
  return __builtin_bit_cast(bf16x8, p);
}
template <int CTRL, int RM = 0xf> DI int dppi(int v) { return __builtin_amdgcn_update_dpp(0, v, CTRL, RM, 0xf, true); }
template <int CTRL, int RM = 0xf> DI float dppf(float v) { return __int_as_float(dppi<CTRL, RM>(__float_as_int(v))); }
DI unsigned umax(unsigned a, unsigned b) { return a > b ? a : b; }
DI unsigned umin(unsigned a, unsigned b) { return a < b ? a : b; }
DI unsigned umax4(unsigned m, u32x4 v) { return umax(umax(umax(m, v.x), v.y), umax(v.z, v.w)); }
DI unsigned wave_umax(unsigned v) {
  v = umax(v, dppi<0x111>(v)); v = umax(v, dppi<0x112>(v)); v = umax(v, dppi<0x114>(v)); v = umax(v, dppi<0x118>(v));
  v = umax(v, dppi<0x142, 0xa>(v)); v = umax(v, dppi<0x143, 0xc>(v));
  return __builtin_amdgcn_readlane(v, 63);
}
DI float wave_sum(float v) {
  v += dppf<0x111>(v); v += dppf<0x112>(v); v += dppf<0x114>(v); v += dppf<0x118>(v);
  v += dppf<0x142, 0xa>(v); v += dppf<0x143, 0xc>(v);
  return __int_as_float(__builtin_amdgcn_readlane(__float_as_int(v), 63));
}
DI void dma4(u32x4 rs, unsigned voff, lptr l) {
  asm volatile("s_mov_b32 m0, %2\n\ts_nop 0\n\tbuffer_load_dword %0, %1, 0 offen sc1 lds" :: "v"(voff), "s"(rs), "s"((unsigned)(size_t)l) : "memory");
}
#define CODE_PREFETCH(NAME, dump, off) do { u64 pc_; unsigned len_; \
  asm volatile("s_getpc_b64 %0\n.L" NAME "_beg:\n\ts_mov_b32 %1, .L" NAME "_end-.L" NAME "_beg" : "=s"(pc_), "=s"(len_)); \
  asm volatile("s_mov_b32 m0, %2\n\ts_nop 0\n\tbuffer_load_dword %0, %1, 0 offen lds" \
               :: "v"(off), "s"(mk_rsrc((const void*)pc_, len_)), "s"((unsigned)(size_t)(dump)) : "memory"); } while (0)
#define CODE_END(NAME) asm volatile(".L" NAME "_end:")
typedef __amdgpu_buffer_rsrc_t rsrc_t;
DI rsrc_t make_rsrc(void* p, unsigned bytes) { return __builtin_amdgcn_make_buffer_rsrc(p, 0, bytes, 0x00020000); }
constexpr int WT = 16;
DI void st128(rsrc_t r, unsigned off, u32x4 v) { __builtin_amdgcn_raw_buffer_store_b128(v, r, off, 0, WT); }
DI void st64(rsrc_t r, unsigned off, u32x2 v) { __builtin_amdgcn_raw_buffer_store_b64(v, r, off, 0, WT); }
DI void st32(rsrc_t r, unsigned off, unsigned v) { __builtin_amdgcn_raw_buffer_store_b32(v, r, off, 0, WT); }

constexpr int L_PQ = 0, L_WO = 66560, L_KVZ = L_WO + 65536, L_BT = L_KVZ + 17408;
struct Out { u32x4 wor; unsigned woo; int wv; lptr lds; };
constexpr unsigned EMPTY = 0xffffffffu;
DI void wo_issue(const Out& o, int gr) { dma16(o.wor, o.woo, gr * 65536, o.lds + L_WO + o.wv * 1024 + gr * 8192); }
constexpr int L_DUMP = 163840 - 512, L_EPOCH = 163840 - 256;
DI u32x2 next_tag(lptr lds, int wv) {
  const u32x2 e = *(const volatile LAS u32x2*)(lds + L_EPOCH + wv * 8);
  const u64 t = (((u64)(unsigned)__builtin_amdgcn_readfirstlane(e.y) << 32) | (unsigned)__builtin_amdgcn_readfirstlane(e.x)) + 1;
  return u32x2{(unsigned)t, (unsigned)(t >> 32)};
}

constexpr int SLOT = 32768, NSLOT = 4;
constexpr int GA_EX = L_KVZ;
constexpr int GA_GW = GA_EX + 8 * 544 * 4;

DI u32x2 gemm_role(lptr lds, const u16* __restrict__ x, const u16* __restrict__ w, const u16* __restrict__ gw, char* ws,
                   int rb, int cg, int rot, const Out& wo STAMP_PARM) {
  const int tid = threadIdx.x, lane = tid & 63;
  const int wv = __builtin_amdgcn_readfirstlane(tid >> 6), rg = wv & 3, hp = wv >> 2;
  const u32x4 xr = mk_rsrc(x, 2048 * 1024), wr_ = mk_rsrc(w, 512 * 3072);
  const unsigned xrow = tid >> 3, xo = rb * 131072 + xrow * 1024 + (((tid & 7) ^ ((xrow >> 1) & 7)) << 4);
  const unsigned wrow = tid >> 4, wp = (tid & 15) ^ ((wrow & 3) << 2);
  const unsigned wo_ = cg * 128 + wrow * 3072 + 1024 + (wp >> 3) * 1024 + ((wp & 7) << 4);
  const unsigned qo = cg * 128 + xrow * 3072 + (((tid & 7) ^ (((xrow >> 1) & 1) << 2)) << 4);
  const lptr lw = lds + wv * 1024;
  auto issue = [&](int c, int i) {
    const lptr slot = lw + (c % NSLOT) * SLOT;
    if (c < 8) {
      const unsigned kc = (c + rot) & 7;
      if (i == 0) dma16(xr, xo, kc * 128, slot);
      if (i == 1) dma16(wr_, wo_, kc * (64 * 3072), slot + 16384);
      if (i == 2) dma16(xr, xo, kc * 128 + 65536, slot + 8192);
      if (i == 3) dma16(wr_, wo_, kc * (64 * 3072) + 32 * 3072, slot + 24576);
    } else {
      dma16(wr_, qo, (((c - 8) * 4 + i + rot) & 7) * (64 * 3072), slot + i * 8192);
    }
  };
  STAMP(0);
  const int gsel = hp == 0 ? ((cg - rot) & 7) : -1;
  if (hp == 0 && lane < 8) dma16(mk_rsrc(gw, 1024), lane * 16, cg * 128, lds + GA_GW + wv * 128);
#pragma unroll
  for (int c = 0; c < 3; ++c) {
#pragma unroll
    for (int i = 0; i < 4; ++i) issue(c, i);
    if (c < 2) bar();
  }
  const rsrc_t wsr = make_rsrc(ws, WS_USED);
  const unsigned pqo = WS_PQF + (((rb * 4 + rg) * 32 + cg * 4 + 2 * hp) * 64 + lane) * 16;
  const int h = lane >> 5, row = (rg >> 1) * 64 + sig(rg & 1, lane & 31);
  const int gi = lane >> 4, blk = gi & 1, q = (lane & 15) >> 2, p4 = lane & 3;
  lptr ap[4], bp[2];
#pragma unroll
  for (int s = 0; s < 4; ++s) ap[s] = lds + row * 128 + (((2 * s + h) ^ ((row >> 1) & 7)) << 4);
#pragma unroll
  for (int ct = 0; ct < 2; ++ct) bp[ct] = lds + 16384 + (8 * h + q) * 256 + (((4 * (2 * ct + hp) + 2 * blk + (p4 >> 1)) ^ (q << 2)) << 4) + (p4 & 1) * 8;
  f32x16 ak, av;
  float g0 = 0.f;
#pragma unroll
  for (int i = 0; i < 16; ++i) { ak[i] = 1e-6f; av[i] = 0.f; }
  bf16x8 xf[32], Bf[3][2];
  u32x2 tag;
  auto gate_steps = [&](int c) {
    if (gsel == c) {
      const lptr gp = lds + GA_GW + wv * 128 + h * 16;
      f32x16 ag = {};
#pragma unroll
      for (int i = 0; i < 4; ++i) ag = mfma32(lds_b128(gp + i * 32), xf[4 * c + i], ag);
      g0 = ag[0];
    }
    PIN(g0);
  };
  lptr qp = lds + (8 * h + q) * 128 + (((4 * hp + 2 * blk + (p4 >> 1)) ^ ((q >> 1) << 2)) << 4) + (p4 & 1) * 8;
  const int dp = lane & 31, e_ = lane & 15;
  lptr exw = lds + GA_EX + rg * 4352 + (2 * hp + (dp >> 4)) * 1024 + e_ * 64 + (((2 * h) ^ (e_ >> 2)) << 4);
  lptr ezw = lds + GA_EX + rg * 4352 + 4096 + (2 * hp + (dp >> 4)) * 64 + (((e_ >> 2) & 1) * 8 + (e_ & 3) + 4 * (e_ >> 3)) * 4;
  lptr exr = lds + GA_EX + (tid < 272 ? tid : 0) * 16;
  unsigned kpo = tid < 272 ? WS_KVP + (rb * 8 + cg) * 8704 + tid * 16 : 0xffff0000u;
  PIN(qp); PIN(exw); PIN(ezw); PIN(exr); PIN(kpo);
  f32x16 aq;
  STAMP(1);
#pragma unroll
  for (int n = 0; n < 32; ++n) {
    const int c = n >> 2, s = n & 3, so = (c & 1) * SLOT;
    if (s == 0) {
      if (c > 0) {
        lgkm0();
        if ((c & 1) == 0) {
          const int dlt = (c == 4) ? -2 * SLOT : 2 * SLOT;
#pragma unroll
          for (int i = 0; i < 4; ++i) { ap[i] += dlt; PIN(ap[i]); }
#pragma unroll
          for (int i = 0; i < 2; ++i) { bp[i] += dlt; PIN(bp[i]); }
        }
      }
      WAIT_VM(8);
      bar();
      if (c == 0) STAMP(2);
      SB;
    }
    if (n >= 2) { ak = mfma32(xf[n - 2], Bf[(n - 2) % 3][0], ak); SB; }
    xf[n] = lds_b128(ap[s] + so); Bf[n % 3][0] = lds_tr2(bp[0] + so + s * 4096, bp[0] + so + s * 4096 + 1024); SB;
    if (n >= 2) { av = mfma32(xf[n - 2], Bf[(n - 2) % 3][1], av); SB; }
    Bf[n % 3][1] = lds_tr2(bp[1] + so + s * 4096, bp[1] + so + s * 4096 + 1024); SB;
    if (c + 3 < 10) { issue(c + 3, s); SB; } else if (s < 3) { wo_issue(wo, s); SB; }
    if (n == 14) { tag = next_tag(lds, wv); SB; }
    if (n == 30) {
#pragma unroll
      for (int i = 0; i < 16; ++i) aq[i] = 1e-6f;
      PIN(aq); SB;
    }
    if (n > 0 && s == 0) { gate_steps(c - 1); SB; }
  }
  STAMP(3);
  f32x16 kv = {}, z = {};
  constexpr int PD = 5;
  bf16x8 Aq[PD + 1], ones;
#pragma unroll
  for (int i = 0; i < 8; ++i) ones[i] = (__bf16)1.0f;
  u32x4 pk[2], vv[2];
  auto rdq = [&](int m) { Aq[m % (PD + 1)] = lds_tr2(qp + m * 2048, qp + m * 2048 + 512); };
#pragma unroll
  for (int m = -2; m < 32; ++m) {
    if (m == -2) { lgkm0(); WAIT_VM(7); bar(); SB; rdq(0); rdq(1); SB; }
    if (m == 11) { lgkm0(); WAIT_VM(6); STAMP(4); bar(); SB; }
    if (m < 0) { ak = mfma32(xf[32 + m], Bf[(32 + m) % 3][0], ak); av = mfma32(xf[32 + m], Bf[(32 + m) % 3][1], av); SB; }
    else { aq = mfma32(Aq[m % (PD + 1)], xf[m], aq); SB; }
    if (m == -2) { rdq(2); SB; }
    if (m + PD < 32) { rdq(m + PD); SB; }
    if (m == 0) { vv[0] = __builtin_bit_cast(u32x4, pack8(av, 0)); vv[1] = __builtin_bit_cast(u32x4, pack8(av, 8)); PIN(vv[0]); PIN(vv[1]); SB; }
    if (m >= 1 && m < 9) {
      const int j = m - 1;
      unsigned p = pk_bf16(phi(ak[2 * j]), phi(ak[2 * j + 1]));
      PIN(p);
      pk[j >> 2][j & 3] = p;
      SB;
      if ((j & 3) == 3) {
        kv = mfma32(__builtin_bit_cast(bf16x8, pk[j >> 2]), __builtin_bit_cast(bf16x8, vv[j >> 2]), kv);
        z = mfma32(ones, __builtin_bit_cast(bf16x8, pk[j >> 2]), z);
        SB;
      }
      if (j == 1 || j == 4 || j == 6) { wo_issue(wo, 3 + j / 2 - (j == 1 ? 0 : 1)); SB; }
    }
    if (m == 10) {
      float sel[8];
#pragma unroll
      for (int i = 0; i < 8; ++i) { float a = kv[i], b = kv[8 + i]; PIN(a); PIN(b); sel[i] = dp < 16 ? a : b; }
      *(LAS f32x4*)exw = f32x4{sel[0], sel[1], sel[2], sel[3]};
      *(LAS f32x4*)((lptr)((unsigned)(size_t)exw ^ 16u)) = f32x4{sel[4], sel[5], sel[6], sel[7]};
      *(LAS float*)ezw = z[0];
      SB;
    }
    if (m == 11) {
      const LAS f32x4* ex = (const LAS f32x4*)exr;
      const u32x4 sum = __builtin_bit_cast(u32x4, (ex[0] + ex[272]) + (ex[544] + ex[816]));
      st128(wsr, kpo, u32x4{sum.x, tag.x, sum.y, tag.x});
      st128(wsr, kpo + 4352, u32x4{sum.z, tag.x, sum.w, tag.x});
      SB;
    }
    if (m == 13) { PIN(aq); gate_steps(7); SB; }
    if (m == 16 || m == 19) { wo_issue(wo, m == 16 ? 6 : 7); SB; }
  }
  PIN(aq);
  STAMP(5);
  {
    if (hp == 0) st32(wsr, h == 0 ? WS_G + cg * 8192 + (rb * 4 + rg) * 128 + lane * 4 : 0xfffffff0u, __float_as_uint(g0));
#pragma unroll
    for (int i = 0; i < 8; ++i) aq[i] = phi(aq[i]);
    st128(wsr, pqo, __builtin_bit_cast(u32x4, pack8(aq, 0)));
#pragma unroll
    for (int i = 8; i < 16; ++i) aq[i] = phi(aq[i]);
    st128(wsr, pqo + 1024, __builtin_bit_cast(u32x4, pack8(aq, 8)));
    STAMP(6);
    WAIT_VM(0);
  }
  STAMP(7);
  bar();
  STAMP(13);
  if (tid == 511) st64(wsr, WS_PF + (rb * 8 + cg) * 128, tag);
  return tag;
}

constexpr int SC_ROW = 2064;
constexpr int KC_CP = 4224, KC_CH = 2 * KC_CP;
constexpr int EX_BASE = 4 * KC_CH;
constexpr int U_BASE = 57 * SC_ROW, U_PAD = 224, U_CP = 4576, U_CH = 2 * U_CP;
constexpr int T_BASE = U_BASE + 4 * U_CH, T_CH = 1536;
static_assert(T_BASE + 4 * T_CH <= 163840 && U_BASE % 16 == 0, "lds");

DI f32x2 cexp_p(float l2, float rv) {
  const float m = __builtin_amdgcn_exp2f(l2), f = __builtin_amdgcn_fractf(rv);
  const f32x2 r = {m * __builtin_amdgcn_cosf(f), m * __builtin_amdgcn_sinf(f)};
  return r;
}
DI bf16x8 lds_2b64(lptr p) {
  const u32x2 a = *(const volatile LAS u32x2*)p, b = *(const volatile LAS u32x2*)(p + 8);
  const u32x4 v = {a.x, a.y, b.x, b.y};
  return __builtin_bit_cast(bf16x8, v);
}
DI unsigned perm(unsigned hi, unsigned lo, unsigned sel) { return __builtin_amdgcn_perm(hi, lo, sel); }
constexpr unsigned LL = 0x01000100, LH = 0x03020302, HL = 0x05040504, SWL = 0x01000302, SWH = 0x05040706, HL_LH = 0x03020504;
template <int E0, int E1, int E2, int E3>
DI void put4(unsigned base, f16x2 a, f16x2 b, f16x2 c, f16x2 d) {
  constexpr int O = E0 < 28 ? 0 : 28;
  static_assert((E1 < 28) == (E0 < 28) && (E2 < 28) == (E0 < 28) && (E3 < 28) == (E0 < 28), "one base");
  asm volatile("s_mov_b32 m0, %4\n\ts_nop 0\n\tds_write_addtid_b32 %0 offset:%5\n\tds_write_addtid_b32 %1 offset:%6\n\t"
               "ds_write_addtid_b32 %2 offset:%7\n\tds_write_addtid_b32 %3 offset:%8"
               :: "v"(a), "v"(b), "v"(c), "v"(d), "s"(base + O * SC_ROW), "i"((E0 - O) * SC_ROW), "i"((E1 - O) * SC_ROW),
                  "i"((E2 - O) * SC_ROW), "i"((E3 - O) * SC_ROW) : "memory");
}

DI u32x2 conv_role(lptr lds, const u16* __restrict__ x, const u16* __restrict__ lar, const u16* __restrict__ aim,
                   const u16* __restrict__ cre, const u16* __restrict__ cim, const u16* __restrict__ ldt,
                   const u16* __restrict__ dsk, char* ws, int ch0, int jrot, const Out& wo STAMP_PARM) {
  const int tid = threadIdx.x, lane = tid & 63;
  const int wv = __builtin_amdgcn_readfirstlane(tid >> 6), cl = wv & 3, half = wv >> 2;
  const int d = ch0 + cl;
  STAMP(0);
  const unsigned r_lar = ((const unsigned*)lar)[d * 64 + lane], r_aim = ((const unsigned*)aim)[d * 64 + lane];
  const unsigned r_cr = ((const unsigned*)cre)[d * 64 + lane], r_ci = ((const unsigned*)cim)[d * 64 + lane];
  const unsigned s_ldt = ((const CAS unsigned*)ldt)[d >> 1], s_dsk = ((const CAS unsigned*)dsk)[d >> 1];
  const int j = lane & 15, g = lane >> 4;
  const rsrc_t wsr = make_rsrc(ws, WS_USED);
  bar();
  const int cj = ch0 >> 2, cg = cj >> 4, rb = cj & 15;
  const unsigned kvo = wv == 4 && lane < 9 ? WS_KVZ + cj * 256 + lane * 16 : 0xffffffe0u;
  const int tt = (tid + jrot * 64) & 511;
  const char* xg = (const char*)x + ch0 * 2;
  const unsigned xoff = tt * 4096;
  u32x2 ur[4];
  ur[0] = *(const u32x2*)(xg + xoff); ur[1] = *(const u32x2*)(xg + xoff + 1024);
  u32x2 up;
  {
    const u32x4 zero = {0, 0, 0, 0};
    if (lane < 28) *(LAS u32x4*)(lds + U_BASE + cl * U_CH + half * U_CP + lane * 16) = zero;
  }
  const unsigned scb = (unsigned)(size_t)lds + cl * 512 + half * 256;
  const lptr fb = lds + cl * 512 + half * 256 + j * SC_ROW + g * 16;
  unsigned selA = lane < 32 ? LL : lane == 32 ? HL : SWH, selB = lane < 32 ? HL_LH : SWH, selC = lane < 32 ? LL : SWH, selE = lane < 32 ? LL : SWL;
  if (half == 0) { const bool odd = !(lane & 1); selA = odd ? LH : LL; selB = odd ? HL : LH; selC = odd ? HL : SWL; }
  PIN(selA); PIN(selB); PIN(selC); PIN(selE);
  const unsigned shl = half ? 0 : 16;
  const float f_ldt = (d & 1) ? bfhi(s_ldt) : bf2f(s_ldt), f_dsk = (d & 1) ? bfhi(s_dsk) : bf2f(s_dsk);
  const float dt = __int_as_float(__builtin_amdgcn_readfirstlane(__float_as_int(rbf(__expf(f_ldt)))));
  SB;
  const float l2 = -rbf(__builtin_amdgcn_exp2f(bfhi(r_lar << shl) * LOG2E)) * dt * LOG2E;
  const float rv = bfhi(r_aim << shl) * dt * 0.15915494309189535f;
  unsigned cm;
  {
    typedef __attribute__((ext_vector_type(2))) unsigned short u16x2;
    const u16x2 m2 = __builtin_elementwise_max(__builtin_bit_cast(u16x2, r_cr & 0x7fff7fffu), __builtin_bit_cast(u16x2, r_ci & 0x7fff7fffu));
    const unsigned m = __builtin_bit_cast(unsigned, m2);
    cm = umax(wave_umax(umax(m << 16, m)), 0x0da24260u);
  }
  const float cmax = __uint_as_float(cm), cinv = __builtin_amdgcn_rcpf(cmax);
  f32x4 kt[3];
  {
    f16x8 rf[4], t0[4];
    const f16x2 one = {(_Float16)1.f, (_Float16)0.f};
    const f32x2 E1 = cexp_p(l2, rv), E8 = cexp_p(l2 * 8.f, rv * 8.f), E128 = cexp_p(l2 * 128.f, rv * 128.f);
    f32x2 E2, E3, E4, E16, E24, E32, E64, E96, E256, E384, E512, H1, H2, H3, H4, dummy;
    const f32x2 H0 = {bfhi(r_cr << shl) * cinv, bfhi(r_ci << shl) * cinv};
    cmul2(E16, E8, E8, E256, E128, E128);
    cmul2(E32, E16, E16, E512, E256, E256);
    cmul2(E24, E16, E8, E2, E1, E1);
    cmul2(E64, E32, E32, H1, H0, E512);
    cmul2(E3, E2, E1, E4, E2, E2);
    cmul2(E96, E64, E32, H2, H1, E512);
    cmul2(E384, E256, E128, H3, H2, E512);
    {
      const f16x2 P1 = pk_f16c(E8), P2 = pk_f16c(E16), P3 = pk_f16c(E24), Q1 = pk_f16c(E32), Q2 = pk_f16c(E64), Q3 = pk_f16c(E96);
      put4<0, 1, 2, 3>(scb, one, P1, P2, P3);
      put4<4, 5, 6, 7>(scb, Q1, cmulh(Q1, P1), cmulh(Q1, P2), cmulh(Q1, P3));
      put4<8, 9, 10, 11>(scb, Q2, cmulh(Q2, P1), cmulh(Q2, P2), cmulh(Q2, P3));
      put4<12, 13, 14, 15>(scb, Q3, cmulh(Q3, P1), cmulh(Q3, P2), cmulh(Q3, P3));
    }
    SB;
    ur[2] = *(const u32x2*)(xg + xoff + 2048);
    SB;
    H4 = cmul(H3, E512); (void)dummy;
    const f16x2 e1 = pk_f16(E1), e2 = pk_f16(E2), e3 = pk_f16(E3), e4 = pk_f16(E4);
    const f16x2 e5 = cmulh(e4, e1), e6 = cmulh(e4, e2), e7 = cmulh(e4, e3);
    const f16x2 S1 = pk_f16(E128), S2 = pk_f16(E256), S3 = pk_f16(E384);
    const f16x2 h0 = pk_f16(H0), h1 = pk_f16(H1), h2 = pk_f16(H2), h3 = pk_f16(H3);
    {
      const f16x2 G1 = cmulh(h0, S1);
      put4<16, 17, 18, 19>(scb, h0, cmulh(h0, e1), cmulh(h0, e2), cmulh(h0, e3));
      put4<20, 21, 22, 23>(scb, cmulh(h0, e4), cmulh(h0, e5), cmulh(h0, e6), cmulh(h0, e7));
      put4<24, 25, 26, 27>(scb, G1, cmulh(G1, e1), cmulh(G1, e2), cmulh(G1, e3));
      put4<28, 29, 30, 31>(scb, cmulh(G1, e4), cmulh(G1, e5), cmulh(G1, e6), cmulh(G1, e7));
    }
#pragma unroll
    for (int s = 0; s < 4; ++s) { rf[s] = *(const LAS f16x8*)(fb + s * 64); t0[s] = *(const LAS f16x8*)(fb + 16 * SC_ROW + s * 64); }
    SB;
    ur[3] = *(const u32x2*)(xg + xoff + 3072);
    up = __builtin_amdgcn_raw_buffer_load_b64(make_rsrc((void*)x, 2048 * 1024), lane == 0 && tt > 0 ? ch0 * 2 + xoff - 1024 : 0xfffffff0u, 0, 0);
    SB;
    {
      const f16x2 G2 = cmulh(h0, S2), G3 = cmulh(h0, S3), G5 = cmulh(h1, S1), G6 = cmulh(h1, S2), G7 = cmulh(h1, S3);
      put4<32, 33, 34, 35>(scb, G2, cmulh(G2, e2), cmulh(G2, e4), cmulh(G2, e6));
      kt[0] = mfma16h(t0[0], rf[0], f32x4{0.f, 0.f, 0.f, 0.f});
      kt[0] = mfma16h(t0[1], rf[1], kt[0]);
      put4<36, 37, 38, 39>(scb, G3, cmulh(G3, e2), cmulh(G3, e4), cmulh(G3, e6));
      kt[0] = mfma16h(t0[2], rf[2], kt[0]);
      put4<40, 41, 42, 43>(scb, h1, cmulh(h1, e4), G5, cmulh(G5, e4));
      kt[0] = mfma16h(t0[3], rf[3], kt[0]);
      put4<44, 45, 46, 47>(scb, G6, cmulh(G6, e4), G7, cmulh(G7, e4));
    }
#pragma unroll
    for (int s = 0; s < 4; ++s) t0[s] = *(const LAS f16x8*)(fb + 32 * SC_ROW + s * 64);
    SB;
    put4<48, 49, 50, 51>(scb, h2, cmulh(h2, S1), cmulh(h2, S2), cmulh(h2, S3));
    put4<52, 53, 54, 55>(scb, h3, cmulh(h3, S1), cmulh(h3, S2), cmulh(h3, S3));
    *(LAS f16x2*)(lds + cl * 512 + half * 256 + lane * 4 + 56 * SC_ROW) = pk_f16(H4);
    f16x8 t2[4];
#pragma unroll
    for (int s = 0; s < 4; ++s) t2[s] = *(const LAS f16x8*)(fb + 48 * SC_ROW + s * 64);
    SB;
    kt[1] = f32x4{0.f, 0.f, 0.f, 0.f}; kt[2] = kt[1];
#pragma unroll
    for (int s = 0; s < 4; ++s) kt[1] = mfma16h(t0[s], rf[s], kt[1]);
#pragma unroll
    for (int s = 0; s < 4; ++s) kt[2] = mfma16h(t2[s], rf[s], kt[2]);
  }
  const lptr Tb = lds + T_BASE + cl * T_CH;
  {
    LAS f32x4* xw = (LAS f32x4*)(lds + cl * 512 + half * 256 + g * SC_ROW) + j;
    const LAS f32x4* xr = (const LAS f32x4*)(lds + cl * 512 + (half ^ 1) * 256 + g * SC_ROW) + j;
    if (half) xw[0] = kt[0]; else { xw[0] = kt[1]; xw[4 * SC_ROW / 16] = kt[2]; }
    const float ks = 2.f * dt * cmax;
    const lptr p1 = half ? Tb + (256 + 64 * g + (g < 2 ? 4 : 2) * j) * 2 : Tb + (128 * (g >> 1) + 4 * (g & 1) + 8 * j) * 2;
    lptr p1b = p1 + (g < 2 ? 4 : 64);
    PIN(p1b);
    lgkm0();
    STAMP(3);
    bar();
    if (half == 0) {
      f32x4 a0 = (kt[0] + xr[0]) * ks;
      if (lane == 0) a0[0] += f_dsk;
      *(LAS u32x2*)p1 = u32x2{pk_bf16(a0[0], a0[1]), pk_bf16(a0[2], a0[3])};
    } else {
      const f32x4 a1 = (kt[1] + xr[0]) * ks, a2 = (kt[2] + xr[4 * SC_ROW / 16]) * ks;
      *(LAS unsigned*)p1 = pk_bf16(a1[0], a1[1]);
      *(LAS unsigned*)p1b = pk_bf16(a1[2], a1[3]);
      const unsigned q0 = pk_bf16(a2[0], a2[1]), q1 = pk_bf16(a2[2], a2[3]);
      LAS u16* p2 = (LAS u16*)Tb + 512 + 64 * g + j;
      p2[0] = (u16)q0; p2[16] = (u16)(q0 >> 16); p2[32] = (u16)q1; p2[48] = (u16)(q1 >> 16);
    }
  }
  lgkm0();
  STAMP(4);
  bar();
  {
    const lptr kc = lds + cl * KC_CH + lane * 16;
    auto emit = [&](int Y0, unsigned P0, unsigned P1, unsigned P2, unsigned P3, unsigned P4) {
      *(LAS u32x4*)(kc + Y0 * 16) = u32x4{P0, P1, P2, P3};
      *(LAS u32x4*)(kc + KC_CP + Y0 * 16) = u32x4{P1, P2, P3, P4};
    };
    if (half == 0) {
      const LAS unsigned* p = (const LAS unsigned*)(Tb + ((575 - lane) >> 1) * 4);
      const unsigned D0 = p[32], D1 = p[33], F0 = p[0], F1 = p[1], Z = *(const LAS unsigned*)Tb;
      SB;
      const unsigned e1 = perm(D1, D0, selB), e0 = perm(D1, D0, selA), f1 = perm(F1, F0, selB), f0 = perm(F1, F0, selA);
      emit(0, e1, e1, perm(D1, D0, selC), e0, e0);
      emit(64, f1, f1, perm(F1, F0, selC), f0, f0);
      if (lane < 4) emit(256, lane == 0 ? perm(0, Z, SWL) : 0u, 0u, 0u, 0u, 0u);
    } else {
      const LAS unsigned* p = (const LAS unsigned*)(Tb + (255 - lane) * 4);
      const LAS unsigned* r = (const LAS unsigned*)(Tb + (lane < 32 ? 760 - 8 * lane : 1008 - 16 * lane));
      const unsigned F0 = p[0], F1 = p[1], D0 = r[0], D1 = r[1], D2 = r[2], D3 = r[3], D4 = r[4];
      SB;
      const unsigned e2 = perm(F1, F0, HL);
      emit(128, e2, e2, perm(F1, F0, LH), perm(F1, F0, SWL), perm(F1, F0, LL));
      emit(192, perm(D4, D2, selA), perm(lane < 32 ? D2 : D3, D1, selB), perm(D2, D1, selC), perm(D1, D0, selB), perm(D1, D0, selE));
    }
  }
  SB;
  {
    const u32x2 pv = {(unsigned)__builtin_amdgcn_update_dpp((int)up.x, (int)ur[3].x, 0x138, 0xf, 0xf, false),
                      (unsigned)__builtin_amdgcn_update_dpp((int)up.y, (int)ur[3].y, 0x138, 0xf, 0xf, false)};
    const lptr ub = lds + U_BASE + (U_PAD + 4 * tt) * 2;
#pragma unroll
    for (int c = 0; c < 4; ++c) {
      const unsigned sel = (c & 1) ? 0x07060302 : 0x05040100;
      const unsigned p = pv[c >> 1], r0 = ur[0][c >> 1], r1 = ur[1][c >> 1], r2 = ur[2][c >> 1], r3 = ur[3][c >> 1];
      *(LAS u32x2*)(ub + c * U_CH) = u32x2{perm(r1, r0, sel), perm(r3, r2, sel)};
      *(LAS u32x2*)(ub + c * U_CH + U_CP) = u32x2{perm(r0, p, sel), perm(r2, r1, sel)};
    }
  }
  const u32x2 tag = next_tag(lds, wv);
  lgkm0();
  STAMP(5);
  bar();
  const u32x4 wsl = mk_rsrc(ws, WS_USED);
  const int a_ = j;
  const lptr ab = lds + cl * KC_CH + ((a_ & 1) ? KC_CP + 4088 : 4096) - 8 * (a_ >> 1) + 16 * g - 3584 - 192 - half * 256;
  const lptr bb = lds + U_BASE + cl * U_CH + ((a_ & 1) ? 0 : U_CP) + (U_PAD + 32 * (a_ >> 1) + 8 * g) * 2 - 192 - half * 256;
  f32x4 acc[8];
#pragma unroll
  for (int i = 0; i < 8; ++i) acc[i] = f32x4{0.f, 0.f, 0.f, 0.f};
  LAS f32x4* exw = (LAS f32x4*)(lds + EX_BASE + wv * 4096) + lane;
  f32x4 keep[4];
  {
    u32x2 Al[2][8], Ah[2][8];
    bf16x8 B[2][8];
    auto rd = [&](int r, int k) {
      const int nb = r & 1, mu = k / 3, w = k % 3;
      if (w == 0) B[nb][mu] = lds_b128(bb + 512 * mu + 64 * (3 - r));
      if (w == 1) Al[nb][mu] = *(const volatile LAS u32x2*)(ab + 512 * (7 - mu) + 64 * (3 - r));
      if (w == 2) Ah[nb][mu] = *(const volatile LAS u32x2*)(ab + 512 * (7 - mu) + 64 * (3 - r) + 8);
    };
    auto AF = [&](int nb, int mu) { return __builtin_bit_cast(bf16x8, u32x4{Al[nb][mu].x, Al[nb][mu].y, Ah[nb][mu].x, Ah[nb][mu].y}); };
#pragma unroll
    for (int k = 0; k < 24; ++k) rd(0, k);
    SB;
#pragma unroll
    for (int r = 1; r < 4; ++r) {
      const int cb = (r - 1) & 1;
      int cnt = 0;

#pragma unroll
      for (int m = 0; m < 8; ++m)
#pragma unroll
        for (int n = 0; n <= m; ++n)
#pragma unroll
          for (int sw = 0; sw < 2; ++sw)
            if (m + n < 8 && (sw == 0 || n < m)) {
              const int mu = sw ? n : m, nu = sw ? m : n;
              acc[m + n] = mfma16(AF(cb, mu), B[cb][nu], acc[m + n]);
              SB;
              if (cnt % 3 != 2) rd(r, cnt - cnt / 3);
              else if (cnt == 14 || cnt == 29) wo_issue(wo, 2 * (r - 1) + (cnt == 29));
              else if ((cnt == 5 || cnt == 20) && r == 1) st128(wsr, WS_BT + ch0 * 4096 + (cnt == 5 ? 0 : 8192) + tid * 16, u32x4{EMPTY, EMPTY, EMPTY, EMPTY});
              else if (cnt == 2 && r == 1) st128(wsr, kvo, u32x4{EMPTY, EMPTY, EMPTY, EMPTY});
              else if (cnt == 2 && r == 3) { WAIT_VM(3); st64(wsr, lane == 0 ? WS_FLAG + (cj * 8 + wv) * 8 : 0xfffffff0u, tag); }
              SB;
              ++cnt;
            }
    }
    auto tiles = [&](int t0) {
#pragma unroll
      for (int mu = 0; mu < 8; ++mu)
#pragma unroll
        for (int n = 0; n < 8; ++n) if (mu + n >= t0 && mu + n < t0 + 4) acc[mu + n] = mfma16(AF(1, mu), B[1][n], acc[mu + n]);
    };
    if (half) {
      tiles(0); SB; tiles(4);
#pragma unroll
      for (int i = 0; i < 4; ++i) { exw[64 * i] = acc[i]; keep[i] = acc[4 + i]; }
    } else {
      tiles(4); SB; tiles(0);
#pragma unroll
      for (int i = 0; i < 4; ++i) { exw[64 * i] = acc[4 + i]; keep[i] = acc[i]; }
    }
  }
  STAMP(6);
  {
    const LAS f32x4* exr = (const LAS f32x4*)(lds + EX_BASE + (wv ^ 4) * 4096) + lane;
    const unsigned bto = WS_BT + d * 4096 + half * 2048 + (g & 1) * 512 + (a_ >> 2) * 128 + (g >> 1) * 64 + (a_ & 3) * 16;
    u32x4 bt[2], k[4];
    constexpr unsigned DONE = 0xffffffe0u;
    const unsigned src = WS_KVP + (j * 8 + cg) * 8704;
    const unsigned ko = src + (rb >> 2) * 1024 + ((rb & 3) * 4 + cl) * 64 + ((g ^ (rb & 3)) << 4);
    const bool zl = wv == 4 && g == 0;
    unsigned ka[4] = {half ? ko : DONE, half ? ko + 4352 : DONE, zl ? src + 4096 + rb * 16 : DONE, zl ? src + 4352 + 4096 + rb * 16 : DONE};
#pragma unroll
    for (int i = 0; i < 4; ++i) k[i] = ld128c(wsl, ka[i]);
    lgkm0();
    STAMP(7);
    bar();
#pragma unroll
    for (int i = 0; i < 4; i += 2) {
      const f32x4 v = keep[i] + exr[64 * i], w = keep[i + 1] + exr[64 * i + 64];
      const auto sx = __builtin_amdgcn_permlane16_swap(umin(pk_bf16(v[0], v[1]), EMPTY - 1), umin(pk_bf16(w[0], w[1]), EMPTY - 1), false, false);
      const auto sy = __builtin_amdgcn_permlane16_swap(umin(pk_bf16(v[2], v[3]), EMPTY - 1), umin(pk_bf16(w[2], w[3]), EMPTY - 1), false, false);
      bt[i >> 1] = u32x4{sx[0], sy[0], sx[1], sy[1]};
      PIN(bt[i >> 1]);
    }
    wo_issue(wo, 6); wo_issue(wo, 7);
    STAMP(2);
    unsigned spin = 0;
    WAIT_VM(2);
    PIN(k[0]); PIN(k[1]); PIN(k[2]); PIN(k[3]);
    const lptr slice = lds + T_BASE;
    if (half == 0) { st128(wsr, bto, bt[0]); st128(wsr, bto + 1024, bt[1]); }
    else {
      unsigned val[8] = {0, 0, 0, 0, 0, 0, 0, 0};
#pragma unroll 1
      for (; spin < SPIN_CAP; ++spin) {
#pragma unroll
        for (int i = 0; i < 4; ++i) {
          const bool got = k[i].y == tag.x && k[i].w == tag.x && ka[i] != DONE;
          val[2 * i] = got ? k[i].x : val[2 * i]; val[2 * i + 1] = got ? k[i].z : val[2 * i + 1];
          ka[i] = got ? DONE : ka[i];
        }
        if (__all((ka[0] & ka[1] & ka[2] & ka[3]) == DONE)) break;
        asm volatile("buffer_load_dwordx4 %0, %4, %8, 0 offen sc1\n\tbuffer_load_dwordx4 %1, %5, %8, 0 offen sc1\n\t"
                     "buffer_load_dwordx4 %2, %6, %8, 0 offen sc1\n\tbuffer_load_dwordx4 %3, %7, %8, 0 offen sc1\n\ts_waitcnt vmcnt(0)"
                     : "=&v"(k[0]), "=&v"(k[1]), "=&v"(k[2]), "=&v"(k[3])
                     : "v"(ka[0]), "v"(ka[1]), "v"(ka[2]), "v"(ka[3]), "s"(wsl) : "memory");
      }
      auto rowsum = [&](unsigned u) { float v = __uint_as_float(u); v += dppf<0x111>(v); v += dppf<0x112>(v); v += dppf<0x114>(v); v += dppf<0x118>(v); return v; };
      const u32x2 kvs = {umin(pk_bf16(rowsum(val[0]), rowsum(val[1])), EMPTY - 1), umin(pk_bf16(rowsum(val[2]), rowsum(val[3])), EMPTY - 1)};
      if (j == 15) *(LAS u32x2*)(slice + (cl * 4 + g) * 8) = kvs;
      if (wv == 4) {
        const u32x2 zs = {umin(pk_bf16(rowsum(val[4]), rowsum(val[5])), EMPTY - 1), umin(pk_bf16(rowsum(val[6]), rowsum(val[7])), EMPTY - 1)};
        if (lane == 15) *(LAS u32x4*)(slice + 128) = u32x4{zs.x, zs.y, 0, 0};
      }
    }
    lgkm0();
    bar();
    if (half) {
      if (wv == 4) st128(wsr, kvo, *(const LAS u32x4*)(slice + (lane < 9 ? lane : 0) * 16));
      st128(wsr, bto, bt[0]); st128(wsr, bto + 1024, bt[1]);
    }
    STAMP_VAL(3, st_[0] + spin);
    STAMP(13);
    return tag;
  }
}

DI void out_phase(lptr lds, char* ws, u16* __restrict__ out, int rt, int ct, u32x2 tag STAMP_PARM) {
  const int tid = threadIdx.x, lane = tid & 63;
  const int wv = __builtin_amdgcn_readfirstlane(tid >> 6), wr = wv & 1, wc = (wv >> 1) & 1, par = wv >> 2;
  const u32x4 wsl = mk_rsrc(ws, WS_USED);
  const int h = lane >> 5, t = lane & 31, trow = rt * 64 + sig(wr, t);
  const unsigned bo = WS_BT + (ct * 64 + wv * 8 + (lane >> 3)) * 4096 + rt * 128 + (((lane & 7) ^ (((lane >> 4) & 1) << 2)) << 4);
  const unsigned fo = lane < 8 ? WS_PF + ((rt >> 1) * 8 + lane) * 128 : lane < 24 ? WS_FLAG + 8 * ((ct * 16 + 2 * wv) * 8 + lane - 8)
                    : lane < 40 ? WS_FLAG + 8 * ((wv * 16 + lane - 24) * 8 + 4) : 0xfffffff0u;
  unsigned tries = 0;
  unsigned gp[4];
  const unsigned go = WS_G + h * 32768 + (rt * 2 + wr) * 128 + t * 4;
  bool b_arm;
#pragma unroll 1
  for (unsigned spin = 0; spin < SPIN_CAP; ++spin) {
    const u32x2 f = poll64(wsl, fo);
    const u64 bad = __ballot(f.x != tag.x || f.y != tag.y);
    ++tries;
    b_arm = (bad & 0xffff00ull) == 0;
    if ((bad & 0xffff0000ffull) == 0) break;
    __builtin_amdgcn_s_sleep(2);
  }
  STAMP(8);
  auto pq_piece = [&](int g) {
    dma16c(wsl, lane * 16, WS_PQF + rt * 65536 + (wv & 1) * 32768 + (wv >> 1) * 1024 + g * 4096, lds + L_PQ + (wv & 1) * 32768 + (wv >> 1) * 1024 + g * 4096);
  };
  {
    constexpr unsigned DONE = 0xffffffe0u;
    const unsigned ko = WS_KVZ + wv * 4096 + (lane >> 3) * 256 + (lane & 7) * 16;
    unsigned ka[3] = {ko, ko + 2048, lane < 16 ? WS_KVZ + wv * 4096 + lane * 256 + 128 : DONE};
    const lptr kl = lds + L_KVZ + wv * 2176 + (lane >> 5) * 544 + (lane & 31) * 16, kz = lds + L_KVZ + wv * 2176 + (lane >> 2) * 544 + 512 + (lane & 3) * 8;
    u32x4 k[3];
#pragma unroll
    for (int i = 0; i < 3; ++i) k[i] = ld128c(wsl, ka[i]);
    pq_piece(0); pq_piece(1);
    WAIT_VM(2);
    PIN(k[0]); PIN(k[1]); PIN(k[2]);
#pragma unroll 1
    for (unsigned spin = 0; spin < SPIN_CAP; ++spin) {
#pragma unroll
      for (int i = 0; i < 3; ++i) {
        if (umax4(0, k[i]) != EMPTY && ka[i] != DONE) {
          if (i < 2) *(LAS u32x4*)(kl + i * 1088) = k[i]; else *(LAS u32x2*)kz = u32x2{k[i].x, k[i].y};
          ka[i] = DONE;
        }
      }
      tries += 256;
      if (__all((ka[0] & ka[1] & ka[2]) == DONE)) break;
      __builtin_amdgcn_s_sleep(3);
      asm volatile("buffer_load_dwordx4 %0, %3, %6, 0 offen sc1\n\tbuffer_load_dwordx4 %1, %4, %6, 0 offen sc1\n\t"
                   "buffer_load_dwordx4 %2, %5, %6, 0 offen sc1\n\ts_waitcnt vmcnt(0)"
                   : "=&v"(k[0]), "=&v"(k[1]), "=&v"(k[2]) : "v"(ka[0]), "v"(ka[1]), "v"(ka[2]), "s"(wsl) : "memory");
    }
  }
  pq_piece(2); pq_piece(3);
  STAMP(9);
  STAMP_VAL(1, st_[0] + tries);
  const int gi = lane >> 4, blk = gi & 1, q = (lane & 15) >> 2, p4 = lane & 3;
  const lptr kvb = lds + L_KVZ + (t < 16 ? t : 16) * 32 + h * 16 + par * 1088;
  const lptr pqb = lds + L_PQ + wr * 32768 + lane * 16 + par * 2048;
  const lptr wob = lds + L_WO + (4 * h + q) * 128 + (((4 * wc + 2 * blk + (p4 >> 1)) ^ ((q >> 1) << 2)) << 4) + (p4 & 1) * 8 + par * 4096;
  f32x16 acc = {};
  f32x16 nd[2];
  bf16x8 wf[2], kf[3], pf[3];
  lgkm0();
#pragma unroll
  for (int n = 0; n < 19; ++n) {
    const int a = n - 2, b = n - 3;
    if (n == 0) { WAIT_VM(2); bar(); STAMP(10); SB; }
    if (n == 4) { WAIT_VM(4); bar(); SB; }
    if (n == 8) { WAIT_VM(2); bar(); SB; }
    if (n == 12) { WAIT_VM(5); bar(); SB; }
    if (a >= 0 && a < 16) { const f32x16 zero = {}; nd[a & 1] = mfma32(kf[a % 3], pf[a % 3], zero); SB; }
    if (n < 16) { kf[n % 3] = lds_b128(kvb + (n >> 1) * 2176 + (n & 1) * 544); pf[n % 3] = lds_b128(pqb + (n >> 1) * 4096 + (n & 1) * 1024); SB; }
    if (b >= 0) {
      f32x16& v = nd[b & 1];
      PIN(v);
      const float r = __builtin_amdgcn_rcpf(v[8] + 1e-6f);
      float o[8];
#pragma unroll
      for (int e = 0; e < 8; ++e) { o[e] = v[e] * r; PIN(o[e]); }
      const u32x4 p = {pk_bf16(o[0], o[1]), pk_bf16(o[2], o[3]), pk_bf16(o[4], o[5]), pk_bf16(o[6], o[7])};
      acc = mfma32(wf[b & 1], __builtin_bit_cast(bf16x8, p), acc);
      SB;
    }
    if (a >= 0 && a < 16) { wf[a & 1] = lds_tr2(wob + (a >> 1) * 8192 + (a & 1) * 2048, wob + (a >> 1) * 8192 + (a & 1) * 2048 + 1024); SB; }
    if (n < 4) { pq_piece(n + 4); SB; }
    if (n == 9) { dma16c(wsl, bo, 0, lds + L_BT + wv * 1024); SB; }
    if (n == 10 || n == 11) { gp[2 * n - 20] = ld32c(wsl, go + (2 * n - 20) * 8192); gp[2 * n - 19] = ld32c(wsl, go + (2 * n - 19) * 8192); SB; }
  }
  PIN(acc);
  STAMP(11);
  WAIT_VM(0);
  PIN(gp[0]); PIN(gp[1]); PIN(gp[2]); PIN(gp[3]);
  const unsigned gs = __float_as_uint((__uint_as_float(gp[0]) + __uint_as_float(gp[1])) + (__uint_as_float(gp[2]) + __uint_as_float(gp[3])));
  const auto gw2 = __builtin_amdgcn_permlane32_swap(gs, gs, false, false);
  float gt = __builtin_amdgcn_rcpf(1.f + __builtin_amdgcn_exp2f(-(__uint_as_float(gw2[0]) + __uint_as_float(gw2[1])) * LOG2E));
  PIN(gt);
#pragma unroll 1
  for (unsigned spin = 0; spin < SPIN_CAP; ++spin) {
    const unsigned m = umax4(0, *(const volatile LAS u32x4*)(lds + L_BT + wv * 1024 + lane * 16));
    if (b_arm && __all(m != EMPTY)) break;
    __builtin_amdgcn_s_sleep(2);
    if (!b_arm) { const u32x2 f = poll64(wsl, fo); b_arm = (__ballot(f.x != tag.x || f.y != tag.y) & 0xffff00ull) == 0; }
    dma16c(wsl, bo, 0, lds + L_BT + wv * 1024);
    WAIT_VM(0);
  }
  {
    LAS f32x4* xw = (LAS f32x4*)(lds + L_PQ + wv * 2048) + lane;
    const LAS f32x4* xr = (const LAS f32x4*)(lds + L_PQ + (wv ^ 4) * 2048) + lane;
    f32x4 keep[2];
#pragma unroll
    for (int G = 0; G < 2; ++G) {
      const f32x4 lo = {acc[4 * G], acc[4 * G + 1], acc[4 * G + 2], acc[4 * G + 3]};
      const f32x4 hi = {acc[8 + 4 * G], acc[9 + 4 * G], acc[10 + 4 * G], acc[11 + 4 * G]};
      if (par) { xw[64 * G] = lo; keep[G] = hi; } else { xw[64 * G] = hi; keep[G] = lo; }
    }
    const lptr bb = lds + L_BT + (wc * 32 + par * 16 + 4 * h + q) * 128 + (((4 * wr + 2 * blk + (p4 >> 1)) ^ ((q >> 1) << 2)) << 4) + (p4 & 1) * 8;
    lgkm0();
    bar();
    const u32x2 bv0 = __builtin_bit_cast(u32x2, lds_tr(bb)), bv1 = __builtin_bit_cast(u32x2, lds_tr(bb + 1024));
    u32x2 o[2];
#pragma unroll
    for (int G = 0; G < 2; ++G) {
      const f32x4 a = keep[G] + xr[64 * G];
      const unsigned b0 = G ? bv1.x : bv0.x, b1 = G ? bv1.y : bv0.y;
      const float bf[4] = {bf2f(b0), bfhi(b0), bf2f(b1), bfhi(b1)};
      float r[4];
#pragma unroll
      for (int e = 0; e < 4; ++e) r[e] = bf[e] + gt * (a[e] - bf[e]);
      o[G] = u32x2{pk_bf16(r[0], r[1]), pk_bf16(r[2], r[3])};
    }
    char* dst = (char*)out + (unsigned)(trow * 1024 + ct * 128 + wc * 64 + par * 32 + h * 16);
    auto sx = __builtin_amdgcn_permlane32_swap(o[0].x, o[1].x, false, false);
    auto sy = __builtin_amdgcn_permlane32_swap(o[0].y, o[1].y, false, false);
    const u32x4 v = {sx[0], sy[0], sx[1], sy[1]};
    *(u32x4*)dst = v;
  }
  if (blockIdx.x == 0 && tid == 0) st64(make_rsrc(ws, WS_USED), WS_EPOCH, tag);
}

__global__ void __launch_bounds__(512, 2) fused(const u16* __restrict__ x, const u16* __restrict__ wqkv, const u16* __restrict__ gw,
                                                const u16* __restrict__ lar, const u16* __restrict__ aim, const u16* __restrict__ cre,
                                                const u16* __restrict__ cim, const u16* __restrict__ ldt, const u16* __restrict__ dsk,
                                                const u16* __restrict__ wo, char* ws, u16* __restrict__ out
) {
  STAMP_DECL;
  st_[0] = __builtin_amdgcn_s_memrealtime();
  st_[1] = st_[0];
  extern __shared__ __attribute__((aligned(16))) char lds_[];
  const lptr lds = (lptr)lds_;
  const int b = blockIdx.x, xcd = b & 7, idx = b >> 3, tid = threadIdx.x, lane = tid & 63;
  const int wv = __builtin_amdgcn_readfirstlane(tid >> 6);
  if (wv == 7) CODE_PREFETCH("fused", lds + L_DUMP, ((idx & 7) * 64 + lane) * 64);
  if ((tid & 63) < 2) dma4(mk_rsrc(ws, WS_USED), WS_EPOCH + (tid & 63) * 4, lds + L_EPOCH + wv * 8);
  const int rt = (xcd >> 1) * 8 + (idx & 7), ct = (xcd & 1) * 4 + (idx >> 3);
  const Out o = {mk_rsrc(wo, 512 * 1024), (unsigned)(ct * 128 + (tid >> 3) * 1024 + (((tid & 7) ^ (((tid >> 4) & 1) << 2)) << 4)),
                 wv, lds};
  u32x2 tag;
  if (idx < 16) {
    tag = conv_role(lds, x, lar, aim, cre, cim, ldt, dsk, ws, xcd * 64 + idx * 4, idx, o STAMP_ARG);
  } else {
    const int rl = idx & 3, cl = (idx >> 2) & 3;
    tag = gemm_role(lds, x, wqkv, gw, ws, (xcd >> 1) * 4 + rl, (xcd & 1) * 4 + cl, 2 * ((rl + cl) & 3), o STAMP_ARG);
  }
  out_phase(lds, ws, out, rt, ct, tag STAMP_ARG);
  STAMP(12);
  STAMP_FLUSH;
  CODE_END("fused");
}

#define STARG(o)

extern "C" void kernel_launch(void* const* d_in, const int* in_sizes, int n_in, void* d_out, int out_size, void* d_ws,
                              size_t ws_size, hipStream_t stream) {
  const u16* x = (const u16*)d_in[0];
  const u16* wqkv = (const u16*)d_in[1];
  const u16* wo = (const u16*)d_in[2];
  const u16* lar = (const u16*)d_in[4];
  const u16* aim = (const u16*)d_in[5];
  const u16* cre = (const u16*)d_in[6];
  const u16* cim = (const u16*)d_in[7];
  const u16* ldt = (const u16*)d_in[8];
  const u16* dsk = (const u16*)d_in[9];
  const u16* gw = (const u16*)d_in[10];
  char* ws = (char*)d_ws;
  (void)hipFuncSetAttribute((const void*)fused, hipFuncAttributeMaxDynamicSharedMemorySize, 163840);
  fused<<<256, 512, 163840, stream>>>(x, wqkv, gw, lar, aim, cre, cim, ldt, dsk, wo, ws, (u16*)d_out STARG(0));
}
```

```cpp
#include <hip/hip_runtime.h>
#include <stdint.h>

typedef unsigned short u16;
typedef unsigned long long u64;
typedef __attribute__((ext_vector_type(8))) __bf16 bf16x8;
typedef __attribute__((ext_vector_type(2))) __bf16 bf16x2;
typedef __attribute__((ext_vector_type(8))) _Float16 f16x8;
typedef __attribute__((ext_vector_type(2))) _Float16 f16x2;
typedef __attribute__((ext_vector_type(4))) short s16x4;
typedef __attribute__((ext_vector_type(16))) float f32x16;
typedef __attribute__((ext_vector_type(4))) float f32x4;
typedef __attribute__((ext_vector_type(2))) float f32x2;
typedef __attribute__((ext_vector_type(4))) unsigned u32x4;
typedef __attribute__((ext_vector_type(2))) unsigned u32x2;
#define LAS __attribute__((address_space(3)))
#define CAS __attribute__((address_space(4)))
#define DI __device__ __forceinline__

constexpr unsigned WS_PQF = 0;
constexpr unsigned WS_BT  = 2u << 20;
constexpr unsigned WS_KVP = 4u << 20;
constexpr unsigned WS_KVZ = 6u << 20;
constexpr unsigned WS_G   = (6u << 20) + 65536;
constexpr unsigned WS_FLAG = (6u << 20) + 131072;
constexpr unsigned WS_EPOCH = WS_FLAG + 65536;
constexpr unsigned WS_PF = WS_FLAG + 131072;
constexpr unsigned WS_USED = 7u << 20;
constexpr unsigned SPIN_CAP = 200000;

constexpr float LOG2E = 1.4426950408889634f;

#define STAMP_DECL u64 st_[2] = {}
#define STAMP_PARM , u64* st_
#define STAMP_ARG , st_
#define STAMP(i) do { if ((i) == 0) { __builtin_amdgcn_sched_barrier(0); asm volatile("s_memrealtime %0\n\ts_waitcnt lgkmcnt(0)" : "=s"(st_[1]) :: "memory"); __builtin_amdgcn_sched_barrier(0); } } while (0)
#define STAMP_VAL(i, v)
#define STAMP_FLUSH do { const int role_ = idx >= 16; const u64 tg_ = ((1 == 2 || role_ == 1) ? st_[1] : st_[0]) + 2500; while (__builtin_amdgcn_s_memrealtime() < tg_) __builtin_amdgcn_s_sleep(1); } while (0)
#define SB __builtin_amdgcn_sched_barrier(0)
#define PIN(x) asm volatile("" : "+v"(x))
#define PINS(x) asm volatile("" : "+s"(x))

typedef LAS char* lptr;
DI float bf2f(unsigned v) { return __uint_as_float(v << 16); }
DI float bfhi(unsigned v) { return __uint_as_float(v & 0xffff0000u); }
DI unsigned pk_bf16(float a, float b) { f32x2 v = {a, b}; return __builtin_bit_cast(unsigned, __builtin_convertvector(v, bf16x2)); }
DI float rbf(float a) { return __uint_as_float(pk_bf16(a, a) << 16); }
DI f16x2 pk_f16(f32x2 v) { return __builtin_convertvector(v, f16x2); }
DI f32x2 cmul(f32x2 a, f32x2 b) { f32x2 t = a.xx * b; f32x2 bs = {-b.y, b.x}; return __builtin_elementwise_fma(a.yy, bs, t); }
DI void cmul2(f32x2& r0, f32x2 a0, f32x2 b0, f32x2& r1, f32x2 a1, f32x2 b1) {
  f32x2 t0, t1;
  asm("v_pk_mul_f32 %0, %2, %3 op_sel_hi:[0,1]\n\tv_pk_mul_f32 %1, %4, %5 op_sel_hi:[0,1]\n\t"
      "v_pk_fma_f32 %0, %2, %3, %0 op_sel:[1,1,0] op_sel_hi:[1,0,1] neg_lo:[0,1,0]\n\t"
      "v_pk_fma_f32 %1, %4, %5, %1 op_sel:[1,1,0] op_sel_hi:[1,0,1] neg_lo:[0,1,0]"
      : "=&v"(t0), "=&v"(t1) : "v"(a0), "v"(b0), "v"(a1), "v"(b1));
  r0 = t0; r1 = t1;
}
DI f16x2 pk_f16c(f32x2 v) { const f32x2 c = {v.x, -v.y}; return __builtin_convertvector(c, f16x2); }
DI f16x2 cmulh(f16x2 a, f16x2 b) { f16x2 t = a.xx * b; f16x2 bs = {-b.y, b.x}; return __builtin_elementwise_fma(a.yy, bs, t); }
DI u32x4 mk_rsrc(const void* p, unsigned bytes) { const u64 a = (u64)p; return u32x4{(unsigned)a, (unsigned)(a >> 32), bytes, 0x00020000u}; }
DI void dma16(u32x4 rs, unsigned voff, unsigned soff, lptr l) {
  asm volatile("s_mov_b32 m0, %3\n\ts_nop 0\n\tbuffer_load_dwordx4 %0, %1, %2 offen lds"
               :: "v"(voff), "s"(rs), "s"(soff), "s"((unsigned)(size_t)l) : "memory");
}
DI void dma16c(u32x4 rs, unsigned voff, unsigned soff, lptr l) {
  asm volatile("s_mov_b32 m0, %3\n\ts_nop 0\n\tbuffer_load_dwordx4 %0, %1, %2 offen sc1 lds"
               :: "v"(voff), "s"(rs), "s"(soff), "s"((unsigned)(size_t)l) : "memory");
}
DI u32x4 ld128c(u32x4 rs, unsigned voff) { u32x4 v; asm volatile("buffer_load_dwordx4 %0, %1, %2, 0 offen sc1" : "=v"(v) : "v"(voff), "s"(rs) : "memory"); return v; }
DI u32x2 poll64(u32x4 rs, unsigned voff) {
  u32x2 v; asm volatile("buffer_load_dwordx2 %0, %1, %2, 0 offen sc1\n\ts_waitcnt vmcnt(0)" : "=&v"(v) : "v"(voff), "s"(rs) : "memory"); return v;
}
DI u32x2 ld64c(u32x4 rs, unsigned voff) { u32x2 v; asm volatile("buffer_load_dwordx2 %0, %1, %2, 0 offen sc1" : "=v"(v) : "v"(voff), "s"(rs) : "memory"); return v; }
DI unsigned ld32c(u32x4 rs, unsigned voff) { unsigned v; asm volatile("buffer_load_dword %0, %1, %2, 0 offen sc1" : "=v"(v) : "v"(voff), "s"(rs) : "memory"); return v; }
DI int sig(int w, int n) { return 32 * (n >> 4) + ((n >> 3) & 1) + 16 * w + 8 * ((n >> 2) & 1) + 2 * (n & 3); }
DI bf16x8 lds_b128(lptr p) { return *(const LAS bf16x8*)p; }
DI s16x4 lds_tr(lptr p) { return __builtin_amdgcn_ds_read_tr16_b64_v4i16((LAS s16x4*)p); }
DI bf16x8 lds_tr2(lptr p0, lptr p1) { s16x4 a = lds_tr(p0), b = lds_tr(p1); return __builtin_bit_cast(bf16x8, __builtin_shufflevector(a, b, 0, 1, 2, 3, 4, 5, 6, 7)); }
DI f32x16 mfma32(bf16x8 a, bf16x8 b, f32x16 c) { return __builtin_amdgcn_mfma_f32_32x32x16_bf16(a, b, c, 0, 0, 0); }
DI f32x4 mfma16(bf16x8 a, bf16x8 b, f32x4 c) { return __builtin_amdgcn_mfma_f32_16x16x32_bf16(a, b, c, 0, 0, 0); }
DI f32x4 mfma16h(f16x8 a, f16x8 b, f32x4 c) { return __builtin_amdgcn_mfma_f32_16x16x32_f16(a, b, c, 0, 0, 0); }
DI void bar() { asm volatile("" ::: "memory"); __builtin_amdgcn_s_barrier(); asm volatile("" ::: "memory"); }
DI void lgkm0() { asm volatile("s_waitcnt lgkmcnt(0)" ::: "memory"); }
#define WAIT_VM(n) asm volatile("s_waitcnt vmcnt(" #n ")" ::: "memory")
DI float phi(float tp) {
  const float e = __builtin_amdgcn_fmed3f(__builtin_amdgcn_exp2f(tp * LOG2E), 0.f, 1.f);
  const int ti = __float_as_int(tp);
  return __int_as_float(ti > 0x358637bd ? ti : 0x358637bd) + e;
}
DI bf16x8 pack8(const f32x16& a, int o) {
  u32x4 p = {pk_bf16(a[o], a[o + 1]), pk_bf16(a[o + 2], a[o + 3]), pk_bf16(a[o + 4], a[o + 5]), pk_bf16(a[o + 6], a[o + 7])};
  return __builtin_bit_cast(bf16x8, p);
}
template <int CTRL, int RM = 0xf> DI int dppi(int v) { return __builtin_amdgcn_update_dpp(0, v, CTRL, RM, 0xf, true); }
template <int CTRL, int RM = 0xf> DI float dppf(float v) { return __int_as_float(dppi<CTRL, RM>(__float_as_int(v))); }
DI unsigned umax(unsigned a, unsigned b) { return a > b ? a : b; }
DI unsigned umin(unsigned a, unsigned b) { return a < b ? a : b; }
DI unsigned umax4(unsigned m, u32x4 v) { return umax(umax(umax(m, v.x), v.y), umax(v.z, v.w)); }
DI unsigned wave_umax(unsigned v) {
  v = umax(v, dppi<0x111>(v)); v = umax(v, dppi<0x112>(v)); v = umax(v, dppi<0x114>(v)); v = umax(v, dppi<0x118>(v));
  v = umax(v, dppi<0x142, 0xa>(v)); v = umax(v, dppi<0x143, 0xc>(v));
  return __builtin_amdgcn_readlane(v, 63);
}
DI float wave_sum(float v) {
  v += dppf<0x111>(v); v += dppf<0x112>(v); v += dppf<0x114>(v); v += dppf<0x118>(v);
  v += dppf<0x142, 0xa>(v); v += dppf<0x143, 0xc>(v);
  return __int_as_float(__builtin_amdgcn_readlane(__float_as_int(v), 63));
}
DI void dma4(u32x4 rs, unsigned voff, lptr l) {
  asm volatile("s_mov_b32 m0, %2\n\ts_nop 0\n\tbuffer_load_dword %0, %1, 0 offen sc1 lds" :: "v"(voff), "s"(rs), "s"((unsigned)(size_t)l) : "memory");
}
#define CODE_PREFETCH(NAME, dump, off) do { u64 pc_; unsigned len_; \
  asm volatile("s_getpc_b64 %0\n.L" NAME "_beg:\n\ts_mov_b32 %1, .L" NAME "_end-.L" NAME "_beg" : "=s"(pc_), "=s"(len_)); \
  asm volatile("s_mov_b32 m0, %2\n\ts_nop 0\n\tbuffer_load_dword %0, %1, 0 offen lds" \
               :: "v"(off), "s"(mk_rsrc((const void*)pc_, len_)), "s"((unsigned)(size_t)(dump)) : "memory"); } while (0)
#define CODE_END(NAME) asm volatile(".L" NAME "_end:")
typedef __amdgpu_buffer_rsrc_t rsrc_t;
DI rsrc_t make_rsrc(void* p, unsigned bytes) { return __builtin_amdgcn_make_buffer_rsrc(p, 0, bytes, 0x00020000); }
constexpr int WT = 16;
DI void st128(rsrc_t r, unsigned off, u32x4 v) { __builtin_amdgcn_raw_buffer_store_b128(v, r, off, 0, WT); }
DI void st64(rsrc_t r, unsigned off, u32x2 v) { __builtin_amdgcn_raw_buffer_store_b64(v, r, off, 0, WT); }
DI void st32(rsrc_t r, unsigned off, unsigned v) { __builtin_amdgcn_raw_buffer_store_b32(v, r, off, 0, WT); }

constexpr int L_PQ = 0, L_WO = 66560, L_KVZ = L_WO + 65536, L_BT = L_KVZ + 17408;
struct Out { u32x4 wor; unsigned woo; int wv; lptr lds; };
constexpr unsigned EMPTY = 0xffffffffu;
DI void wo_issue(const Out& o, int gr) { dma16(o.wor, o.woo, gr * 65536, o.lds + L_WO + o.wv * 1024 + gr * 8192); }
constexpr int L_DUMP = 163840 - 512, L_EPOCH = 163840 - 256;
DI u32x2 next_tag(lptr lds, int wv) {
  const u32x2 e = *(const volatile LAS u32x2*)(lds + L_EPOCH + wv * 8);
  const u64 t = (((u64)(unsigned)__builtin_amdgcn_readfirstlane(e.y) << 32) | (unsigned)__builtin_amdgcn_readfirstlane(e.x)) + 1;
  return u32x2{(unsigned)t, (unsigned)(t >> 32)};
}

constexpr int SLOT = 32768, NSLOT = 4;
constexpr int GA_EX = L_KVZ;
constexpr int GA_GW = GA_EX + 8 * 544 * 4;

DI u32x2 gemm_role(lptr lds, const u16* __restrict__ x, const u16* __restrict__ w, const u16* __restrict__ gw, char* ws,
                   int rb, int cg, int rot, const Out& wo STAMP_PARM) {
  const int tid = threadIdx.x, lane = tid & 63;
  const int wv = __builtin_amdgcn_readfirstlane(tid >> 6), rg = wv & 3, hp = wv >> 2;
  const u32x4 xr = mk_rsrc(x, 2048 * 1024), wr_ = mk_rsrc(w, 512 * 3072);
  const unsigned xrow = tid >> 3, xo = rb * 131072 + xrow * 1024 + (((tid & 7) ^ ((xrow >> 1) & 7)) << 4);
  const unsigned wrow = tid >> 4, wp = (tid & 15) ^ ((wrow & 3) << 2);
  const unsigned wo_ = cg * 128 + wrow * 3072 + 1024 + (wp >> 3) * 1024 + ((wp & 7) << 4);
  const unsigned qo = cg * 128 + xrow * 3072 + (((tid & 7) ^ (((xrow >> 1) & 1) << 2)) << 4);
  const lptr lw = lds + wv * 1024;
  auto issue = [&](int c, int i) {
    const lptr slot = lw + (c % NSLOT) * SLOT;
    if (c < 8) {
      const unsigned kc = (c + rot) & 7;
      if (i == 0) dma16(xr, xo, kc * 128, slot);
      if (i == 1) dma16(wr_, wo_, kc * (64 * 3072), slot + 16384);
      if (i == 2) dma16(xr, xo, kc * 128 + 65536, slot + 8192);
      if (i == 3) dma16(wr_, wo_, kc * (64 * 3072) + 32 * 3072, slot + 24576);
    } else {
      dma16(wr_, qo, (((c - 8) * 4 + i + rot) & 7) * (64 * 3072), slot + i * 8192);
    }
  };
  STAMP(0);
  const int gsel = hp == 0 ? ((cg - rot) & 7) : -1;
  if (hp == 0 && lane < 8) dma16(mk_rsrc(gw, 1024), lane * 16, cg * 128, lds + GA_GW + wv * 128);
#pragma unroll
  for (int c = 0; c < 3; ++c) {
#pragma unroll
    for (int i = 0; i < 4; ++i) issue(c, i);
    if (c == 0) STAMP(16);
    if (c < 2) bar();
  }
  const rsrc_t wsr = make_rsrc(ws, WS_USED);
  const unsigned pqo = WS_PQF + (((rb * 4 + rg) * 32 + cg * 4 + 2 * hp) * 64 + lane) * 16;
  const int h = lane >> 5, row = (rg >> 1) * 64 + sig(rg & 1, lane & 31);
  const int gi = lane >> 4, blk = gi & 1, q = (lane & 15) >> 2, p4 = lane & 3;
  lptr ap[4], bp[2];
#pragma unroll
  for (int s = 0; s < 4; ++s) ap[s] = lds + row * 128 + (((2 * s + h) ^ ((row >> 1) & 7)) << 4);
#pragma unroll
  for (int ct = 0; ct < 2; ++ct) bp[ct] = lds + 16384 + (8 * h + q) * 256 + (((4 * (2 * ct + hp) + 2 * blk + (p4 >> 1)) ^ (q << 2)) << 4) + (p4 & 1) * 8;
  f32x16 ak, av;
  float g0 = 0.f;
#pragma unroll
  for (int i = 0; i < 16; ++i) { ak[i] = 1e-6f; av[i] = 0.f; }
  bf16x8 xf[32], Bf[3][2];
  u32x2 tag;
  auto gate_steps = [&](int c) {
    if (gsel == c) {
      const lptr gp = lds + GA_GW + wv * 128 + h * 16;
      f32x16 ag = {};
#pragma unroll
      for (int i = 0; i < 4; ++i) ag = mfma32(lds_b128(gp + i * 32), xf[4 * c + i], ag);
      g0 = ag[0];
    }
    PIN(g0);
  };
  lptr qp = lds + (8 * h + q) * 128 + (((4 * hp + 2 * blk + (p4 >> 1)) ^ ((q >> 1) << 2)) << 4) + (p4 & 1) * 8;
  const int dp = lane & 31, e_ = lane & 15;
  lptr exw = lds + GA_EX + rg * 4352 + (2 * hp + (dp >> 4)) * 1024 + e_ * 64 + (((2 * h) ^ (e_ >> 2)) << 4);
  lptr ezw = lds + GA_EX + rg * 4352 + 4096 + (2 * hp + (dp >> 4)) * 64 + (((e_ >> 2) & 1) * 8 + (e_ & 3) + 4 * (e_ >> 3)) * 4;
  lptr exr = lds + GA_EX + (tid < 272 ? tid : 0) * 16;
  unsigned kpo = tid < 272 ? WS_KVP + (rb * 8 + cg) * 8704 + tid * 16 : 0xffff0000u;
  PIN(qp); PIN(exw); PIN(ezw); PIN(exr); PIN(kpo);
  f32x16 aq;
  STAMP(1);
#pragma unroll
  for (int n = 0; n < 32; ++n) {
    const int c = n >> 2, s = n & 3, so = (c & 1) * SLOT;
    if (s == 0) {
      if (c > 0) {
        lgkm0();
        if ((c & 1) == 0) {
          const int dlt = (c == 4) ? -2 * SLOT : 2 * SLOT;
#pragma unroll
          for (int i = 0; i < 4; ++i) { ap[i] += dlt; PIN(ap[i]); }
#pragma unroll
          for (int i = 0; i < 2; ++i) { bp[i] += dlt; PIN(bp[i]); }
        }
      }
      WAIT_VM(8);
      bar();
      if (c == 0) STAMP(2);
      SB;
    }
    if (n >= 2) { ak = mfma32(xf[n - 2], Bf[(n - 2) % 3][0], ak); SB; }
    xf[n] = lds_b128(ap[s] + so); Bf[n % 3][0] = lds_tr2(bp[0] + so + s * 4096, bp[0] + so + s * 4096 + 1024); SB;
    if (n >= 2) { av = mfma32(xf[n - 2], Bf[(n - 2) % 3][1], av); SB; }
    Bf[n % 3][1] = lds_tr2(bp[1] + so + s * 4096, bp[1] + so + s * 4096 + 1024); SB;
    if (c + 3 < 10) { issue(c + 3, s); SB; } else if (s < 3) { wo_issue(wo, s); SB; }
    if (n == 14) { tag = next_tag(lds, wv); SB; }
    if (n == 30) {
#pragma unroll
      for (int i = 0; i < 16; ++i) aq[i] = 1e-6f;
      PIN(aq); SB;
    }
    if (n > 0 && s == 0) { gate_steps(c - 1); SB; }
  }
  STAMP(3);
  f32x16 kv = {}, z = {};
  constexpr int PD = 5;
  bf16x8 Aq[PD + 1], ones;
#pragma unroll
  for (int i = 0; i < 8; ++i) ones[i] = (__bf16)1.0f;
  u32x4 pk[2], vv[2];
  auto rdq = [&](int m) { Aq[m % (PD + 1)] = lds_tr2(qp + m * 2048, qp + m * 2048 + 512); };
#pragma unroll
  for (int m = -2; m < 32; ++m) {
    if (m == -2) { lgkm0(); WAIT_VM(7); bar(); SB; rdq(0); rdq(1); SB; }
    if (m == 11) { lgkm0(); WAIT_VM(6); STAMP(4); bar(); SB; }
    if (m < 0) { ak = mfma32(xf[32 + m], Bf[(32 + m) % 3][0], ak); av = mfma32(xf[32 + m], Bf[(32 + m) % 3][1], av); SB; }
    else { aq = mfma32(Aq[m % (PD + 1)], xf[m], aq); SB; }
    if (m == -2) { rdq(2); SB; }
    if (m + PD < 32) { rdq(m + PD); SB; }
    if (m == 0) { vv[0] = __builtin_bit_cast(u32x4, pack8(av, 0)); vv[1] = __builtin_bit_cast(u32x4, pack8(av, 8)); PIN(vv[0]); PIN(vv[1]); SB; }
    if (m >= 1 && m < 9) {
      const int j = m - 1;
      unsigned p = pk_bf16(phi(ak[2 * j]), phi(ak[2 * j + 1]));
      PIN(p);
      pk[j >> 2][j & 3] = p;
      SB;
      if ((j & 3) == 3) {
        kv = mfma32(__builtin_bit_cast(bf16x8, pk[j >> 2]), __builtin_bit_cast(bf16x8, vv[j >> 2]), kv);
        z = mfma32(ones, __builtin_bit_cast(bf16x8, pk[j >> 2]), z);
        SB;
      }
      if (j == 1 || j == 4 || j == 6) { wo_issue(wo, 3 + j / 2 - (j == 1 ? 0 : 1)); SB; }
    }
    if (m == 10) {
      float sel[8];
#pragma unroll
      for (int i = 0; i < 8; ++i) { float a = kv[i], b = kv[8 + i]; PIN(a); PIN(b); sel[i] = dp < 16 ? a : b; }
      *(LAS f32x4*)exw = f32x4{sel[0], sel[1], sel[2], sel[3]};
      *(LAS f32x4*)((lptr)((unsigned)(size_t)exw ^ 16u)) = f32x4{sel[4], sel[5], sel[6], sel[7]};
      *(LAS float*)ezw = z[0];
      SB;
    }
    if (m == 11) {
      const LAS f32x4* ex = (const LAS f32x4*)exr;
      const u32x4 sum = __builtin_bit_cast(u32x4, (ex[0] + ex[272]) + (ex[544] + ex[816]));
      st128(wsr, kpo, u32x4{sum.x, tag.x, sum.y, tag.x});
      st128(wsr, kpo + 4352, u32x4{sum.z, tag.x, sum.w, tag.x});
      SB;
    }
    if (m == 13) { PIN(aq); gate_steps(7); SB; }
    if (m == 16 || m == 19) { wo_issue(wo, m == 16 ? 6 : 7); SB; }
  }
  PIN(aq);
  STAMP(5);
  {
    if (hp == 0) st32(wsr, h == 0 ? WS_G + cg * 8192 + (rb * 4 + rg) * 128 + lane * 4 : 0xfffffff0u, __float_as_uint(g0));
#pragma unroll
    for (int i = 0; i < 8; ++i) aq[i] = phi(aq[i]);
    st128(wsr, pqo, __builtin_bit_cast(u32x4, pack8(aq, 0)));
#pragma unroll
    for (int i = 8; i < 16; ++i) aq[i] = phi(aq[i]);
    st128(wsr, pqo + 1024, __builtin_bit_cast(u32x4, pack8(aq, 8)));
    STAMP(6);
    WAIT_VM(0);
  }
  STAMP(7);
  bar();
  STAMP(13);
  if (tid == 511) st64(wsr, WS_PF + (rb * 8 + cg) * 128, tag);
  return tag;
}

constexpr int SC_ROW = 2064;
constexpr int KC_CP = 4224, KC_CH = 2 * KC_CP;
constexpr int EX_BASE = 4 * KC_CH;
constexpr int U_BASE = 57 * SC_ROW, U_PAD = 224, U_CP = 4576, U_CH = 2 * U_CP;
constexpr int T_BASE = U_BASE + 4 * U_CH, T_CH = 1536;
static_assert(T_BASE + 4 * T_CH <= 163840 && U_BASE % 16 == 0, "lds");

DI f32x2 cexp_p(float l2, float rv) {
  const float m = __builtin_amdgcn_exp2f(l2), f = __builtin_amdgcn_fractf(rv);
  const f32x2 r = {m * __builtin_amdgcn_cosf(f), m * __builtin_amdgcn_sinf(f)};
  return r;
}
DI bf16x8 lds_2b64(lptr p) {
  const u32x2 a = *(const volatile LAS u32x2*)p, b = *(const volatile LAS u32x2*)(p + 8);
  const u32x4 v = {a.x, a.y, b.x, b.y};
  return __builtin_bit_cast(bf16x8, v);
}
DI unsigned perm(unsigned hi, unsigned lo, unsigned sel) { return __builtin_amdgcn_perm(hi, lo, sel); }
constexpr unsigned LL = 0x01000100, LH = 0x03020302, HL = 0x05040504, SWL = 0x01000302, SWH = 0x05040706, HL_LH = 0x03020504;
template <int E0, int E1, int E2, int E3>
DI void put4(unsigned base, f16x2 a, f16x2 b, f16x2 c, f16x2 d) {
  constexpr int O = E0 < 28 ? 0 : 28;
  static_assert((E1 < 28) == (E0 < 28) && (E2 < 28) == (E0 < 28) && (E3 < 28) == (E0 < 28), "one base");
  asm volatile("s_mov_b32 m0, %4\n\ts_nop 0\n\tds_write_addtid_b32 %0 offset:%5\n\tds_write_addtid_b32 %1 offset:%6\n\t"
               "ds_write_addtid_b32 %2 offset:%7\n\tds_write_addtid_b32 %3 offset:%8"
               :: "v"(a), "v"(b), "v"(c), "v"(d), "s"(base + O * SC_ROW), "i"((E0 - O) * SC_ROW), "i"((E1 - O) * SC_ROW),
                  "i"((E2 - O) * SC_ROW), "i"((E3 - O) * SC_ROW) : "memory");
}

DI u32x2 conv_role(lptr lds, const u16* __restrict__ x, const u16* __restrict__ lar, const u16* __restrict__ aim,
                   const u16* __restrict__ cre, const u16* __restrict__ cim, const u16* __restrict__ ldt,
                   const u16* __restrict__ dsk, char* ws, int ch0, int jrot, const Out& wo STAMP_PARM) {
  const int tid = threadIdx.x, lane = tid & 63;
  const int wv = __builtin_amdgcn_readfirstlane(tid >> 6), cl = wv & 3, half = wv >> 2;
  const int d = ch0 + cl;
  STAMP(0);
  const unsigned r_lar = ((const unsigned*)lar)[d * 64 + lane], r_aim = ((const unsigned*)aim)[d * 64 + lane];
  const unsigned r_cr = ((const unsigned*)cre)[d * 64 + lane], r_ci = ((const unsigned*)cim)[d * 64 + lane];
  const unsigned s_ldt = ((const CAS unsigned*)ldt)[d >> 1], s_dsk = ((const CAS unsigned*)dsk)[d >> 1];
  const int j = lane & 15, g = lane >> 4;
  const rsrc_t wsr = make_rsrc(ws, WS_USED);
  bar();
  const int cj = ch0 >> 2, cg = cj >> 4, rb = cj & 15;
  const unsigned kvo = wv == 4 && lane < 9 ? WS_KVZ + cj * 256 + lane * 16 : 0xffffffe0u;
  const int tt = (tid + jrot * 64) & 511;
  const char* xg = (const char*)x + ch0 * 2;
  const unsigned xoff = tt * 4096;
  u32x2 ur[4];
  ur[0] = *(const u32x2*)(xg + xoff); ur[1] = *(const u32x2*)(xg + xoff + 1024);
  u32x2 up;
  {
    const u32x4 zero = {0, 0, 0, 0};
    if (lane < 28) *(LAS u32x4*)(lds + U_BASE + cl * U_CH + half * U_CP + lane * 16) = zero;
  }
  const unsigned scb = (unsigned)(size_t)lds + cl * 512 + half * 256;
  const lptr fb = lds + cl * 512 + half * 256 + j * SC_ROW + g * 16;
  unsigned selA = lane < 32 ? LL : lane == 32 ? HL : SWH, selB = lane < 32 ? HL_LH : SWH, selC = lane < 32 ? LL : SWH, selE = lane < 32 ? LL : SWL;
  if (half == 0) { const bool odd = !(lane & 1); selA = odd ? LH : LL; selB = odd ? HL : LH; selC = odd ? HL : SWL; }
  PIN(selA); PIN(selB); PIN(selC); PIN(selE);
  const unsigned shl = half ? 0 : 16;
  const float f_ldt = (d & 1) ? bfhi(s_ldt) : bf2f(s_ldt), f_dsk = (d & 1) ? bfhi(s_dsk) : bf2f(s_dsk);
  const float dt = __int_as_float(__builtin_amdgcn_readfirstlane(__float_as_int(rbf(__expf(f_ldt)))));
  SB;
  const float l2 = -rbf(__builtin_amdgcn_exp2f(bfhi(r_lar << shl) * LOG2E)) * dt * LOG2E;
  const float rv = bfhi(r_aim << shl) * dt * 0.15915494309189535f;
  unsigned cm;
  {
    typedef __attribute__((ext_vector_type(2))) unsigned short u16x2;
    const u16x2 m2 = __builtin_elementwise_max(__builtin_bit_cast(u16x2, r_cr & 0x7fff7fffu), __builtin_bit_cast(u16x2, r_ci & 0x7fff7fffu));
    const unsigned m = __builtin_bit_cast(unsigned, m2);
    cm = umax(wave_umax(umax(m << 16, m)), 0x0da24260u);
  }
  STAMP(14);
  const float cmax = __uint_as_float(cm), cinv = __builtin_amdgcn_rcpf(cmax);
  f32x4 kt[3];
  {
    f16x8 rf[4], t0[4];
    const f16x2 one = {(_Float16)1.f, (_Float16)0.f};
    const f32x2 E1 = cexp_p(l2, rv), E8 = cexp_p(l2 * 8.f, rv * 8.f), E128 = cexp_p(l2 * 128.f, rv * 128.f);
    f32x2 E2, E3, E4, E16, E24, E32, E64, E96, E256, E384, E512, H1, H2, H3, H4, dummy;
    const f32x2 H0 = {bfhi(r_cr << shl) * cinv, bfhi(r_ci << shl) * cinv};
    cmul2(E16, E8, E8, E256, E128, E128);
    cmul2(E32, E16, E16, E512, E256, E256);
    cmul2(E24, E16, E8, E2, E1, E1);
    cmul2(E64, E32, E32, H1, H0, E512);
    cmul2(E3, E2, E1, E4, E2, E2);
    cmul2(E96, E64, E32, H2, H1, E512);
    cmul2(E384, E256, E128, H3, H2, E512);
    {
      const f16x2 P1 = pk_f16c(E8), P2 = pk_f16c(E16), P3 = pk_f16c(E24), Q1 = pk_f16c(E32), Q2 = pk_f16c(E64), Q3 = pk_f16c(E96);
      put4<0, 1, 2, 3>(scb, one, P1, P2, P3);
      put4<4, 5, 6, 7>(scb, Q1, cmulh(Q1, P1), cmulh(Q1, P2), cmulh(Q1, P3));
      put4<8, 9, 10, 11>(scb, Q2, cmulh(Q2, P1), cmulh(Q2, P2), cmulh(Q2, P3));
      put4<12, 13, 14, 15>(scb, Q3, cmulh(Q3, P1), cmulh(Q3, P2), cmulh(Q3, P3));
    }
    SB;
    ur[2] = *(const u32x2*)(xg + xoff + 2048);
    SB;
    H4 = cmul(H3, E512); (void)dummy;
    const f16x2 e1 = pk_f16(E1), e2 = pk_f16(E2), e3 = pk_f16(E3), e4 = pk_f16(E4);
    const f16x2 e5 = cmulh(e4, e1), e6 = cmulh(e4, e2), e7 = cmulh(e4, e3);
    const f16x2 S1 = pk_f16(E128), S2 = pk_f16(E256), S3 = pk_f16(E384);
    const f16x2 h0 = pk_f16(H0), h1 = pk_f16(H1), h2 = pk_f16(H2), h3 = pk_f16(H3);
    {
      const f16x2 G1 = cmulh(h0, S1);
      put4<16, 17, 18, 19>(scb, h0, cmulh(h0, e1), cmulh(h0, e2), cmulh(h0, e3));
      put4<20, 21, 22, 23>(scb, cmulh(h0, e4), cmulh(h0, e5), cmulh(h0, e6), cmulh(h0, e7));
      put4<24, 25, 26, 27>(scb, G1, cmulh(G1, e1), cmulh(G1, e2), cmulh(G1, e3));
      put4<28, 29, 30, 31>(scb, cmulh(G1, e4), cmulh(G1, e5), cmulh(G1, e6), cmulh(G1, e7));
    }
#pragma unroll
    for (int s = 0; s < 4; ++s) { rf[s] = *(const LAS f16x8*)(fb + s * 64); t0[s] = *(const LAS f16x8*)(fb + 16 * SC_ROW + s * 64); }
    SB;
    ur[3] = *(const u32x2*)(xg + xoff + 3072);
    up = __builtin_amdgcn_raw_buffer_load_b64(make_rsrc((void*)x, 2048 * 1024), lane == 0 && tt > 0 ? ch0 * 2 + xoff - 1024 : 0xfffffff0u, 0, 0);
    SB;
    {
      const f16x2 G2 = cmulh(h0, S2), G3 = cmulh(h0, S3), G5 = cmulh(h1, S1), G6 = cmulh(h1, S2), G7 = cmulh(h1, S3);
      put4<32, 33, 34, 35>(scb, G2, cmulh(G2, e2), cmulh(G2, e4), cmulh(G2, e6));
      kt[0] = mfma16h(t0[0], rf[0], f32x4{0.f, 0.f, 0.f, 0.f});
      kt[0] = mfma16h(t0[1], rf[1], kt[0]);
      put4<36, 37, 38, 39>(scb, G3, cmulh(G3, e2), cmulh(G3, e4), cmulh(G3, e6));
      kt[0] = mfma16h(t0[2], rf[2], kt[0]);
      put4<40, 41, 42, 43>(scb, h1, cmulh(h1, e4), G5, cmulh(G5, e4));
      kt[0] = mfma16h(t0[3], rf[3], kt[0]);
      put4<44, 45, 46, 47>(scb, G6, cmulh(G6, e4), G7, cmulh(G7, e4));
    }
#pragma unroll
    for (int s = 0; s < 4; ++s) t0[s] = *(const LAS f16x8*)(fb + 32 * SC_ROW + s * 64);
    SB;
    put4<48, 49, 50, 51>(scb, h2, cmulh(h2, S1), cmulh(h2, S2), cmulh(h2, S3));
    put4<52, 53, 54, 55>(scb, h3, cmulh(h3, S1), cmulh(h3, S2), cmulh(h3, S3));
    *(LAS f16x2*)(lds + cl * 512 + half * 256 + lane * 4 + 56 * SC_ROW) = pk_f16(H4);
    f16x8 t2[4];
#pragma unroll
    for (int s = 0; s < 4; ++s) t2[s] = *(const LAS f16x8*)(fb + 48 * SC_ROW + s * 64);
    SB;
    kt[1] = f32x4{0.f, 0.f, 0.f, 0.f}; kt[2] = kt[1];
#pragma unroll
    for (int s = 0; s < 4; ++s) kt[1] = mfma16h(t0[s], rf[s], kt[1]);
#pragma unroll
    for (int s = 0; s < 4; ++s) kt[2] = mfma16h(t2[s], rf[s], kt[2]);
  }
  STAMP(15);
  const lptr Tb = lds + T_BASE + cl * T_CH;
  {
    LAS f32x4* xw = (LAS f32x4*)(lds + cl * 512 + half * 256 + g * SC_ROW) + j;
    const LAS f32x4* xr = (const LAS f32x4*)(lds + cl * 512 + (half ^ 1) * 256 + g * SC_ROW) + j;
    if (half) xw[0] = kt[0]; else { xw[0] = kt[1]; xw[4 * SC_ROW / 16] = kt[2]; }
    const float ks = 2.f * dt * cmax;
    const lptr p1 = half ? Tb + (256 + 64 * g + (g < 2 ? 4 : 2) * j) * 2 : Tb + (128 * (g >> 1) + 4 * (g & 1) + 8 * j) * 2;
    lptr p1b = p1 + (g < 2 ? 4 : 64);
    PIN(p1b);
    lgkm0();
    STAMP(3);
    bar();
    if (half == 0) {
      f32x4 a0 = (kt[0] + xr[0]) * ks;
      if (lane == 0) a0[0] += f_dsk;
      *(LAS u32x2*)p1 = u32x2{pk_bf16(a0[0], a0[1]), pk_bf16(a0[2], a0[3])};
    } else {
      const f32x4 a1 = (kt[1] + xr[0]) * ks, a2 = (kt[2] + xr[4 * SC_ROW / 16]) * ks;
      *(LAS unsigned*)p1 = pk_bf16(a1[0], a1[1]);
      *(LAS unsigned*)p1b = pk_bf16(a1[2], a1[3]);
      const unsigned q0 = pk_bf16(a2[0], a2[1]), q1 = pk_bf16(a2[2], a2[3]);
      LAS u16* p2 = (LAS u16*)Tb + 512 + 64 * g + j;
      p2[0] = (u16)q0; p2[16] = (u16)(q0 >> 16); p2[32] = (u16)q1; p2[48] = (u16)(q1 >> 16);
    }
  }
  lgkm0();
  STAMP(4);
  bar();
  {
    const lptr kc = lds + cl * KC_CH + lane * 16;
    auto emit = [&](int Y0, unsigned P0, unsigned P1, unsigned P2, unsigned P3, unsigned P4) {
      *(LAS u32x4*)(kc + Y0 * 16) = u32x4{P0, P1, P2, P3};
      *(LAS u32x4*)(kc + KC_CP + Y0 * 16) = u32x4{P1, P2, P3, P4};
    };
    if (half == 0) {
      const LAS unsigned* p = (const LAS unsigned*)(Tb + ((575 - lane) >> 1) * 4);
      const unsigned D0 = p[32], D1 = p[33], F0 = p[0], F1 = p[1], Z = *(const LAS unsigned*)Tb;
      SB;
      const unsigned e1 = perm(D1, D0, selB), e0 = perm(D1, D0, selA), f1 = perm(F1, F0, selB), f0 = perm(F1, F0, selA);
      emit(0, e1, e1, perm(D1, D0, selC), e0, e0);
      emit(64, f1, f1, perm(F1, F0, selC), f0, f0);
      if (lane < 4) emit(256, lane == 0 ? perm(0, Z, SWL) : 0u, 0u, 0u, 0u, 0u);
    } else {
      const LAS unsigned* p = (const LAS unsigned*)(Tb + (255 - lane) * 4);
      const LAS unsigned* r = (const LAS unsigned*)(Tb + (lane < 32 ? 760 - 8 * lane : 1008 - 16 * lane));
      const unsigned F0 = p[0], F1 = p[1], D0 = r[0], D1 = r[1], D2 = r[2], D3 = r[3], D4 = r[4];
      SB;
      const unsigned e2 = perm(F1, F0, HL);
      emit(128, e2, e2, perm(F1, F0, LH), perm(F1, F0, SWL), perm(F1, F0, LL));
      emit(192, perm(D4, D2, selA), perm(lane < 32 ? D2 : D3, D1, selB), perm(D2, D1, selC), perm(D1, D0, selB), perm(D1, D0, selE));
    }
  }
  SB;
  {
    const u32x2 pv = {(unsigned)__builtin_amdgcn_update_dpp((int)up.x, (int)ur[3].x, 0x138, 0xf, 0xf, false),
                      (unsigned)__builtin_amdgcn_update_dpp((int)up.y, (int)ur[3].y, 0x138, 0xf, 0xf, false)};
    const lptr ub = lds + U_BASE + (U_PAD + 4 * tt) * 2;
#pragma unroll
    for (int c = 0; c < 4; ++c) {
      const unsigned sel = (c & 1) ? 0x07060302 : 0x05040100;
      const unsigned p = pv[c >> 1], r0 = ur[0][c >> 1], r1 = ur[1][c >> 1], r2 = ur[2][c >> 1], r3 = ur[3][c >> 1];
      *(LAS u32x2*)(ub + c * U_CH) = u32x2{perm(r1, r0, sel), perm(r3, r2, sel)};
      *(LAS u32x2*)(ub + c * U_CH + U_CP) = u32x2{perm(r0, p, sel), perm(r2, r1, sel)};
    }
  }
  const u32x2 tag = next_tag(lds, wv);
  lgkm0();
  STAMP(5);
  bar();
  const u32x4 wsl = mk_rsrc(ws, WS_USED);
  const int a_ = j;
  const lptr ab = lds + cl * KC_CH + ((a_ & 1) ? KC_CP + 4088 : 4096) - 8 * (a_ >> 1) + 16 * g - 3584 - 192 - half * 256;
  const lptr bb = lds + U_BASE + cl * U_CH + ((a_ & 1) ? 0 : U_CP) + (U_PAD + 32 * (a_ >> 1) + 8 * g) * 2 - 192 - half * 256;
  f32x4 acc[8];
#pragma unroll
  for (int i = 0; i < 8; ++i) acc[i] = f32x4{0.f, 0.f, 0.f, 0.f};
  LAS f32x4* exw = (LAS f32x4*)(lds + EX_BASE + wv * 4096) + lane;
  f32x4 keep[4];
  {
    u32x2 Al[2][8], Ah[2][8];
    bf16x8 B[2][8];
    auto rd = [&](int r, int k) {
      const int nb = r & 1, mu = k / 3, w = k % 3;
      if (w == 0) B[nb][mu] = lds_b128(bb + 512 * mu + 64 * (3 - r));
      if (w == 1) Al[nb][mu] = *(const volatile LAS u32x2*)(ab + 512 * (7 - mu) + 64 * (3 - r));
      if (w == 2) Ah[nb][mu] = *(const volatile LAS u32x2*)(ab + 512 * (7 - mu) + 64 * (3 - r) + 8);
    };
    auto AF = [&](int nb, int mu) { return __builtin_bit_cast(bf16x8, u32x4{Al[nb][mu].x, Al[nb][mu].y, Ah[nb][mu].x, Ah[nb][mu].y}); };
#pragma unroll
    for (int k = 0; k < 24; ++k) rd(0, k);
    SB;
#pragma unroll
    for (int r = 1; r < 4; ++r) {
      const int cb = (r - 1) & 1;
      int cnt = 0;

#pragma unroll
      for (int m = 0; m < 8; ++m)
#pragma unroll
        for (int n = 0; n <= m; ++n)
#pragma unroll
          for (int sw = 0; sw < 2; ++sw)
            if (m + n < 8 && (sw == 0 || n < m)) {
              const int mu = sw ? n : m, nu = sw ? m : n;
              acc[m + n] = mfma16(AF(cb, mu), B[cb][nu], acc[m + n]);
              SB;
              if (cnt % 3 != 2) rd(r, cnt - cnt / 3);
              else if (cnt == 14 || cnt == 29) wo_issue(wo, 2 * (r - 1) + (cnt == 29));
              else if ((cnt == 5 || cnt == 20) && r == 1) st128(wsr, WS_BT + ch0 * 4096 + (cnt == 5 ? 0 : 8192) + tid * 16, u32x4{EMPTY, EMPTY, EMPTY, EMPTY});
              else if (cnt == 2 && r == 1) st128(wsr, kvo, u32x4{EMPTY, EMPTY, EMPTY, EMPTY});
              else if (cnt == 2 && r == 3) { WAIT_VM(3); st64(wsr, lane == 0 ? WS_FLAG + (cj * 8 + wv) * 8 : 0xfffffff0u, tag); }
              SB;
              ++cnt;
            }
    }
    auto tiles = [&](int t0) {
#pragma unroll
      for (int mu = 0; mu < 8; ++mu)
#pragma unroll
        for (int n = 0; n < 8; ++n) if (mu + n >= t0 && mu + n < t0 + 4) acc[mu + n] = mfma16(AF(1, mu), B[1][n], acc[mu + n]);
    };
    if (half) {
      tiles(0); SB; tiles(4);
#pragma unroll
      for (int i = 0; i < 4; ++i) { exw[64 * i] = acc[i]; keep[i] = acc[4 + i]; }
    } else {
      tiles(4); SB; tiles(0);
#pragma unroll
      for (int i = 0; i < 4; ++i) { exw[64 * i] = acc[4 + i]; keep[i] = acc[i]; }
    }
  }
  STAMP(6);
  {
    const LAS f32x4* exr = (const LAS f32x4*)(lds + EX_BASE + (wv ^ 4) * 4096) + lane;
    const unsigned bto = WS_BT + d * 4096 + half * 2048 + (g & 1) * 512 + (a_ >> 2) * 128 + (g >> 1) * 64 + (a_ & 3) * 16;
    u32x4 bt[2], k[4];
    constexpr unsigned DONE = 0xffffffe0u;
    const unsigned src = WS_KVP + (j * 8 + cg) * 8704;
    const unsigned ko = src + (rb >> 2) * 1024 + ((rb & 3) * 4 + cl) * 64 + ((g ^ (rb & 3)) << 4);
    const bool zl = wv == 4 && g == 0;
    unsigned ka[4] = {half ? ko : DONE, half ? ko + 4352 : DONE, zl ? src + 4096 + rb * 16 : DONE, zl ? src + 4352 + 4096 + rb * 16 : DONE};
#pragma unroll
    for (int i = 0; i < 4; ++i) k[i] = ld128c(wsl, ka[i]);
    lgkm0();
    STAMP(7);
    bar();
#pragma unroll
    for (int i = 0; i < 4; i += 2) {
      const f32x4 v = keep[i] + exr[64 * i], w = keep[i + 1] + exr[64 * i + 64];
      const auto sx = __builtin_amdgcn_permlane16_swap(umin(pk_bf16(v[0], v[1]), EMPTY - 1), umin(pk_bf16(w[0], w[1]), EMPTY - 1), false, false);
      const auto sy = __builtin_amdgcn_permlane16_swap(umin(pk_bf16(v[2], v[3]), EMPTY - 1), umin(pk_bf16(w[2], w[3]), EMPTY - 1), false, false);
      bt[i >> 1] = u32x4{sx[0], sy[0], sx[1], sy[1]};
      PIN(bt[i >> 1]);
    }
    wo_issue(wo, 6); wo_issue(wo, 7);
    STAMP(2);
    unsigned spin = 0;
    WAIT_VM(2);
    PIN(k[0]); PIN(k[1]); PIN(k[2]); PIN(k[3]);
    const lptr slice = lds + T_BASE;
    if (half == 0) { st128(wsr, bto, bt[0]); st128(wsr, bto + 1024, bt[1]); }
    else {
      unsigned val[8] = {0, 0, 0, 0, 0, 0, 0, 0};
#pragma unroll 1
      for (; spin < SPIN_CAP; ++spin) {
#pragma unroll
        for (int i = 0; i < 4; ++i) {
          const bool got = k[i].y == tag.x && k[i].w == tag.x && ka[i] != DONE;
          val[2 * i] = got ? k[i].x : val[2 * i]; val[2 * i + 1] = got ? k[i].z : val[2 * i + 1];
          ka[i] = got ? DONE : ka[i];
        }
        if (__all((ka[0] & ka[1] & ka[2] & ka[3]) == DONE)) break;
        asm volatile("buffer_load_dwordx4 %0, %4, %8, 0 offen sc1\n\tbuffer_load_dwordx4 %1, %5, %8, 0 offen sc1\n\t"
                     "buffer_load_dwordx4 %2, %6, %8, 0 offen sc1\n\tbuffer_load_dwordx4 %3, %7, %8, 0 offen sc1\n\ts_waitcnt vmcnt(0)"
                     : "=&v"(k[0]), "=&v"(k[1]), "=&v"(k[2]), "=&v"(k[3])
                     : "v"(ka[0]), "v"(ka[1]), "v"(ka[2]), "v"(ka[3]), "s"(wsl) : "memory");
      }
      auto rowsum = [&](unsigned u) { float v = __uint_as_float(u); v += dppf<0x111>(v); v += dppf<0x112>(v); v += dppf<0x114>(v); v += dppf<0x118>(v); return v; };
      const u32x2 kvs = {umin(pk_bf16(rowsum(val[0]), rowsum(val[1])), EMPTY - 1), umin(pk_bf16(rowsum(val[2]), rowsum(val[3])), EMPTY - 1)};
      if (j == 15) *(LAS u32x2*)(slice + (cl * 4 + g) * 8) = kvs;
      if (wv == 4) {
        const u32x2 zs = {umin(pk_bf16(rowsum(val[4]), rowsum(val[5])), EMPTY - 1), umin(pk_bf16(rowsum(val[6]), rowsum(val[7])), EMPTY - 1)};
        if (lane == 15) *(LAS u32x4*)(slice + 128) = u32x4{zs.x, zs.y, 0, 0};
      }
    }
    lgkm0();
    bar();
    if (half) {
      if (wv == 4) st128(wsr, kvo, *(const LAS u32x4*)(slice + (lane < 9 ? lane : 0) * 16));
      st128(wsr, bto, bt[0]); st128(wsr, bto + 1024, bt[1]);
    }
    STAMP_VAL(3, st_[0] + spin);
    STAMP(13);
    return tag;
  }
}

DI void out_phase(lptr lds, char* ws, u16* __restrict__ out, int rt, int ct, u32x2 tag STAMP_PARM) {
  const int tid = threadIdx.x, lane = tid & 63;
  const int wv = __builtin_amdgcn_readfirstlane(tid >> 6), wr = wv & 1, wc = (wv >> 1) & 1, par = wv >> 2;
  const u32x4 wsl = mk_rsrc(ws, WS_USED);
  const int h = lane >> 5, t = lane & 31, trow = rt * 64 + sig(wr, t);
  const unsigned bo = WS_BT + (ct * 64 + wv * 8 + (lane >> 3)) * 4096 + rt * 128 + (((lane & 7) ^ (((lane >> 4) & 1) << 2)) << 4);
  const unsigned fo = lane < 8 ? WS_PF + ((rt >> 1) * 8 + lane) * 128 : lane < 24 ? WS_FLAG + 8 * ((ct * 16 + 2 * wv) * 8 + lane - 8)
                    : lane < 40 ? WS_FLAG + 8 * ((wv * 16 + lane - 24) * 8 + 4) : 0xfffffff0u;
  unsigned tries = 0;
  unsigned gp[4];
  const unsigned go = WS_G + h * 32768 + (rt * 2 + wr) * 128 + t * 4;
  bool b_arm;
#pragma unroll 1
  for (unsigned spin = 0; spin < SPIN_CAP; ++spin) {
    const u32x2 f = poll64(wsl, fo);
    const u64 bad = __ballot(f.x != tag.x || f.y != tag.y);
    ++tries;
    b_arm = (bad & 0xffff00ull) == 0;
    if ((bad & 0xffff0000ffull) == 0) break;
    __builtin_amdgcn_s_sleep(2);
  }
  STAMP(8);
  auto pq_piece = [&](int g) {
    dma16c(wsl, lane * 16, WS_PQF + rt * 65536 + (wv & 1) * 32768 + (wv >> 1) * 1024 + g * 4096, lds + L_PQ + (wv & 1) * 32768 + (wv >> 1) * 1024 + g * 4096);
  };
  {
    constexpr unsigned DONE = 0xffffffe0u;
    const unsigned ko = WS_KVZ + wv * 4096 + (lane >> 3) * 256 + (lane & 7) * 16;
    unsigned ka[3] = {ko, ko + 2048, lane < 16 ? WS_KVZ + wv * 4096 + lane * 256 + 128 : DONE};
    const lptr kl = lds + L_KVZ + wv * 2176 + (lane >> 5) * 544 + (lane & 31) * 16, kz = lds + L_KVZ + wv * 2176 + (lane >> 2) * 544 + 512 + (lane & 3) * 8;
    u32x4 k[3];
#pragma unroll
    for (int i = 0; i < 3; ++i) k[i] = ld128c(wsl, ka[i]);
    pq_piece(0); pq_piece(1);
    WAIT_VM(2);
    PIN(k[0]); PIN(k[1]); PIN(k[2]);
#pragma unroll 1
    for (unsigned spin = 0; spin < SPIN_CAP; ++spin) {
#pragma unroll
      for (int i = 0; i < 3; ++i) {
        if (umax4(0, k[i]) != EMPTY && ka[i] != DONE) {
          if (i < 2) *(LAS u32x4*)(kl + i * 1088) = k[i]; else *(LAS u32x2*)kz = u32x2{k[i].x, k[i].y};
          ka[i] = DONE;
        }
      }
      tries += 256;
      if (__all((ka[0] & ka[1] & ka[2]) == DONE)) break;
      __builtin_amdgcn_s_sleep(3);
      asm volatile("buffer_load_dwordx4 %0, %3, %6, 0 offen sc1\n\tbuffer_load_dwordx4 %1, %4, %6, 0 offen sc1\n\t"
                   "buffer_load_dwordx4 %2, %5, %6, 0 offen sc1\n\ts_waitcnt vmcnt(0)"
                   : "=&v"(k[0]), "=&v"(k[1]), "=&v"(k[2]) : "v"(ka[0]), "v"(ka[1]), "v"(ka[2]), "s"(wsl) : "memory");
    }
  }
  pq_piece(2); pq_piece(3);
  STAMP(9);
  STAMP_VAL(1, st_[0] + tries);
  const int gi = lane >> 4, blk = gi & 1, q = (lane & 15) >> 2, p4 = lane & 3;
  const lptr kvb = lds + L_KVZ + (t < 16 ? t : 16) * 32 + h * 16 + par * 1088;
  const lptr pqb = lds + L_PQ + wr * 32768 + lane * 16 + par * 2048;
  const lptr wob = lds + L_WO + (4 * h + q) * 128 + (((4 * wc + 2 * blk + (p4 >> 1)) ^ ((q >> 1) << 2)) << 4) + (p4 & 1) * 8 + par * 4096;
  f32x16 acc = {};
  f32x16 nd[2];
  bf16x8 wf[2], kf[3], pf[3];
  lgkm0();
#pragma unroll
  for (int n = 0; n < 19; ++n) {
    const int a = n - 2, b = n - 3;
    if (n == 0) { WAIT_VM(2); bar(); STAMP(10); SB; }
    if (n == 4) { WAIT_VM(4); bar(); SB; }
    if (n == 8) { WAIT_VM(2); bar(); SB; }
    if (n == 12) { WAIT_VM(5); bar(); SB; }
    if (a >= 0 && a < 16) { const f32x16 zero = {}; nd[a & 1] = mfma32(kf[a % 3], pf[a % 3], zero); SB; }
    if (n < 16) { kf[n % 3] = lds_b128(kvb + (n >> 1) * 2176 + (n & 1) * 544); pf[n % 3] = lds_b128(pqb + (n >> 1) * 4096 + (n & 1) * 1024); SB; }
    if (b >= 0) {
      f32x16& v = nd[b & 1];
      PIN(v);
      const float r = __builtin_amdgcn_rcpf(v[8] + 1e-6f);
      float o[8];
#pragma unroll
      for (int e = 0; e < 8; ++e) { o[e] = v[e] * r; PIN(o[e]); }
      const u32x4 p = {pk_bf16(o[0], o[1]), pk_bf16(o[2], o[3]), pk_bf16(o[4], o[5]), pk_bf16(o[6], o[7])};
      acc = mfma32(wf[b & 1], __builtin_bit_cast(bf16x8, p), acc);
      SB;
    }
    if (a >= 0 && a < 16) { wf[a & 1] = lds_tr2(wob + (a >> 1) * 8192 + (a & 1) * 2048, wob + (a >> 1) * 8192 + (a & 1) * 2048 + 1024); SB; }
    if (n < 4) { pq_piece(n + 4); SB; }
    if (n == 9) { dma16c(wsl, bo, 0, lds + L_BT + wv * 1024); SB; }
    if (n == 10 || n == 11) { gp[2 * n - 20] = ld32c(wsl, go + (2 * n - 20) * 8192); gp[2 * n - 19] = ld32c(wsl, go + (2 * n - 19) * 8192); SB; }
  }
  PIN(acc);
  STAMP(11);
  WAIT_VM(0);
  PIN(gp[0]); PIN(gp[1]); PIN(gp[2]); PIN(gp[3]);
  const unsigned gs = __float_as_uint((__uint_as_float(gp[0]) + __uint_as_float(gp[1])) + (__uint_as_float(gp[2]) + __uint_as_float(gp[3])));
  const auto gw2 = __builtin_amdgcn_permlane32_swap(gs, gs, false, false);
  float gt = __builtin_amdgcn_rcpf(1.f + __builtin_amdgcn_exp2f(-(__uint_as_float(gw2[0]) + __uint_as_float(gw2[1])) * LOG2E));
  PIN(gt);
#pragma unroll 1
  for (unsigned spin = 0; spin < SPIN_CAP; ++spin) {
    const unsigned m = umax4(0, *(const volatile LAS u32x4*)(lds + L_BT + wv * 1024 + lane * 16));
    if (b_arm && __all(m != EMPTY)) break;
    __builtin_amdgcn_s_sleep(2);
    if (!b_arm) { const u32x2 f = poll64(wsl, fo); b_arm = (__ballot(f.x != tag.x || f.y != tag.y) & 0xffff00ull) == 0; }
    dma16c(wsl, bo, 0, lds + L_BT + wv * 1024);
    WAIT_VM(0);
  }
  {
    LAS f32x4* xw = (LAS f32x4*)(lds + L_PQ + wv * 2048) + lane;
    const LAS f32x4* xr = (const LAS f32x4*)(lds + L_PQ + (wv ^ 4) * 2048) + lane;
    f32x4 keep[2];
#pragma unroll
    for (int G = 0; G < 2; ++G) {
      const f32x4 lo = {acc[4 * G], acc[4 * G + 1], acc[4 * G + 2], acc[4 * G + 3]};
      const f32x4 hi = {acc[8 + 4 * G], acc[9 + 4 * G], acc[10 + 4 * G], acc[11 + 4 * G]};
      if (par) { xw[64 * G] = lo; keep[G] = hi; } else { xw[64 * G] = hi; keep[G] = lo; }
    }
    const lptr bb = lds + L_BT + (wc * 32 + par * 16 + 4 * h + q) * 128 + (((4 * wr + 2 * blk + (p4 >> 1)) ^ ((q >> 1) << 2)) << 4) + (p4 & 1) * 8;
    lgkm0();
    bar();
    const u32x2 bv0 = __builtin_bit_cast(u32x2, lds_tr(bb)), bv1 = __builtin_bit_cast(u32x2, lds_tr(bb + 1024));
    u32x2 o[2];
#pragma unroll
    for (int G = 0; G < 2; ++G) {
      const f32x4 a = keep[G] + xr[64 * G];
      const unsigned b0 = G ? bv1.x : bv0.x, b1 = G ? bv1.y : bv0.y;
      const float bf[4] = {bf2f(b0), bfhi(b0), bf2f(b1), bfhi(b1)};
      float r[4];
#pragma unroll
      for (int e = 0; e < 4; ++e) r[e] = bf[e] + gt * (a[e] - bf[e]);
      o[G] = u32x2{pk_bf16(r[0], r[1]), pk_bf16(r[2], r[3])};
    }
    char* dst = (char*)out + (unsigned)(trow * 1024 + ct * 128 + wc * 64 + par * 32 + h * 16);
    auto sx = __builtin_amdgcn_permlane32_swap(o[0].x, o[1].x, false, false);
    auto sy = __builtin_amdgcn_permlane32_swap(o[0].y, o[1].y, false, false);
    const u32x4 v = {sx[0], sy[0], sx[1], sy[1]};
    *(u32x4*)dst = v;
  }
  if (blockIdx.x == 0 && tid == 0) st64(make_rsrc(ws, WS_USED), WS_EPOCH, tag);
}

__global__ void __launch_bounds__(512, 2) fused(const u16* __restrict__ x, const u16* __restrict__ wqkv, const u16* __restrict__ gw,
                                                const u16* __restrict__ lar, const u16* __restrict__ aim, const u16* __restrict__ cre,
                                                const u16* __restrict__ cim, const u16* __restrict__ ldt, const u16* __restrict__ dsk,
                                                const u16* __restrict__ wo, char* ws, u16* __restrict__ out
) {
  STAMP_DECL;
  st_[0] = __builtin_amdgcn_s_memrealtime();
  st_[1] = st_[0];
  extern __shared__ __attribute__((aligned(16))) char lds_[];
  const lptr lds = (lptr)lds_;
  const int b = blockIdx.x, xcd = b & 7, idx = b >> 3, tid = threadIdx.x, lane = tid & 63;
  const int wv = __builtin_amdgcn_readfirstlane(tid >> 6);
  if (wv == 7) CODE_PREFETCH("fused", lds + L_DUMP, ((idx & 7) * 64 + lane) * 64);
  if ((tid & 63) < 2) dma4(mk_rsrc(ws, WS_USED), WS_EPOCH + (tid & 63) * 4, lds + L_EPOCH + wv * 8);
  const int rt = (xcd >> 1) * 8 + (idx & 7), ct = (xcd & 1) * 4 + (idx >> 3);
  const Out o = {mk_rsrc(wo, 512 * 1024), (unsigned)(ct * 128 + (tid >> 3) * 1024 + (((tid & 7) ^ (((tid >> 4) & 1) << 2)) << 4)),
                 wv, lds};
  u32x2 tag;
  if (idx < 16) {
    tag = conv_role(lds, x, lar, aim, cre, cim, ldt, dsk, ws, xcd * 64 + idx * 4, idx, o STAMP_ARG);
  } else {
    const int rl = idx & 3, cl = (idx >> 2) & 3;
    tag = gemm_role(lds, x, wqkv, gw, ws, (xcd >> 1) * 4 + rl, (xcd & 1) * 4 + cl, 2 * ((rl + cl) & 3), o STAMP_ARG);
  }
  out_phase(lds, ws, out, rt, ct, tag STAMP_ARG);
  STAMP(12);
  STAMP_FLUSH;
  CODE_END("fused");
}

#define STARG(o)

extern "C" void kernel_launch(void* const* d_in, const int* in_sizes, int n_in, void* d_out, int out_size, void* d_ws,
                              size_t ws_size, hipStream_t stream) {
  const u16* x = (const u16*)d_in[0];
  const u16* wqkv = (const u16*)d_in[1];
  const u16* wo = (const u16*)d_in[2];
  const u16* lar = (const u16*)d_in[4];
  const u16* aim = (const u16*)d_in[5];
  const u16* cre = (const u16*)d_in[6];
  const u16* cim = (const u16*)d_in[7];
  const u16* ldt = (const u16*)d_in[8];
  const u16* dsk = (const u16*)d_in[9];
  const u16* gw = (const u16*)d_in[10];
  char* ws = (char*)d_ws;
  (void)hipFuncSetAttribute((const void*)fused, hipFuncAttributeMaxDynamicSharedMemorySize, 163840);
  fused<<<256, 512, 163840, stream>>>(x, wqkv, gw, lar, aim, cre, cim, ldt, dsk, wo, ws, (u16*)d_out STARG(0));
}
```

```cpp
#include <hip/hip_runtime.h>
#include <stdint.h>

typedef unsigned short u16;
typedef unsigned long long u64;
typedef __attribute__((ext_vector_type(8))) __bf16 bf16x8;
typedef __attribute__((ext_vector_type(2))) __bf16 bf16x2;
typedef __attribute__((ext_vector_type(8))) _Float16 f16x8;
typedef __attribute__((ext_vector_type(2))) _Float16 f16x2;
typedef __attribute__((ext_vector_type(4))) short s16x4;
typedef __attribute__((ext_vector_type(16))) float f32x16;
typedef __attribute__((ext_vector_type(4))) float f32x4;
typedef __attribute__((ext_vector_type(2))) float f32x2;
typedef __attribute__((ext_vector_type(4))) unsigned u32x4;
typedef __attribute__((ext_vector_type(2))) unsigned u32x2;
#define LAS __attribute__((address_space(3)))
#define CAS __attribute__((address_space(4)))
#define DI __device__ __forceinline__

constexpr unsigned WS_PQF = 0;
constexpr unsigned WS_BT  = 2u << 20;
constexpr unsigned WS_KVP = 4u << 20;
constexpr unsigned WS_KVZ = 6u << 20;
constexpr unsigned WS_G   = (6u << 20) + 65536;
constexpr unsigned WS_FLAG = (6u << 20) + 131072;
constexpr unsigned WS_EPOCH = WS_FLAG + 65536;
constexpr unsigned WS_PF = WS_FLAG + 131072;
constexpr unsigned WS_USED = 7u << 20;
constexpr unsigned SPIN_CAP = 200000;

constexpr float LOG2E = 1.4426950408889634f;

#define STAMP_DECL
#define STAMP_PARM
#define STAMP_ARG
#define STAMP(i)
#define STAMP_VAL(i, v)
#define STAMP_FLUSH
#define SB __builtin_amdgcn_sched_barrier(0)
#define PIN(x) asm volatile("" : "+v"(x))
#define PINS(x) asm volatile("" : "+s"(x))

typedef LAS char* lptr;
DI float bf2f(unsigned v) { return __uint_as_float(v << 16); }
DI float bfhi(unsigned v) { return __uint_as_float(v & 0xffff0000u); }
DI unsigned pk_bf16(float a, float b) { f32x2 v = {a, b}; return __builtin_bit_cast(unsigned, __builtin_convertvector(v, bf16x2)); }
DI float rbf(float a) { return __uint_as_float(pk_bf16(a, a) << 16); }
DI f16x2 pk_f16(f32x2 v) { return __builtin_convertvector(v, f16x2); }
DI f32x2 cmul(f32x2 a, f32x2 b) { f32x2 t = a.xx * b; f32x2 bs = {-b.y, b.x}; return __builtin_elementwise_fma(a.yy, bs, t); }
DI void cmul2(f32x2& r0, f32x2 a0, f32x2 b0, f32x2& r1, f32x2 a1, f32x2 b1) {
  f32x2 t0, t1;
  asm("v_pk_mul_f32 %0, %2, %3 op_sel_hi:[0,1]\n\tv_pk_mul_f32 %1, %4, %5 op_sel_hi:[0,1]\n\t"
      "v_pk_fma_f32 %0, %2, %3, %0 op_sel:[1,1,0] op_sel_hi:[1,0,1] neg_lo:[0,1,0]\n\t"
      "v_pk_fma_f32 %1, %4, %5, %1 op_sel:[1,1,0] op_sel_hi:[1,0,1] neg_lo:[0,1,0]"
      : "=&v"(t0), "=&v"(t1) : "v"(a0), "v"(b0), "v"(a1), "v"(b1));
  r0 = t0; r1 = t1;
}
DI f16x2 pk_f16c(f32x2 v) { const f32x2 c = {v.x, -v.y}; return __builtin_convertvector(c, f16x2); }
DI f16x2 cmulh(f16x2 a, f16x2 b) { f16x2 t = a.xx * b; f16x2 bs = {-b.y, b.x}; return __builtin_elementwise_fma(a.yy, bs, t); }
DI u32x4 mk_rsrc(const void* p, unsigned bytes) { const u64 a = (u64)p; return u32x4{(unsigned)a, (unsigned)(a >> 32), bytes, 0x00020000u}; }
DI void dma16(u32x4 rs, unsigned voff, unsigned soff, lptr l) {
  asm volatile("s_mov_b32 m0, %3\n\ts_nop 0\n\tbuffer_load_dwordx4 %0, %1, %2 offen lds"
               :: "v"(voff), "s"(rs), "s"(soff), "s"((unsigned)(size_t)l) : "memory");
}
DI void dma16c(u32x4 rs, unsigned voff, unsigned soff, lptr l) {
  asm volatile("s_mov_b32 m0, %3\n\ts_nop 0\n\tbuffer_load_dwordx4 %0, %1, %2 offen sc1 lds"
               :: "v"(voff), "s"(rs), "s"(soff), "s"((unsigned)(size_t)l) : "memory");
}
DI u32x4 ld128c(u32x4 rs, unsigned voff) { u32x4 v; asm volatile("buffer_load_dwordx4 %0, %1, %2, 0 offen sc1" : "=v"(v) : "v"(voff), "s"(rs) : "memory"); return v; }
DI u32x2 poll64(u32x4 rs, unsigned voff) {
  u32x2 v; asm volatile("buffer_load_dwordx2 %0, %1, %2, 0 offen sc1\n\ts_waitcnt vmcnt(0)" : "=&v"(v) : "v"(voff), "s"(rs) : "memory"); return v;
}
DI u32x2 ld64c(u32x4 rs, unsigned voff) { u32x2 v; asm volatile("buffer_load_dwordx2 %0, %1, %2, 0 offen sc1" : "=v"(v) : "v"(voff), "s"(rs) : "memory"); return v; }
DI unsigned ld32c(u32x4 rs, unsigned voff) { unsigned v; asm volatile("buffer_load_dword %0, %1, %2, 0 offen sc1" : "=v"(v) : "v"(voff), "s"(rs) : "memory"); return v; }
DI int sig(int w, int n) { return 32 * (n >> 4) + ((n >> 3) & 1) + 16 * w + 8 * ((n >> 2) & 1) + 2 * (n & 3); }
DI bf16x8 lds_b128(lptr p) { return *(const LAS bf16x8*)p; }
DI s16x4 lds_tr(lptr p) { return __builtin_amdgcn_ds_read_tr16_b64_v4i16((LAS s16x4*)p); }
DI bf16x8 lds_tr2(lptr p0, lptr p1) { s16x4 a = lds_tr(p0), b = lds_tr(p1); return __builtin_bit_cast(bf16x8, __builtin_shufflevector(a, b, 0, 1, 2, 3, 4, 5, 6, 7)); }
DI f32x16 mfma32(bf16x8 a, bf16x8 b, f32x16 c) { return __builtin_amdgcn_mfma_f32_32x32x16_bf16(a, b, c, 0, 0, 0); }
DI f32x4 mfma16(bf16x8 a, bf16x8 b, f32x4 c) { return __builtin_amdgcn_mfma_f32_16x16x32_bf16(a, b, c, 0, 0, 0); }
DI f32x4 mfma16h(f16x8 a, f16x8 b, f32x4 c) { return __builtin_amdgcn_mfma_f32_16x16x32_f16(a, b, c, 0, 0, 0); }
DI void bar() { asm volatile("" ::: "memory"); __builtin_amdgcn_s_barrier(); asm volatile("" ::: "memory"); }
DI void lgkm0() { asm volatile("s_waitcnt lgkmcnt(0)" ::: "memory"); }
#define WAIT_VM(n) asm volatile("s_waitcnt vmcnt(" #n ")" ::: "memory")
DI float phi(float tp) {
  const float e = __builtin_amdgcn_fmed3f(__builtin_amdgcn_exp2f(tp * LOG2E), 0.f, 1.f);
  const int ti = __float_as_int(tp);
  return __int_as_float(ti > 0x358637bd ? ti : 0x358637bd) + e;
}
DI bf16x8 pack8(const f32x16& a, int o) {
  u32x4 p = {pk_bf16(a[o], a[o + 1]), pk_bf16(a[o + 2], a[o + 3]), pk_bf16(a[o + 4], a[o + 5]), pk_bf16(a[o + 6], a[o + 7])};
  return __builtin_bit_cast(bf16x8, p);
}
template <int CTRL, int RM = 0xf> DI int dppi(int v) { return __builtin_amdgcn_update_dpp(0, v, CTRL, RM, 0xf, true); }
template <int CTRL, int RM = 0xf> DI float dppf(float v) { return __int_as_float(dppi<CTRL, RM>(__float_as_int(v))); }
DI unsigned umax(unsigned a, unsigned b) { return a > b ? a : b; }
DI unsigned umin(unsigned a, unsigned b) { return a < b ? a : b; }
DI unsigned umax4(unsigned m, u32x4 v) { return umax(umax(umax(m, v.x), v.y), umax(v.z, v.w)); }
DI unsigned wave_umax(unsigned v) {
  v = umax(v, dppi<0x111>(v)); v = umax(v, dppi<0x112>(v)); v = umax(v, dppi<0x114>(v)); v = umax(v, dppi<0x118>(v));
  v = umax(v, dppi<0x142, 0xa>(v)); v = umax(v, dppi<0x143, 0xc>(v));
  return __builtin_amdgcn_readlane(v, 63);
}
DI float wave_sum(float v) {
  v += dppf<0x111>(v); v += dppf<0x112>(v); v += dppf<0x114>(v); v += dppf<0x118>(v);
  v += dppf<0x142, 0xa>(v); v += dppf<0x143, 0xc>(v);
  return __int_as_float(__builtin_amdgcn_readlane(__float_as_int(v), 63));
}
DI void dma4(u32x4 rs, unsigned voff, lptr l) {
  asm volatile("s_mov_b32 m0, %2\n\ts_nop 0\n\tbuffer_load_dword %0, %1, 0 offen sc1 lds" :: "v"(voff), "s"(rs), "s"((unsigned)(size_t)l) : "memory");
}
#define CODE_PREFETCH(NAME, dump, off) do { u64 pc_; unsigned len_; \
  asm volatile("s_getpc_b64 %0\n.L" NAME "_beg:\n\ts_mov_b32 %1, .L" NAME "_end-.L" NAME "_beg" : "=s"(pc_), "=s"(len_)); \
  asm volatile("s_mov_b32 m0, %2\n\ts_nop 0\n\tbuffer_load_dword %0, %1, 0 offen lds" \
               :: "v"(off), "s"(mk_rsrc((const void*)pc_, len_)), "s"((unsigned)(size_t)(dump)) : "memory"); } while (0)
#define CODE_END(NAME) asm volatile(".L" NAME "_end:")
typedef __amdgpu_buffer_rsrc_t rsrc_t;
DI rsrc_t make_rsrc(void* p, unsigned bytes) { return __builtin_amdgcn_make_buffer_rsrc(p, 0, bytes, 0x00020000); }
constexpr int WT = 16;
DI void st128(rsrc_t r, unsigned off, u32x4 v) { __builtin_amdgcn_raw_buffer_store_b128(v, r, off, 0, WT); }
DI void st64(rsrc_t r, unsigned off, u32x2 v) { __builtin_amdgcn_raw_buffer_store_b64(v, r, off, 0, WT); }
DI void st32(rsrc_t r, unsigned off, unsigned v) { __builtin_amdgcn_raw_buffer_store_b32(v, r, off, 0, WT); }

constexpr int L_PQ = 0, L_WO = 66560, L_KVZ = L_WO + 65536, L_BT = L_KVZ + 17408;
struct Out { u32x4 wor; unsigned woo; int wv; lptr lds; };
constexpr unsigned EMPTY = 0xffffffffu;
DI void wo_issue(const Out& o, int gr) { dma16(o.wor, o.woo, gr * 65536, o.lds + L_WO + o.wv * 1024 + gr * 8192); }
constexpr int L_SIG = 158720;
constexpr int L_DUMP = 163840 - 512, L_EPOCH = 163840 - 256;
DI u32x2 next_tag(lptr lds, int wv) {
  const u32x2 e = *(const volatile LAS u32x2*)(lds + L_EPOCH + wv * 8);
  const u64 t = (((u64)(unsigned)__builtin_amdgcn_readfirstlane(e.y) << 32) | (unsigned)__builtin_amdgcn_readfirstlane(e.x)) + 1;
  return u32x2{(unsigned)t, (unsigned)(t >> 32)};
}

constexpr int SLOT = 32768, NSLOT = 4;
constexpr int GA_EX = L_KVZ;
constexpr int GA_GW = GA_EX + 8 * 544 * 4;

DI u32x2 gemm_role(lptr lds, const u16* __restrict__ x, const u16* __restrict__ w, const u16* __restrict__ gw, char* ws,
                   int rb, int cg, int rot, const Out& wo STAMP_PARM) {
  const int tid = threadIdx.x, lane = tid & 63;
  const int wv = __builtin_amdgcn_readfirstlane(tid >> 6), rg = wv & 3, hp = wv >> 2;
  const u32x4 xr = mk_rsrc(x, 2048 * 1024), wr_ = mk_rsrc(w, 512 * 3072);
  const unsigned xrow = tid >> 3, xo = rb * 131072 + xrow * 1024 + (((tid & 7) ^ ((xrow >> 1) & 7)) << 4);
  const unsigned wrow = tid >> 4, wp = (tid & 15) ^ ((wrow & 3) << 2);
  const unsigned wo_ = cg * 128 + wrow * 3072 + 1024 + (wp >> 3) * 1024 + ((wp & 7) << 4);
  const unsigned qo = cg * 128 + xrow * 3072 + (((tid & 7) ^ (((xrow >> 1) & 1) << 2)) << 4);
  const lptr lw = lds + wv * 1024;
  auto issue = [&](int c, int i) {
    const lptr slot = lw + (c % NSLOT) * SLOT;
    if (c < 8) {
      const unsigned kc = (c + rot) & 7;
      if (i == 0) dma16(xr, xo, kc * 128, slot);
      if (i == 1) dma16(wr_, wo_, kc * (64 * 3072), slot + 16384);
      if (i == 2) dma16(xr, xo, kc * 128 + 65536, slot + 8192);
      if (i == 3) dma16(wr_, wo_, kc * (64 * 3072) + 32 * 3072, slot + 24576);
    } else {
      dma16(wr_, qo, (((c - 8) * 4 + i + rot) & 7) * (64 * 3072), slot + i * 8192);
    }
  };
  STAMP(0);
  const int gsel = hp == 0 ? ((cg - rot) & 7) : -1;
  if (hp == 0 && lane < 8) dma16(mk_rsrc(gw, 1024), lane * 16, cg * 128, lds + GA_GW + wv * 128);
#pragma unroll
  for (int c = 0; c < 3; ++c) {
#pragma unroll
    for (int i = 0; i < 4; ++i) issue(c, i);
    if (c < 2) bar();
  }
  const rsrc_t wsr = make_rsrc(ws, WS_USED);
  const unsigned pqo = WS_PQF + (((rb * 4 + rg) * 32 + cg * 4 + 2 * hp) * 64 + lane) * 16;
  const int h = lane >> 5, row = (rg >> 1) * 64 + sig(rg & 1, lane & 31);
  const int gi = lane >> 4, blk = gi & 1, q = (lane & 15) >> 2, p4 = lane & 3;
  lptr ap[4], bp[2];
#pragma unroll
  for (int s = 0; s < 4; ++s) ap[s] = lds + row * 128 + (((2 * s + h) ^ ((row >> 1) & 7)) << 4);
#pragma unroll
  for (int ct = 0; ct < 2; ++ct) bp[ct] = lds + 16384 + (8 * h + q) * 256 + (((4 * (2 * ct + hp) + 2 * blk + (p4 >> 1)) ^ (q << 2)) << 4) + (p4 & 1) * 8;
  f32x16 ak, av;
  float g0 = 0.f;
#pragma unroll
  for (int i = 0; i < 16; ++i) { ak[i] = 1e-6f; av[i] = 0.f; }
  bf16x8 xf[32], Bf[3][2];
  u32x2 tag;
  auto gate_steps = [&](int c) {
    if (gsel == c) {
      const lptr gp = lds + GA_GW + wv * 128 + h * 16;
      f32x16 ag = {};
#pragma unroll
      for (int i = 0; i < 4; ++i) ag = mfma32(lds_b128(gp + i * 32), xf[4 * c + i], ag);
      g0 = ag[0];
    }
    PIN(g0);
  };
  lptr qp = lds + (8 * h + q) * 128 + (((4 * hp + 2 * blk + (p4 >> 1)) ^ ((q >> 1) << 2)) << 4) + (p4 & 1) * 8;
  const int dp = lane & 31, e_ = lane & 15;
  lptr exw = lds + GA_EX + rg * 4352 + (2 * hp + (dp >> 4)) * 1024 + e_ * 64 + (((2 * h) ^ (e_ >> 2)) << 4);
  lptr ezw = lds + GA_EX + rg * 4352 + 4096 + (2 * hp + (dp >> 4)) * 64 + (((e_ >> 2) & 1) * 8 + (e_ & 3) + 4 * (e_ >> 3)) * 4;
  lptr exr = lds + GA_EX + (tid < 272 ? tid : 0) * 16;
  unsigned kpo = tid < 272 ? WS_KVP + (rb * 8 + cg) * 8704 + tid * 16 : 0xffff0000u;
  PIN(qp); PIN(exw); PIN(ezw); PIN(exr); PIN(kpo);
  f32x16 aq;
  STAMP(1);
#pragma unroll
  for (int n = 0; n < 32; ++n) {
    const int c = n >> 2, s = n & 3, so = (c & 1) * SLOT;
    if (s == 0) {
      if (c > 0) {
        lgkm0();
        if ((c & 1) == 0) {
          const int dlt = (c == 4) ? -2 * SLOT : 2 * SLOT;
#pragma unroll
          for (int i = 0; i < 4; ++i) { ap[i] += dlt; PIN(ap[i]); }
#pragma unroll
          for (int i = 0; i < 2; ++i) { bp[i] += dlt; PIN(bp[i]); }
        }
      }
      WAIT_VM(8);
      bar();
      if (c == 0) STAMP(2);
      SB;
    }
    if (n >= 2) { ak = mfma32(xf[n - 2], Bf[(n - 2) % 3][0], ak); SB; }
    xf[n] = lds_b128(ap[s] + so); Bf[n % 3][0] = lds_tr2(bp[0] + so + s * 4096, bp[0] + so + s * 4096 + 1024); SB;
    if (n >= 2) { av = mfma32(xf[n - 2], Bf[(n - 2) % 3][1], av); SB; }
    Bf[n % 3][1] = lds_tr2(bp[1] + so + s * 4096, bp[1] + so + s * 4096 + 1024); SB;
    if (c + 3 < 10) { issue(c + 3, s); SB; } else if (s < 3) { wo_issue(wo, s); SB; }
    if (n == 14) { tag = next_tag(lds, wv); SB; }
    if (n == 30) {
#pragma unroll
      for (int i = 0; i < 16; ++i) aq[i] = 1e-6f;
      PIN(aq); SB;
    }
    if (n > 0 && s == 0) { gate_steps(c - 1); SB; }
  }
  STAMP(3);
  f32x16 kv = {}, z = {};
  constexpr int PD = 5;
  bf16x8 Aq[PD + 1], ones;
#pragma unroll
  for (int i = 0; i < 8; ++i) ones[i] = (__bf16)1.0f;
  u32x4 pk[2], vv[2];
  auto rdq = [&](int m) { Aq[m % (PD + 1)] = lds_tr2(qp + m * 2048, qp + m * 2048 + 512); };
  auto qs = [&](int i) {
    aq = mfma32(Aq[i % (PD + 1)], xf[i], aq); SB;
    if (i + PD < 32 && (i + PD < 16 || i >= 16)) { rdq(i + PD); SB; }
  };
  lgkm0(); WAIT_VM(7); bar(); SB;
  rdq(0); rdq(1); SB;
  ak = mfma32(xf[30], Bf[0][0], ak); av = mfma32(xf[30], Bf[0][1], av); SB;
  rdq(2); rdq(3); SB;
  ak = mfma32(xf[31], Bf[1][0], ak); av = mfma32(xf[31], Bf[1][1], av); SB;
  rdq(4); SB;
  qs(0); qs(1);
#pragma unroll
  for (int j = 0; j < 8; ++j) {
    float p0 = phi(ak[2 * j]);
    PIN(p0); SB;
    if (j < 5) qs(2 + 2 * j);
    float p1 = phi(ak[2 * j + 1]);
    PIN(p1);
    unsigned p = pk_bf16(p0, p1), v = pk_bf16(av[2 * j], av[2 * j + 1]);
    PIN(p); PIN(v);
    pk[j >> 2][j & 3] = p; vv[j >> 2][j & 3] = v;
    SB;
    if ((j & 3) == 3) {
      kv = mfma32(__builtin_bit_cast(bf16x8, pk[j >> 2]), __builtin_bit_cast(bf16x8, vv[j >> 2]), kv);
      z = mfma32(ones, __builtin_bit_cast(bf16x8, pk[j >> 2]), z);
      SB;
    }
    qs(j < 5 ? 3 + 2 * j : 7 + j);
    if (j == 1 || j == 4 || j == 6) { wo_issue(wo, 3 + j / 2 - (j == 1 ? 0 : 1)); SB; }
  }
  qs(15);
  {
    float sel[8];
#pragma unroll
    for (int i = 0; i < 8; ++i) { float a = kv[i], b = kv[8 + i]; PIN(a); PIN(b); sel[i] = dp < 16 ? a : b; }
    *(LAS f32x4*)exw = f32x4{sel[0], sel[1], sel[2], sel[3]};
    *(LAS f32x4*)((lptr)((size_t)exw ^ 16u)) = f32x4{sel[4], sel[5], sel[6], sel[7]};
    *(LAS float*)ezw = z[0];
  }
  lgkm0(); WAIT_VM(6); STAMP(4); bar(); SB;
  {
    const LAS f32x4* ex = (const LAS f32x4*)exr;
    const f32x4 e0 = ex[0], e1 = ex[272], e2 = ex[544], e3 = ex[816];
    SB;
#pragma unroll
    for (int i = 16; i < 16 + PD; ++i) rdq(i);
    SB;
    float sum[4];
#pragma unroll
    for (int i = 0; i < 4; ++i) { float a = e0[i] + e1[i], b = e2[i] + e3[i]; PIN(a); PIN(b); sum[i] = a + b; PIN(sum[i]); }
    st128(wsr, kpo, u32x4{__float_as_uint(sum[0]), tag.x, __float_as_uint(sum[1]), tag.x});
    SB;
    qs(16);
    st128(wsr, kpo + 4352, u32x4{__float_as_uint(sum[2]), tag.x, __float_as_uint(sum[3]), tag.x});
    SB;
  }
#pragma unroll
  for (int i = 17; i < 32; ++i) {
    qs(i);
    if (i == 18) { PIN(aq); gate_steps(7); SB; }
    if (i == 21 || i == 24) { wo_issue(wo, i == 21 ? 6 : 7); SB; }
  }
  PIN(aq);
  STAMP(5);
  {
    if (hp == 0) st32(wsr, h == 0 ? WS_G + cg * 8192 + (rb * 4 + rg) * 128 + lane * 4 : 0xfffffff0u, __float_as_uint(g0));
#pragma unroll
    for (int i = 0; i < 8; ++i) { float p = phi(aq[i]); PIN(p); aq[i] = p; }
    st128(wsr, pqo, __builtin_bit_cast(u32x4, pack8(aq, 0)));
#pragma unroll
    for (int i = 8; i < 16; ++i) { float p = phi(aq[i]); PIN(p); aq[i] = p; }
    st128(wsr, pqo + 1024, __builtin_bit_cast(u32x4, pack8(aq, 8)));
    STAMP(6);
    WAIT_VM(0);
  }
  STAMP(7);
  bar();
  STAMP(13);
  if (tid == 511) st64(wsr, WS_PF + (rb * 8 + cg) * 128, tag);
  return tag;
}

constexpr int SC_ROW = 2064;
constexpr int KC_CP = 4224, KC_CH = 2 * KC_CP;
constexpr int EX_BASE = 4 * KC_CH;
constexpr int U_BASE = 57 * SC_ROW, U_PAD = 224, U_CP = 4576, U_CH = 2 * U_CP;
constexpr int T_BASE = U_BASE + 4 * U_CH, T_CH = 1536;
static_assert(T_BASE + 4 * T_CH <= 163840 && U_BASE % 16 == 0, "lds");

DI f32x2 cexp_p(float l2, float rv) {
  const float m = __builtin_amdgcn_exp2f(l2), f = __builtin_amdgcn_fractf(rv);
  const f32x2 r = {m * __builtin_amdgcn_cosf(f), m * __builtin_amdgcn_sinf(f)};
  return r;
}
DI bf16x8 lds_2b64(lptr p) {
  const u32x2 a = *(const volatile LAS u32x2*)p, b = *(const volatile LAS u32x2*)(p + 8);
  const u32x4 v = {a.x, a.y, b.x, b.y};
  return __builtin_bit_cast(bf16x8, v);
}
DI unsigned perm(unsigned hi, unsigned lo, unsigned sel) { return __builtin_amdgcn_perm(hi, lo, sel); }
constexpr unsigned LL = 0x01000100, LH = 0x03020302, HL = 0x05040504, SWL = 0x01000302, SWH = 0x05040706, HL_LH = 0x03020504;
template <int E0, int E1, int E2, int E3>
DI void put4(unsigned base, f16x2 a, f16x2 b, f16x2 c, f16x2 d) {
  constexpr int O = E0 < 28 ? 0 : 28;
  static_assert((E1 < 28) == (E0 < 28) && (E2 < 28) == (E0 < 28) && (E3 < 28) == (E0 < 28), "one base");
  asm volatile("s_mov_b32 m0, %4\n\ts_nop 0\n\tds_write_addtid_b32 %0 offset:%5\n\tds_write_addtid_b32 %1 offset:%6\n\t"
               "ds_write_addtid_b32 %2 offset:%7\n\tds_write_addtid_b32 %3 offset:%8"
               :: "v"(a), "v"(b), "v"(c), "v"(d), "s"(base + O * SC_ROW), "i"((E0 - O) * SC_ROW), "i"((E1 - O) * SC_ROW),
                  "i"((E2 - O) * SC_ROW), "i"((E3 - O) * SC_ROW) : "memory");
}

DI u32x2 conv_role(lptr lds, const u16* __restrict__ x, const u16* __restrict__ lar, const u16* __restrict__ aim,
                   const u16* __restrict__ cre, const u16* __restrict__ cim, const u16* __restrict__ ldt,
                   const u16* __restrict__ dsk, char* ws, int ch0, int jrot, const Out& wo STAMP_PARM) {
  const int tid = threadIdx.x, lane = tid & 63;
  const int wv = __builtin_amdgcn_readfirstlane(tid >> 6), cl = wv & 3, half = wv >> 2;
  const int d = ch0 + cl;
  STAMP(0);
  const unsigned r_lar = ((const unsigned*)lar)[d * 64 + lane], r_aim = ((const unsigned*)aim)[d * 64 + lane];
  const unsigned r_cr = ((const unsigned*)cre)[d * 64 + lane], r_ci = ((const unsigned*)cim)[d * 64 + lane];
  const unsigned s_ldt = ((const CAS unsigned*)ldt)[d >> 1], s_dsk = ((const CAS unsigned*)dsk)[d >> 1];
  const int j = lane & 15, g = lane >> 4;
  const rsrc_t wsr = make_rsrc(ws, WS_USED);
  bar();
  const int cj = ch0 >> 2, cg = cj >> 4, rb = cj & 15;
  const unsigned kvo = wv == 4 && lane < 9 ? WS_KVZ + cj * 256 + lane * 16 : 0xffffffe0u;
  const int tt = (tid + jrot * 64) & 511;
  const char* xg = (const char*)x + ch0 * 2;
  const unsigned xoff = tt * 4096;
  u32x2 ur[4];
  ur[0] = *(const u32x2*)(xg + xoff); ur[1] = *(const u32x2*)(xg + xoff + 1024);
  u32x2 up;
  {
    const u32x4 zero = {0, 0, 0, 0};
    if (lane < 28) *(LAS u32x4*)(lds + U_BASE + cl * U_CH + half * U_CP + lane * 16) = zero;
  }
  const unsigned scb = (unsigned)(size_t)lds + cl * 512 + half * 256;
  const lptr fb = lds + cl * 512 + half * 256 + j * SC_ROW + g * 16;
  unsigned selA = lane < 32 ? LL : lane == 32 ? HL : SWH, selB = lane < 32 ? HL_LH : SWH, selC = lane < 32 ? LL : SWH, selE = lane < 32 ? LL : SWL;
  if (half == 0) { const bool odd = !(lane & 1); selA = odd ? LH : LL; selB = odd ? HL : LH; selC = odd ? HL : SWL; }
  PIN(selA); PIN(selB); PIN(selC); PIN(selE);
  const unsigned shl = half ? 0 : 16;
  const float f_ldt = (d & 1) ? bfhi(s_ldt) : bf2f(s_ldt), f_dsk = (d & 1) ? bfhi(s_dsk) : bf2f(s_dsk);
  const float dt = __int_as_float(__builtin_amdgcn_readfirstlane(__float_as_int(rbf(__expf(f_ldt)))));
  SB;
  const float l2 = -rbf(__builtin_amdgcn_exp2f(bfhi(r_lar << shl) * LOG2E)) * dt * LOG2E;
  const float rv = bfhi(r_aim << shl) * dt * 0.15915494309189535f;
  unsigned cm;
  {
    typedef __attribute__((ext_vector_type(2))) unsigned short u16x2;
    const u16x2 m2 = __builtin_elementwise_max(__builtin_bit_cast(u16x2, r_cr & 0x7fff7fffu), __builtin_bit_cast(u16x2, r_ci & 0x7fff7fffu));
    const unsigned m = __builtin_bit_cast(unsigned, m2);
    cm = umax(wave_umax(umax(m << 16, m)), 0x0da24260u);
  }
  const float cmax = __uint_as_float(cm), cinv = __builtin_amdgcn_rcpf(cmax);
  f32x4 kt[3];
  {
    f16x8 rf[4], t0[4];
    const f16x2 one = {(_Float16)1.f, (_Float16)0.f};
    const f32x2 E1 = cexp_p(l2, rv), E8 = cexp_p(l2 * 8.f, rv * 8.f), E128 = cexp_p(l2 * 128.f, rv * 128.f);
    f32x2 E2, E3, E4, E16, E24, E32, E64, E96, E256, E384, E512, H1, H2, H3, H4, dummy;
    const f32x2 H0 = {bfhi(r_cr << shl) * cinv, bfhi(r_ci << shl) * cinv};
    cmul2(E16, E8, E8, E256, E128, E128);
    cmul2(E32, E16, E16, E512, E256, E256);
    cmul2(E24, E16, E8, E2, E1, E1);
    cmul2(E64, E32, E32, H1, H0, E512);
    cmul2(E3, E2, E1, E4, E2, E2);
    cmul2(E96, E64, E32, H2, H1, E512);
    cmul2(E384, E256, E128, H3, H2, E512);
    {
      const f16x2 P1 = pk_f16c(E8), P2 = pk_f16c(E16), P3 = pk_f16c(E24), Q1 = pk_f16c(E32), Q2 = pk_f16c(E64), Q3 = pk_f16c(E96);
      put4<0, 1, 2, 3>(scb, one, P1, P2, P3);
      put4<4, 5, 6, 7>(scb, Q1, cmulh(Q1, P1), cmulh(Q1, P2), cmulh(Q1, P3));
      put4<8, 9, 10, 11>(scb, Q2, cmulh(Q2, P1), cmulh(Q2, P2), cmulh(Q2, P3));
      put4<12, 13, 14, 15>(scb, Q3, cmulh(Q3, P1), cmulh(Q3, P2), cmulh(Q3, P3));
    }
    SB;
    ur[2] = *(const u32x2*)(xg + xoff + 2048);
    SB;
    H4 = cmul(H3, E512); (void)dummy;
    const f16x2 e1 = pk_f16(E1), e2 = pk_f16(E2), e3 = pk_f16(E3), e4 = pk_f16(E4);
    const f16x2 e5 = cmulh(e4, e1), e6 = cmulh(e4, e2), e7 = cmulh(e4, e3);
    const f16x2 S1 = pk_f16(E128), S2 = pk_f16(E256), S3 = pk_f16(E384);
    const f16x2 h0 = pk_f16(H0), h1 = pk_f16(H1), h2 = pk_f16(H2), h3 = pk_f16(H3);
    {
      const f16x2 G1 = cmulh(h0, S1);
      put4<16, 17, 18, 19>(scb, h0, cmulh(h0, e1), cmulh(h0, e2), cmulh(h0, e3));
      put4<20, 21, 22, 23>(scb, cmulh(h0, e4), cmulh(h0, e5), cmulh(h0, e6), cmulh(h0, e7));
      put4<24, 25, 26, 27>(scb, G1, cmulh(G1, e1), cmulh(G1, e2), cmulh(G1, e3));
      put4<28, 29, 30, 31>(scb, cmulh(G1, e4), cmulh(G1, e5), cmulh(G1, e6), cmulh(G1, e7));
    }
#pragma unroll
    for (int s = 0; s < 4; ++s) { rf[s] = *(const LAS f16x8*)(fb + s * 64); t0[s] = *(const LAS f16x8*)(fb + 16 * SC_ROW + s * 64); }
    SB;
    ur[3] = *(const u32x2*)(xg + xoff + 3072);
    up = __builtin_amdgcn_raw_buffer_load_b64(make_rsrc((void*)x, 2048 * 1024), lane == 0 && tt > 0 ? ch0 * 2 + xoff - 1024 : 0xfffffff0u, 0, 0);
    SB;
    {
      const f16x2 G2 = cmulh(h0, S2), G3 = cmulh(h0, S3), G5 = cmulh(h1, S1), G6 = cmulh(h1, S2), G7 = cmulh(h1, S3);
      put4<32, 33, 34, 35>(scb, G2, cmulh(G2, e2), cmulh(G2, e4), cmulh(G2, e6));
      kt[0] = mfma16h(t0[0], rf[0], f32x4{0.f, 0.f, 0.f, 0.f});
      kt[0] = mfma16h(t0[1], rf[1], kt[0]);
      put4<36, 37, 38, 39>(scb, G3, cmulh(G3, e2), cmulh(G3, e4), cmulh(G3, e6));
      kt[0] = mfma16h(t0[2], rf[2], kt[0]);
      put4<40, 41, 42, 43>(scb, h1, cmulh(h1, e4), G5, cmulh(G5, e4));
      kt[0] = mfma16h(t0[3], rf[3], kt[0]);
      put4<44, 45, 46, 47>(scb, G6, cmulh(G6, e4), G7, cmulh(G7, e4));
    }
#pragma unroll
    for (int s = 0; s < 4; ++s) t0[s] = *(const LAS f16x8*)(fb + 32 * SC_ROW + s * 64);
    SB;
    put4<48, 49, 50, 51>(scb, h2, cmulh(h2, S1), cmulh(h2, S2), cmulh(h2, S3));
    put4<52, 53, 54, 55>(scb, h3, cmulh(h3, S1), cmulh(h3, S2), cmulh(h3, S3));
    *(LAS f16x2*)(lds + cl * 512 + half * 256 + lane * 4 + 56 * SC_ROW) = pk_f16(H4);
    f16x8 t2[4];
#pragma unroll
    for (int s = 0; s < 4; ++s) t2[s] = *(const LAS f16x8*)(fb + 48 * SC_ROW + s * 64);
    SB;
    kt[1] = f32x4{0.f, 0.f, 0.f, 0.f}; kt[2] = kt[1];
#pragma unroll
    for (int s = 0; s < 4; ++s) kt[1] = mfma16h(t0[s], rf[s], kt[1]);
#pragma unroll
    for (int s = 0; s < 4; ++s) kt[2] = mfma16h(t2[s], rf[s], kt[2]);
  }
  auto put_u = [&]() {
    const u32x2 pv = {(unsigned)__builtin_amdgcn_update_dpp((int)up.x, (int)ur[3].x, 0x138, 0xf, 0xf, false),
                      (unsigned)__builtin_amdgcn_update_dpp((int)up.y, (int)ur[3].y, 0x138, 0xf, 0xf, false)};
    const lptr ub = lds + U_BASE + (U_PAD + 4 * tt) * 2;
#pragma unroll
    for (int c = 0; c < 4; ++c) {
      const unsigned sel = (c & 1) ? 0x07060302 : 0x05040100;
      const unsigned p = pv[c >> 1], r0 = ur[0][c >> 1], r1 = ur[1][c >> 1], r2 = ur[2][c >> 1], r3 = ur[3][c >> 1];
      *(LAS u32x2*)(ub + c * U_CH) = u32x2{perm(r1, r0, sel), perm(r3, r2, sel)};
      *(LAS u32x2*)(ub + c * U_CH + U_CP) = u32x2{perm(r0, p, sel), perm(r2, r1, sel)};
    }
  };
  const lptr Tb = lds + T_BASE + cl * T_CH;
  {
    LAS f32x4* xw = (LAS f32x4*)(lds + cl * 512 + half * 256 + g * SC_ROW) + j;
    const LAS f32x4* xr = (const LAS f32x4*)(lds + cl * 512 + (half ^ 1) * 256 + g * SC_ROW) + j;
    if (half) xw[0] = kt[0]; else { xw[0] = kt[1]; xw[4 * SC_ROW / 16] = kt[2]; }
    const float ks = 2.f * dt * cmax;
    const lptr p1 = half ? Tb + (256 + 64 * g + (g < 2 ? 4 : 2) * j) * 2 : Tb + (128 * (g >> 1) + 4 * (g & 1) + 8 * j) * 2;
    lptr p1b = p1 + (g < 2 ? 4 : 64);
    PIN(p1b);
    lgkm0();
    STAMP(3);
    bar();
    if (half == 0) {
      f32x4 a0 = (kt[0] + xr[0]) * ks;
      if (lane == 0) a0[0] += f_dsk;
      *(LAS u32x2*)p1 = u32x2{pk_bf16(a0[0], a0[1]), pk_bf16(a0[2], a0[3])};
    } else {
      const f32x4 a1 = (kt[1] + xr[0]) * ks, a2 = (kt[2] + xr[4 * SC_ROW / 16]) * ks;
      *(LAS unsigned*)p1 = pk_bf16(a1[0], a1[1]);
      *(LAS unsigned*)p1b = pk_bf16(a1[2], a1[3]);
      const unsigned q0 = pk_bf16(a2[0], a2[1]), q1 = pk_bf16(a2[2], a2[3]);
      LAS u16* p2 = (LAS u16*)Tb + 512 + 64 * g + j;
      p2[0] = (u16)q0; p2[16] = (u16)(q0 >> 16); p2[32] = (u16)q1; p2[48] = (u16)(q1 >> 16);
    }
  }
  lgkm0();
  STAMP(4);
  bar();
  {
    const lptr kc = lds + cl * KC_CH + lane * 16;
    auto emit = [&](int Y0, unsigned P0, unsigned P1, unsigned P2, unsigned P3, unsigned P4) {
      *(LAS u32x4*)(kc + Y0 * 16) = u32x4{P0, P1, P2, P3};
      *(LAS u32x4*)(kc + KC_CP + Y0 * 16) = u32x4{P1, P2, P3, P4};
    };
    if (half == 0) {
      const LAS unsigned* p = (const LAS unsigned*)(Tb + ((575 - lane) >> 1) * 4);
      const unsigned D0 = p[32], D1 = p[33], F0 = p[0], F1 = p[1], Z = *(const LAS unsigned*)Tb;
      SB;
      const unsigned e1 = perm(D1, D0, selB), e0 = perm(D1, D0, selA), f1 = perm(F1, F0, selB), f0 = perm(F1, F0, selA);
      emit(0, e1, e1, perm(D1, D0, selC), e0, e0);
      emit(64, f1, f1, perm(F1, F0, selC), f0, f0);
      if (lane < 4) emit(256, lane == 0 ? perm(0, Z, SWL) : 0u, 0u, 0u, 0u, 0u);
    } else {
      const LAS unsigned* p = (const LAS unsigned*)(Tb + (255 - lane) * 4);
      const LAS unsigned* r = (const LAS unsigned*)(Tb + (lane < 32 ? 760 - 8 * lane : 1008 - 16 * lane));
      const unsigned F0 = p[0], F1 = p[1], D0 = r[0], D1 = r[1], D2 = r[2], D3 = r[3], D4 = r[4];
      SB;
      const unsigned e2 = perm(F1, F0, HL);
      emit(128, e2, e2, perm(F1, F0, LH), perm(F1, F0, SWL), perm(F1, F0, LL));
      emit(192, perm(D4, D2, selA), perm(lane < 32 ? D2 : D3, D1, selB), perm(D2, D1, selC), perm(D1, D0, selB), perm(D1, D0, selE));
    }
  }
  SB;
  put_u();
  const u32x2 tag = next_tag(lds, wv);
  lgkm0();
  STAMP(5);
  bar();
  const u32x4 wsl = mk_rsrc(ws, WS_USED);
  const int a_ = j;
  const lptr ab = lds + cl * KC_CH + ((a_ & 1) ? KC_CP + 4088 : 4096) - 8 * (a_ >> 1) + 16 * g - 3584 - 192 - half * 256;
  const lptr bb = lds + U_BASE + cl * U_CH + ((a_ & 1) ? 0 : U_CP) + (U_PAD + 32 * (a_ >> 1) + 8 * g) * 2 - 192 - half * 256;
  f32x4 acc[8];
#pragma unroll
  for (int i = 0; i < 8; ++i) acc[i] = f32x4{0.f, 0.f, 0.f, 0.f};
  LAS f32x4* exw = (LAS f32x4*)(lds + EX_BASE + wv * 4096) + lane;
  f32x4 keep[4];
  u32x4 k[3];
  constexpr unsigned DONE = 0xffffffe0u;
  const unsigned src = WS_KVP + (j * 8 + cg) * 8704;
  const unsigned ko = src + (rb >> 2) * 1024 + ((rb & 3) * 4 + cl) * 64 + ((g ^ (rb & 3)) << 4);
  const bool zl = wv == 4 && g < 2;
  unsigned ka[3] = {half ? ko : DONE, half ? ko + 4352 : DONE, zl ? src + g * 4352 + 4096 + rb * 16 : DONE};
  unsigned tagz = zl ? tag.x : 0u;
  lptr slw = lds + T_BASE + (j == 15 ? (cl * 4 + g) * 8 : 256), zsw = lds + T_BASE + (j == 15 && zl ? 128 + g * 4 : 256), slr = lds + T_BASE + (lane < 9 ? lane : 0) * 16;
  PIN(ka[0]); PIN(ka[1]); PIN(ka[2]); PIN(tagz); PIN(slw); PIN(zsw); PIN(slr);
  {
    u32x2 Al[2][8], Ah[2][8];
    bf16x8 B[2][8];
    auto rd = [&](int r, int k) {
      const int nb = r & 1, mu = k / 3, w = k % 3;
      if (w == 0) B[nb][mu] = lds_b128(bb + 512 * mu + 64 * (3 - r));
      if (w == 1) Al[nb][mu] = *(const volatile LAS u32x2*)(ab + 512 * (7 - mu) + 64 * (3 - r));
      if (w == 2) Ah[nb][mu] = *(const volatile LAS u32x2*)(ab + 512 * (7 - mu) + 64 * (3 - r) + 8);
    };
    auto AF = [&](int nb, int mu) { return __builtin_bit_cast(bf16x8, u32x4{Al[nb][mu].x, Al[nb][mu].y, Ah[nb][mu].x, Ah[nb][mu].y}); };
#pragma unroll
    for (int k = 0; k < 24; ++k) rd(0, k);
    SB;
#pragma unroll
    for (int r = 1; r < 4; ++r) {
      const int cb = (r - 1) & 1;
      int cnt = 0;

#pragma unroll
      for (int m = 0; m < 8; ++m)
#pragma unroll
        for (int n = 0; n <= m; ++n)
#pragma unroll
          for (int sw = 0; sw < 2; ++sw)
            if (m + n < 8 && (sw == 0 || n < m)) {
              const int mu = sw ? n : m, nu = sw ? m : n;
              acc[m + n] = mfma16(AF(cb, mu), B[cb][nu], acc[m + n]);
              SB;
              if (cnt % 3 != 2) rd(r, cnt - cnt / 3);
              else if (cnt == 14 || cnt == 29) wo_issue(wo, 2 * (r - 1) + (cnt == 29));
              else if ((cnt == 5 || cnt == 20) && r == 1) st128(wsr, WS_BT + ch0 * 4096 + (cnt == 5 ? 0 : 8192) + tid * 16, u32x4{EMPTY, EMPTY, EMPTY, EMPTY});
              else if (cnt == 2 && r == 1) st128(wsr, kvo, u32x4{EMPTY, EMPTY, EMPTY, EMPTY});
              else if ((cnt == 26 || cnt == 32 || cnt == 35) && r == 3) k[cnt == 26 ? 2 : cnt == 35] = ld128c(wsl, ka[cnt == 26 ? 2 : cnt == 35]);
              else if (cnt == 2 && r == 3) { WAIT_VM(3); st64(wsr, lane == 0 ? WS_FLAG + (cj * 8 + wv) * 8 : 0xfffffff0u, tag); }
              SB;
              ++cnt;
            }
    }
    auto tiles = [&](int t0) {
#pragma unroll
      for (int mu = 0; mu < 8; ++mu)
#pragma unroll
        for (int n = 0; n < 8; ++n) if (mu + n >= t0 && mu + n < t0 + 4) acc[mu + n] = mfma16(AF(1, mu), B[1][n], acc[mu + n]);
    };
    if (half) {
      tiles(0); SB; tiles(4);
#pragma unroll
      for (int i = 0; i < 4; ++i) { exw[64 * i] = acc[i]; keep[i] = acc[4 + i]; }
    } else {
      tiles(4); SB; tiles(0);
#pragma unroll
      for (int i = 0; i < 4; ++i) { exw[64 * i] = acc[4 + i]; keep[i] = acc[i]; }
    }
  }
  STAMP(6);
  {
    const LAS f32x4* exr = (const LAS f32x4*)(lds + EX_BASE + (wv ^ 4) * 4096) + lane;
    const unsigned bto = WS_BT + d * 4096 + half * 2048 + (g & 1) * 512 + (a_ >> 2) * 128 + (g >> 1) * 64 + (a_ & 3) * 16;
    u32x4 bt[2];
    lgkm0();
    STAMP(7);
    bar();
    f32x4 ex[4];
#pragma unroll
    for (int i = 0; i < 4; ++i) ex[i] = exr[64 * i];
    SB;
    STAMP(2);
    unsigned spin = 0;
    WAIT_VM(0);
    PIN(k[0]); PIN(k[1]); PIN(k[2]);
    if (half) {
#pragma unroll 1
      for (; spin < SPIN_CAP; ++spin) {
        const unsigned x = ((k[0].y ^ tag.x) | (k[0].w ^ tag.x) | (k[1].y ^ tag.x)) | ((k[1].w ^ tag.x) | (k[2].y ^ tagz) | (k[2].w ^ tagz));
        if (__all(x == 0)) break;
        asm volatile("buffer_load_dwordx4 %0, %3, %6, 0 offen sc1\n\tbuffer_load_dwordx4 %1, %4, %6, 0 offen sc1\n\t"
                     "buffer_load_dwordx4 %2, %5, %6, 0 offen sc1\n\ts_waitcnt vmcnt(0)"
                     : "=&v"(k[0]), "=&v"(k[1]), "=&v"(k[2]) : "v"(ka[0]), "v"(ka[1]), "v"(ka[2]), "s"(wsl) : "memory");
      }
      float v[6] = {__uint_as_float(k[0].x), __uint_as_float(k[0].z), __uint_as_float(k[1].x), __uint_as_float(k[1].z),
                    __uint_as_float(k[2].x), __uint_as_float(k[2].z)};
#pragma unroll
      for (int i = 0; i < 6; ++i) { v[i] += dppf<0x111>(v[i]); PIN(v[i]); }
#pragma unroll
      for (int i = 0; i < 6; ++i) { v[i] += dppf<0x112>(v[i]); PIN(v[i]); }
#pragma unroll
      for (int i = 0; i < 6; ++i) { v[i] += dppf<0x114>(v[i]); PIN(v[i]); }
#pragma unroll
      for (int i = 0; i < 6; ++i) { v[i] += dppf<0x118>(v[i]); PIN(v[i]); }
      *(LAS u32x2*)slw = u32x2{umin(pk_bf16(v[0], v[1]), EMPTY - 1), umin(pk_bf16(v[2], v[3]), EMPTY - 1)};
      *(LAS unsigned*)zsw = umin(pk_bf16(v[4], v[5]), EMPTY - 1);
    }
    lgkm0();
    bar();
    PIN(ex[0]); PIN(ex[1]); PIN(ex[2]); PIN(ex[3]);
    if (wv == 4) st128(wsr, kvo, *(const LAS u32x4*)slr);
    STAMP(4);
#pragma unroll
    for (int i = 0; i < 4; i += 2) {
      const f32x4 v = keep[i] + ex[i], w = keep[i + 1] + ex[i + 1];
      const auto sx = __builtin_amdgcn_permlane16_swap(umin(pk_bf16(v[0], v[1]), EMPTY - 1), umin(pk_bf16(w[0], w[1]), EMPTY - 1), false, false);
      const auto sy = __builtin_amdgcn_permlane16_swap(umin(pk_bf16(v[2], v[3]), EMPTY - 1), umin(pk_bf16(w[2], w[3]), EMPTY - 1), false, false);
      bt[i >> 1] = u32x4{sx[0], sy[0], sx[1], sy[1]};
      PIN(bt[i >> 1]);
    }
    st128(wsr, bto, bt[0]); st128(wsr, bto + 1024, bt[1]);
    wo_issue(wo, 6); wo_issue(wo, 7);
    STAMP_VAL(3, st_[0] + spin);
    STAMP(13);
    return tag;
  }
}

DI void out_phase(lptr lds, char* ws, u16* __restrict__ out, int rt, int ct, u32x2 tag STAMP_PARM) {
  const int tid = threadIdx.x, lane = tid & 63;
  const int wv = __builtin_amdgcn_readfirstlane(tid >> 6), wr = wv & 1, hq = wv >> 1;
  const u32x4 wsl = mk_rsrc(ws, WS_USED);
  const int h = lane >> 5, t = lane & 31, trow = rt * 64 + sig(wr, t);
  const unsigned bo = WS_BT + (ct * 64 + wv * 8 + (lane >> 3)) * 4096 + rt * 128 + (((lane & 7) ^ (((lane >> 4) & 1) << 2)) << 4);
  const unsigned fo = lane == 0 ? WS_PF + ((rt >> 1) * 8 + wv) * 128 : lane < 17 ? WS_FLAG + 8 * ((wv * 16 + lane - 1) * 8 + 4)
                    : lane < 25 ? WS_FLAG + 8 * ((ct * 16 + 2 * wv) * 8 + 2 * (lane - 17)) : 0xffffffe0u;
  const lptr fl = lds + L_SIG + wv * 512;
  auto sig_bad = [&]() {
    const u32x4 f = *(const volatile LAS u32x4*)(fl + t * 16);
    return __ballot(f.x != tag.x || f.y != tag.y || (lane >= 17 && (f.z != tag.x || f.w != tag.y)));
  };
  *(volatile LAS u32x4*)(fl + t * 16) = u32x4{~tag.x, 0, 0, 0};
  lgkm0();
  unsigned tries = 0;
  unsigned gp[4];
  const unsigned go = WS_G + h * 32768 + (rt * 2 + wr) * 128 + t * 4;
  bool b_arm;
#pragma unroll 1
  for (unsigned spin = 0; spin < 4 * SPIN_CAP; ++spin) {
    if ((spin & 3) == 0) { if (lane < 32) dma16c(wsl, fo, 0, fl); ++tries; }
    __builtin_amdgcn_s_sleep(1);
    const u64 bad = sig_bad();
    b_arm = (bad & 0x1fe0000ull) == 0;
    if ((bad & 0x1ffffull) == 0) break;
  }
  STAMP(8);
  auto pq_piece = [&](int g) {
    dma16c(wsl, lane * 16, WS_PQF + rt * 65536 + wv * 4096 + (g & 1) * 32768 + (g >> 1) * 1024, lds + L_PQ + wv * 4096 + (g & 1) * 32768 + (g >> 1) * 1024);
  };
  {
    constexpr unsigned DONE = 0xffffffe0u;
    const unsigned ko = WS_KVZ + wv * 4096 + (lane >> 3) * 256 + (lane & 7) * 16;
    unsigned ka[3] = {ko, ko + 2048, lane < 16 ? WS_KVZ + wv * 4096 + lane * 256 + 128 : DONE};
    const lptr kl = lds + L_KVZ + wv * 2176 + (lane >> 5) * 544 + (lane & 31) * 16, kz = lane < 16 ? lds + L_KVZ + wv * 2176 + (lane >> 2) * 544 + 512 + (lane & 3) * 8 : lds + L_DUMP;
    u32x4 k[3];
#pragma unroll
    for (int i = 0; i < 3; ++i) k[i] = ld128c(wsl, ka[i]);
    pq_piece(0); pq_piece(1);
    WAIT_VM(2);
    PIN(k[0]); PIN(k[1]); PIN(k[2]);
    if (__all(umax4(umax4(umax(k[2].x, k[2].y), k[0]), k[1]) != EMPTY)) {
      *(LAS u32x4*)kl = k[0]; *(LAS u32x4*)(kl + 1088) = k[1]; *(LAS u32x2*)kz = u32x2{k[2].x, k[2].y};
      tries += 256;
    } else {
#pragma unroll 1
      for (unsigned spin = 0; spin < SPIN_CAP; ++spin) {
#pragma unroll
        for (int i = 0; i < 3; ++i) {
          if ((i < 2 ? umax4(0, k[i]) : umax(k[i].x, k[i].y)) != EMPTY && ka[i] != DONE) {
            if (i < 2) *(LAS u32x4*)(kl + i * 1088) = k[i]; else *(LAS u32x2*)kz = u32x2{k[i].x, k[i].y};
            ka[i] = DONE;
          }
        }
        tries += 256;
        if (__all((ka[0] & ka[1] & ka[2]) == DONE)) break;
        __builtin_amdgcn_s_sleep(3);
        asm volatile("buffer_load_dwordx4 %0, %3, %6, 0 offen sc1\n\tbuffer_load_dwordx4 %1, %4, %6, 0 offen sc1\n\t"
                     "buffer_load_dwordx4 %2, %5, %6, 0 offen sc1\n\ts_waitcnt vmcnt(0)"
                     : "=&v"(k[0]), "=&v"(k[1]), "=&v"(k[2]) : "v"(ka[0]), "v"(ka[1]), "v"(ka[2]), "s"(wsl) : "memory");
      }
    }
  }
#pragma unroll
  for (int i = 2; i < 8; ++i) pq_piece(i);
  STAMP(9);
  STAMP_VAL(1, st_[0] + tries);
  const int gi = lane >> 4, blk = gi & 1, q = (lane & 15) >> 2, p4 = lane & 3;
  const lptr kvb = lds + L_KVZ + (t < 16 ? t : 16) * 32 + h * 16 + hq * 544;
  const lptr pqb = lds + L_PQ + wr * 32768 + lane * 16 + hq * 1024;
  lptr wob[2];
#pragma unroll
  for (int c = 0; c < 2; ++c)
    wob[c] = lds + L_WO + (4 * h + q) * 128 + (((2 * ((2 * c + blk + hq) & 3) + (p4 >> 1)) ^ ((q >> 1) << 2)) << 4) + (p4 & 1) * 8 + hq * 2048;
  const lptr bb = lds + L_BT + (hq * 16 + 4 * h + q) * 128 + (((4 * wr + 2 * blk + (p4 >> 1)) ^ ((q >> 1) << 2)) << 4) + (p4 & 1) * 8;
  char* dst = (char*)out + (unsigned)(trow * 1024 + ct * 128 + hq * 32 + h * 16);
  PIN(dst);
  float gt;
  u32x4 bchk;
  bool b_ok;
  f32x16 acc[2] = {}, nd[2];
  bf16x8 wf[2][2], kf[2], pf[2];
  lgkm0();
#define WAIT_IF(q, n) asm volatile("s_cmp_lg_u32 %0, " #q "\n\ts_cbranch_scc1 .Lw%=\n\ts_waitcnt vmcnt(" #n ")\n.Lw%=:" :: "s"(hq) : "memory", "scc")
#pragma unroll
  for (int n = 0; n < 10; ++n) {
    const int a = n - 1, b = n - 2;
    if (n == 0) { WAIT_IF(0, 0); bar(); STAMP(10); SB; }
    if (n == 2) { WAIT_IF(1, 0); bar(); SB; }
    if (n == 4) { WAIT_IF(2, 5); bar(); SB; }
    if (n == 6) { lgkm0(); WAIT_IF(3, 5); bar(); SB; }
    if (a >= 0 && a < 8) { const f32x16 zero = {}; nd[a & 1] = mfma32(kf[a & 1], pf[a & 1], zero); SB; }
    if (n < 8) { kf[n & 1] = lds_b128(kvb + n * 2176); pf[n & 1] = lds_b128(pqb + n * 4096); SB; }
    if (n == 2) { dma16c(wsl, bo, 0, lds + L_BT + wv * 1024); SB; }
    if (n == 3) { gp[0] = ld32c(wsl, go); gp[1] = ld32c(wsl, go + 8192); SB; }
    if (b >= 0) {
      f32x16& v = nd[b & 1];
      PIN(v);
      const float r = __builtin_amdgcn_rcpf(v[8] + 1e-6f);
      float o[8];
#pragma unroll
      for (int e = 0; e < 8; ++e) { o[e] = v[e] * r; PIN(o[e]); }
      const u32x4 p = {pk_bf16(o[0], o[1]), pk_bf16(o[2], o[3]), pk_bf16(o[4], o[5]), pk_bf16(o[6], o[7])};
      acc[1] = mfma32(wf[b & 1][1], __builtin_bit_cast(bf16x8, p), acc[1]);
      acc[0] = mfma32(wf[b & 1][0], __builtin_bit_cast(bf16x8, p), acc[0]);
      SB;
    }
    if (a >= 0 && a < 8) {
#pragma unroll
      for (int c = 0; c < 2; ++c) wf[a & 1][c] = lds_tr2(wob[c] + a * 8192, wob[c] + a * 8192 + 1024);
      SB;
    }
    if (n == 3) { gp[2] = ld32c(wsl, go + 16384); gp[3] = ld32c(wsl, go + 24576); SB; }
    if (n == 8) {
      WAIT_VM(0);
      PIN(gp[0]); PIN(gp[1]); PIN(gp[2]); PIN(gp[3]);
      bchk = *(const volatile LAS u32x4*)(lds + L_BT + wv * 1024 + lane * 16);
      const unsigned gs = __float_as_uint((__uint_as_float(gp[0]) + __uint_as_float(gp[1])) + (__uint_as_float(gp[2]) + __uint_as_float(gp[3])));
      const auto gw2 = __builtin_amdgcn_permlane32_swap(gs, gs, false, false);
      gt = __builtin_amdgcn_rcpf(1.f + __builtin_amdgcn_exp2f(-(__uint_as_float(gw2[0]) + __uint_as_float(gw2[1])) * LOG2E));
      PIN(gt);
      SB;
    }
    if (n == 9) { b_ok = b_arm && __all(umax4(0, bchk) != EMPTY); SB; }
  }
  PIN(acc[0]); PIN(acc[1]);
  STAMP(11);
#pragma unroll 1
  for (unsigned spin = 0; spin < SPIN_CAP && !b_ok; ++spin) {
    __builtin_amdgcn_s_sleep(2);
    if (!b_arm) { if (lane < 32) dma16c(wsl, fo, 0, fl); WAIT_VM(0); b_arm = (sig_bad() & 0x1fe0000ull) == 0; }
    dma16c(wsl, bo, 0, lds + L_BT + wv * 1024);
    WAIT_VM(0);
    b_ok = b_arm && __all(umax4(0, *(const volatile LAS u32x4*)(lds + L_BT + wv * 1024 + lane * 16)) != EMPTY);
  }
  {
    const lptr xb = lds + L_PQ + wr * 32768 + lane * 16;
#pragma unroll
    for (int p = 3; p >= 1; --p) {
      const f32x16& v = acc[p >> 1];
      LAS f32x4* xw = (LAS f32x4*)(xb + (((hq + p) & 3) * 3 + p - 1) * 2048);
#pragma unroll
      for (int G = 0; G < 2; ++G) { const int i = 8 * (p & 1) + 4 * G; xw[64 * G] = f32x4{v[i], v[i + 1], v[i + 2], v[i + 3]}; }
    }
    const LAS f32x4* xr = (const LAS f32x4*)(xb + hq * 6144);
    lgkm0();
    bar();
    const u32x2 bv0 = __builtin_bit_cast(u32x2, lds_tr(bb)), bv1 = __builtin_bit_cast(u32x2, lds_tr(bb + 1024));
    u32x2 o[2];
#pragma unroll
    for (int G = 0; G < 2; ++G) {
      const unsigned b0 = G ? bv1.x : bv0.x, b1 = G ? bv1.y : bv0.y;
      const f32x4 bf = {bf2f(b0), bfhi(b0), bf2f(b1), bfhi(b1)};
      const f32x4 own = {acc[0][4 * G], acc[0][4 * G + 1], acc[0][4 * G + 2], acc[0][4 * G + 3]};
      const f32x4 r = (((own + xr[64 * G]) + (xr[64 * G + 128] + xr[64 * G + 256])) - bf) * gt + bf;
      o[G] = u32x2{pk_bf16(r[0], r[1]), pk_bf16(r[2], r[3])};
    }
    auto sx = __builtin_amdgcn_permlane32_swap(o[0].x, o[1].x, false, false);
    auto sy = __builtin_amdgcn_permlane32_swap(o[0].y, o[1].y, false, false);
    const u32x4 v = {sx[0], sy[0], sx[1], sy[1]};
    *(u32x4*)dst = v;
  }
  if (blockIdx.x == 0 && tid == 0) st64(make_rsrc(ws, WS_USED), WS_EPOCH, tag);
}

__global__ void __launch_bounds__(512, 2) fused(const u16* __restrict__ x, const u16* __restrict__ wqkv, const u16* __restrict__ gw,
                                                const u16* __restrict__ lar, const u16* __restrict__ aim, const u16* __restrict__ cre,
                                                const u16* __restrict__ cim, const u16* __restrict__ ldt, const u16* __restrict__ dsk,
                                                const u16* __restrict__ wo, char* ws, u16* __restrict__ out
) {
  STAMP_DECL;
  extern __shared__ __attribute__((aligned(16))) char lds_[];
  const lptr lds = (lptr)lds_;
  const int b = blockIdx.x, xcd = b & 7, idx = b >> 3, tid = threadIdx.x, lane = tid & 63;
  const int wv = __builtin_amdgcn_readfirstlane(tid >> 6);
  if (wv == 7) CODE_PREFETCH("fused", lds + L_DUMP, ((idx & 7) * 64 + lane) * 64);
  asm volatile("" :: "s"(x), "s"(wqkv), "s"(gw), "s"(lar), "s"(aim), "s"(cre), "s"(cim), "s"(ldt), "s"(dsk), "s"(wo), "s"(ws), "s"(out));
  if ((tid & 63) < 2) dma4(mk_rsrc(ws, WS_USED), WS_EPOCH + (tid & 63) * 4, lds + L_EPOCH + wv * 8);
  const int rt = (xcd >> 1) * 8 + (idx & 7), ct = (xcd & 1) * 4 + (idx >> 3);
  const Out o = {mk_rsrc(wo, 512 * 1024), (unsigned)(ct * 128 + (tid >> 3) * 1024 + (((tid & 7) ^ (((tid >> 4) & 1) << 2)) << 4)),
                 wv, lds};
  u32x2 tag;
  if (idx < 16) {
    tag = conv_role(lds, x, lar, aim, cre, cim, ldt, dsk, ws, xcd * 64 + idx * 4, idx, o STAMP_ARG);
  } else {
    const int rl = idx & 3, cl = (idx >> 2) & 3;
    tag = gemm_role(lds, x, wqkv, gw, ws, (xcd >> 1) * 4 + rl, (xcd & 1) * 4 + cl, 2 * ((rl + cl) & 3), o STAMP_ARG);
  }
  out_phase(lds, ws, out, rt, ct, tag STAMP_ARG);
  STAMP(12);
  STAMP_FLUSH;
  CODE_END("fused");
}

#define STARG(o)

extern "C" void kernel_launch(void* const* d_in, const int* in_sizes, int n_in, void* d_out, int out_size, void* d_ws,
                              size_t ws_size, hipStream_t stream) {
  const u16* x = (const u16*)d_in[0];
  const u16* wqkv = (const u16*)d_in[1];
  const u16* wo = (const u16*)d_in[2];
  const u16* lar = (const u16*)d_in[4];
  const u16* aim = (const u16*)d_in[5];
  const u16* cre = (const u16*)d_in[6];
  const u16* cim = (const u16*)d_in[7];
  const u16* ldt = (const u16*)d_in[8];
  const u16* dsk = (const u16*)d_in[9];
  const u16* gw = (const u16*)d_in[10];
  char* ws = (char*)d_ws;
  (void)hipFuncSetAttribute((const void*)fused, hipFuncAttributeMaxDynamicSharedMemorySize, 163840);
  fused<<<256, 512, 163840, stream>>>(x, wqkv, gw, lar, aim, cre, cim, ldt, dsk, wo, ws, (u16*)d_out STARG(0));
}
```

```cpp
#include <hip/hip_runtime.h>
#include <stdint.h>

typedef unsigned short u16;
typedef unsigned long long u64;
typedef __attribute__((ext_vector_type(8))) __bf16 bf16x8;
typedef __attribute__((ext_vector_type(2))) __bf16 bf16x2;
typedef __attribute__((ext_vector_type(8))) _Float16 f16x8;
typedef __attribute__((ext_vector_type(2))) _Float16 f16x2;
typedef __attribute__((ext_vector_type(4))) short s16x4;
typedef __attribute__((ext_vector_type(16))) float f32x16;
typedef __attribute__((ext_vector_type(4))) float f32x4;
typedef __attribute__((ext_vector_type(2))) float f32x2;
typedef __attribute__((ext_vector_type(4))) unsigned u32x4;
typedef __attribute__((ext_vector_type(2))) unsigned u32x2;
#define LAS __attribute__((address_space(3)))
#define CAS __attribute__((address_space(4)))
#define DI __device__ __forceinline__

constexpr unsigned WS_PQF = 0;
constexpr unsigned WS_BT  = 2u << 20;
constexpr unsigned WS_KVP = 4u << 20;
constexpr unsigned WS_KVZ = 6u << 20;
constexpr unsigned WS_G   = (6u << 20) + 65536;
constexpr unsigned WS_FLAG = (6u << 20) + 131072;
constexpr unsigned WS_EPOCH = WS_FLAG + 65536;
constexpr unsigned WS_PF = WS_FLAG + 131072;
constexpr unsigned WS_USED = 7u << 20;
constexpr unsigned SPIN_CAP = 200000;

constexpr float LOG2E = 1.4426950408889634f;

#define STAMP_DECL
#define STAMP_PARM
#define STAMP_ARG
#define STAMP(i)
#define STAMP_VAL(i, v)
#define STAMP_FLUSH
#define SB __builtin_amdgcn_sched_barrier(0)
#define PIN(x) asm volatile("" : "+v"(x))
#define PINS(x) asm volatile("" : "+s"(x))

typedef LAS char* lptr;
DI float bf2f(unsigned v) { return __uint_as_float(v << 16); }
DI float bfhi(unsigned v) { return __uint_as_float(v & 0xffff0000u); }
DI unsigned pk_bf16(float a, float b) { f32x2 v = {a, b}; return __builtin_bit_cast(unsigned, __builtin_convertvector(v, bf16x2)); }
DI float rbf(float a) { return __uint_as_float(pk_bf16(a, a) << 16); }
DI f16x2 pk_f16(f32x2 v) { return __builtin_convertvector(v, f16x2); }
DI f32x2 cmul(f32x2 a, f32x2 b) { f32x2 t = a.xx * b; f32x2 bs = {-b.y, b.x}; return __builtin_elementwise_fma(a.yy, bs, t); }
DI void cmul2(f32x2& r0, f32x2 a0, f32x2 b0, f32x2& r1, f32x2 a1, f32x2 b1) {
  f32x2 t0, t1;
  asm("v_pk_mul_f32 %0, %2, %3 op_sel_hi:[0,1]\n\tv_pk_mul_f32 %1, %4, %5 op_sel_hi:[0,1]\n\t"
      "v_pk_fma_f32 %0, %2, %3, %0 op_sel:[1,1,0] op_sel_hi:[1,0,1] neg_lo:[0,1,0]\n\t"
      "v_pk_fma_f32 %1, %4, %5, %1 op_sel:[1,1,0] op_sel_hi:[1,0,1] neg_lo:[0,1,0]"
      : "=&v"(t0), "=&v"(t1) : "v"(a0), "v"(b0), "v"(a1), "v"(b1));
  r0 = t0; r1 = t1;
}
DI f16x2 pk_f16c(f32x2 v) { const f32x2 c = {v.x, -v.y}; return __builtin_convertvector(c, f16x2); }
DI f16x2 cmulh(f16x2 a, f16x2 b) { f16x2 t = a.xx * b; f16x2 bs = {-b.y, b.x}; return __builtin_elementwise_fma(a.yy, bs, t); }
DI u32x4 mk_rsrc(const void* p, unsigned bytes) { const u64 a = (u64)p; return u32x4{(unsigned)a, (unsigned)(a >> 32), bytes, 0x00020000u}; }
DI void dma16(u32x4 rs, unsigned voff, unsigned soff, lptr l) {
  asm volatile("s_mov_b32 m0, %3\n\ts_nop 0\n\tbuffer_load_dwordx4 %0, %1, %2 offen lds"
               :: "v"(voff), "s"(rs), "s"(soff), "s"((unsigned)(size_t)l) : "memory");
}
DI void dma16c(u32x4 rs, unsigned voff, unsigned soff, lptr l) {
  asm volatile("s_mov_b32 m0, %3\n\ts_nop 0\n\tbuffer_load_dwordx4 %0, %1, %2 offen sc1 lds"
               :: "v"(voff), "s"(rs), "s"(soff), "s"((unsigned)(size_t)l) : "memory");
}
DI u32x4 ld128c(u32x4 rs, unsigned voff) { u32x4 v; asm volatile("buffer_load_dwordx4 %0, %1, %2, 0 offen sc1" : "=v"(v) : "v"(voff), "s"(rs) : "memory"); return v; }
DI u32x2 poll64(u32x4 rs, unsigned voff) {
  u32x2 v; asm volatile("buffer_load_dwordx2 %0, %1, %2, 0 offen sc1\n\ts_waitcnt vmcnt(0)" : "=&v"(v) : "v"(voff), "s"(rs) : "memory"); return v;
}
DI u32x2 ld64c(u32x4 rs, unsigned voff) { u32x2 v; asm volatile("buffer_load_dwordx2 %0, %1, %2, 0 offen sc1" : "=v"(v) : "v"(voff), "s"(rs) : "memory"); return v; }
DI unsigned ld32c(u32x4 rs, unsigned voff) { unsigned v; asm volatile("buffer_load_dword %0, %1, %2, 0 offen sc1" : "=v"(v) : "v"(voff), "s"(rs) : "memory"); return v; }
DI int sig(int w, int n) { return 32 * (n >> 4) + ((n >> 3) & 1) + 16 * w + 8 * ((n >> 2) & 1) + 2 * (n & 3); }
DI bf16x8 lds_b128(lptr p) { return *(const LAS bf16x8*)p; }
DI s16x4 lds_tr(lptr p) { return __builtin_amdgcn_ds_read_tr16_b64_v4i16((LAS s16x4*)p); }
DI bf16x8 lds_tr2(lptr p0, lptr p1) { s16x4 a = lds_tr(p0), b = lds_tr(p1); return __builtin_bit_cast(bf16x8, __builtin_shufflevector(a, b, 0, 1, 2, 3, 4, 5, 6, 7)); }
DI f32x16 mfma32(bf16x8 a, bf16x8 b, f32x16 c) { return __builtin_amdgcn_mfma_f32_32x32x16_bf16(a, b, c, 0, 0, 0); }
DI f32x4 mfma16(bf16x8 a, bf16x8 b, f32x4 c) { return __builtin_amdgcn_mfma_f32_16x16x32_bf16(a, b, c, 0, 0, 0); }
DI f32x4 mfma16h(f16x8 a, f16x8 b, f32x4 c) { return __builtin_amdgcn_mfma_f32_16x16x32_f16(a, b, c, 0, 0, 0); }
DI void bar() { asm volatile("" ::: "memory"); __builtin_amdgcn_s_barrier(); asm volatile("" ::: "memory"); }
DI void lgkm0() { asm volatile("s_waitcnt lgkmcnt(0)" ::: "memory"); }
#define WAIT_VM(n) asm volatile("s_waitcnt vmcnt(" #n ")" ::: "memory")
DI float phi(float tp) {
  const float e = __builtin_amdgcn_fmed3f(__builtin_amdgcn_exp2f(tp * LOG2E), 0.f, 1.f);
  const int ti = __float_as_int(tp);
  return __int_as_float(ti > 0x358637bd ? ti : 0x358637bd) + e;
}
DI bf16x8 pack8(const f32x16& a, int o) {
  u32x4 p = {pk_bf16(a[o], a[o + 1]), pk_bf16(a[o + 2], a[o + 3]), pk_bf16(a[o + 4], a[o + 5]), pk_bf16(a[o + 6], a[o + 7])};
  return __builtin_bit_cast(bf16x8, p);
}
template <int CTRL, int RM = 0xf> DI int dppi(int v) { return __builtin_amdgcn_update_dpp(0, v, CTRL, RM, 0xf, true); }
template <int CTRL, int RM = 0xf> DI float dppf(float v) { return __int_as_float(dppi<CTRL, RM>(__float_as_int(v))); }
DI unsigned umax(unsigned a, unsigned b) { return a > b ? a : b; }
DI unsigned umin(unsigned a, unsigned b) { return a < b ? a : b; }
DI unsigned umax4(unsigned m, u32x4 v) { return umax(umax(umax(m, v.x), v.y), umax(v.z, v.w)); }
DI unsigned wave_umax(unsigned v) {
  v = umax(v, dppi<0x111>(v)); v = umax(v, dppi<0x112>(v)); v = umax(v, dppi<0x114>(v)); v = umax(v, dppi<0x118>(v));
  v = umax(v, dppi<0x142, 0xa>(v)); v = umax(v, dppi<0x143, 0xc>(v));
  return __builtin_amdgcn_readlane(v, 63);
}
DI float wave_sum(float v) {
  v += dppf<0x111>(v); v += dppf<0x112>(v); v += dppf<0x114>(v); v += dppf<0x118>(v);
  v += dppf<0x142, 0xa>(v); v += dppf<0x143, 0xc>(v);
  return __int_as_float(__builtin_amdgcn_readlane(__float_as_int(v), 63));
}
DI void dma4(u32x4 rs, unsigned voff, lptr l) {
  asm volatile("s_mov_b32 m0, %2\n\ts_nop 0\n\tbuffer_load_dword %0, %1, 0 offen sc1 lds" :: "v"(voff), "s"(rs), "s"((unsigned)(size_t)l) : "memory");
}
#define CODE_PREFETCH(NAME, dump, off) do { u64 pc_; unsigned len_; \
  asm volatile("s_getpc_b64 %0\n.L" NAME "_beg:\n\ts_mov_b32 %1, .L" NAME "_end-.L" NAME "_beg" : "=s"(pc_), "=s"(len_)); \
  asm volatile("s_mov_b32 m0, %2\n\ts_nop 0\n\tbuffer_load_dword %0, %1, 0 offen lds" \
               :: "v"(off), "s"(mk_rsrc((const void*)pc_, len_)), "s"((unsigned)(size_t)(dump)) : "memory"); } while (0)
#define CODE_END(NAME) asm volatile(".L" NAME "_end:")
typedef __amdgpu_buffer_rsrc_t rsrc_t;
DI rsrc_t make_rsrc(void* p, unsigned bytes) { return __builtin_amdgcn_make_buffer_rsrc(p, 0, bytes, 0x00020000); }
constexpr int WT = 16;
DI void st128(rsrc_t r, unsigned off, u32x4 v) { __builtin_amdgcn_raw_buffer_store_b128(v, r, off, 0, WT); }
DI void st64(rsrc_t r, unsigned off, u32x2 v) { __builtin_amdgcn_raw_buffer_store_b64(v, r, off, 0, WT); }
DI void st32(rsrc_t r, unsigned off, unsigned v) { __builtin_amdgcn_raw_buffer_store_b32(v, r, off, 0, WT); }

constexpr int L_PQ = 0, L_WO = 66560, L_KVZ = L_WO + 65536, L_BT = L_KVZ + 17408;
struct Out { u32x4 wor; unsigned woo; int wv; lptr lds; };
constexpr unsigned EMPTY = 0xffffffffu;
DI void wo_issue(const Out& o, int gr) { dma16(o.wor, o.woo, gr * 65536, o.lds + L_WO + o.wv * 1024 + gr * 8192); }
constexpr int L_DUMP = 163840 - 512, L_EPOCH = 163840 - 256;
DI u32x2 next_tag(lptr lds, int wv) {
  const u32x2 e = *(const volatile LAS u32x2*)(lds + L_EPOCH + wv * 8);
  const u64 t = (((u64)(unsigned)__builtin_amdgcn_readfirstlane(e.y) << 32) | (unsigned)__builtin_amdgcn_readfirstlane(e.x)) + 1;
  return u32x2{(unsigned)t, (unsigned)(t >> 32)};
}

constexpr int SLOT = 32768, NSLOT = 4;
constexpr int GA_EX = L_KVZ;
constexpr int GA_GW = GA_EX + 8 * 544 * 4;

DI u32x2 gemm_role(lptr lds, const u16* __restrict__ x, const u16* __restrict__ w, const u16* __restrict__ gw, char* ws,
                   int rb, int cg, int rot, const Out& wo STAMP_PARM) {
  const int tid = threadIdx.x, lane = tid & 63;
  const int wv = __builtin_amdgcn_readfirstlane(tid >> 6), rg = wv & 3, hp = wv >> 2;
  const u32x4 xr = mk_rsrc(x, 2048 * 1024), wr_ = mk_rsrc(w, 512 * 3072);
  const unsigned xrow = tid >> 3, xo = rb * 131072 + xrow * 1024 + (((tid & 7) ^ ((xrow >> 1) & 7)) << 4);
  const unsigned wrow = tid >> 4, wp = (tid & 15) ^ ((wrow & 3) << 2);
  const unsigned wo_ = cg * 128 + wrow * 3072 + 1024 + (wp >> 3) * 1024 + ((wp & 7) << 4);
  const unsigned qo = cg * 128 + xrow * 3072 + (((tid & 7) ^ (((xrow >> 1) & 1) << 2)) << 4);
  const lptr lw = lds + wv * 1024;
  auto issue = [&](int c, int i) {
    const lptr slot = lw + (c % NSLOT) * SLOT;
    if (c < 8) {
      const unsigned kc = (c + rot) & 7;
      if (i == 0) dma16(xr, xo, kc * 128, slot);
      if (i == 1) dma16(wr_, wo_, kc * (64 * 3072), slot + 16384);
      if (i == 2) dma16(xr, xo, kc * 128 + 65536, slot + 8192);
      if (i == 3) dma16(wr_, wo_, kc * (64 * 3072) + 32 * 3072, slot + 24576);
    } else {
      dma16(wr_, qo, (((c - 8) * 4 + i + rot) & 7) * (64 * 3072), slot + i * 8192);
    }
  };
  STAMP(0);
  const int gsel = hp == 0 ? ((cg - rot) & 7) : -1;
  if (hp == 0 && lane < 8) dma16(mk_rsrc(gw, 1024), lane * 16, cg * 128, lds + GA_GW + wv * 128);
#pragma unroll
  for (int c = 0; c < 3; ++c) {
#pragma unroll
    for (int i = 0; i < 4; ++i) issue(c, i);
    if (c < 2) bar();
  }
  const rsrc_t wsr = make_rsrc(ws, WS_USED);
  const unsigned pqo = WS_PQF + (((rb * 4 + rg) * 32 + cg * 4 + 2 * hp) * 64 + lane) * 16;
  const int h = lane >> 5, row = (rg >> 1) * 64 + sig(rg & 1, lane & 31);
  const int gi = lane >> 4, blk = gi & 1, q = (lane & 15) >> 2, p4 = lane & 3;
  lptr ap[4], bp[2];
#pragma unroll
  for (int s = 0; s < 4; ++s) ap[s] = lds + row * 128 + (((2 * s + h) ^ ((row >> 1) & 7)) << 4);
#pragma unroll
  for (int ct = 0; ct < 2; ++ct) bp[ct] = lds + 16384 + (8 * h + q) * 256 + (((4 * (2 * ct + hp) + 2 * blk + (p4 >> 1)) ^ (q << 2)) << 4) + (p4 & 1) * 8;
  f32x16 ak, av;
  float g0 = 0.f;
#pragma unroll
  for (int i = 0; i < 16; ++i) { ak[i] = 1e-6f; av[i] = 0.f; }
  bf16x8 xf[32], Bf[3][2];
  u32x2 tag;
  auto gate_steps = [&](int c) {
    if (gsel == c) {
      const lptr gp = lds + GA_GW + wv * 128 + h * 16;
      f32x16 ag = {};
#pragma unroll
      for (int i = 0; i < 4; ++i) ag = mfma32(lds_b128(gp + i * 32), xf[4 * c + i], ag);
      g0 = ag[0];
    }
    PIN(g0);
  };
  lptr qp = lds + (8 * h + q) * 128 + (((4 * hp + 2 * blk + (p4 >> 1)) ^ ((q >> 1) << 2)) << 4) + (p4 & 1) * 8;
  const int dp = lane & 31, e_ = lane & 15;
  lptr exw = lds + GA_EX + rg * 4352 + (2 * hp + (dp >> 4)) * 1024 + e_ * 64 + (((2 * h) ^ (e_ >> 2)) << 4);
  lptr ezw = lds + GA_EX + rg * 4352 + 4096 + (2 * hp + (dp >> 4)) * 64 + (((e_ >> 2) & 1) * 8 + (e_ & 3) + 4 * (e_ >> 3)) * 4;
  lptr exr = lds + GA_EX + (tid < 272 ? tid : 0) * 16;
  unsigned kpo = tid < 272 ? WS_KVP + (rb * 8 + cg) * 8704 + tid * 16 : 0xffff0000u;
  PIN(qp); PIN(exw); PIN(ezw); PIN(exr); PIN(kpo);
  f32x16 aq;
  STAMP(1);
#pragma unroll
  for (int n = 0; n < 32; ++n) {
    const int c = n >> 2, s = n & 3, so = (c & 1) * SLOT;
    if (s == 0) {
      if (c > 0) {
        lgkm0();
        if ((c & 1) == 0) {
          const int dlt = (c == 4) ? -2 * SLOT : 2 * SLOT;
#pragma unroll
          for (int i = 0; i < 4; ++i) { ap[i] += dlt; PIN(ap[i]); }
#pragma unroll
          for (int i = 0; i < 2; ++i) { bp[i] += dlt; PIN(bp[i]); }
        }
      }
      WAIT_VM(8);
      bar();
      if (c == 0) STAMP(2);
      SB;
    }
    if (n >= 2) { ak = mfma32(xf[n - 2], Bf[(n - 2) % 3][0], ak); SB; }
    xf[n] = lds_b128(ap[s] + so); Bf[n % 3][0] = lds_tr2(bp[0] + so + s * 4096, bp[0] + so + s * 4096 + 1024); SB;
    if (n >= 2) { av = mfma32(xf[n - 2], Bf[(n - 2) % 3][1], av); SB; }
    Bf[n % 3][1] = lds_tr2(bp[1] + so + s * 4096, bp[1] + so + s * 4096 + 1024); SB;
    if (c + 3 < 10) { issue(c + 3, s); SB; } else if (s < 3) { wo_issue(wo, s); SB; }
    if (n == 14) { tag = next_tag(lds, wv); SB; }
    if (n == 30) {
#pragma unroll
      for (int i = 0; i < 16; ++i) aq[i] = 1e-6f;
      PIN(aq); SB;
    }
    if (n > 0 && s == 0) { gate_steps(c - 1); SB; }
  }
  STAMP(3);
  f32x16 kv = {}, z = {};
  constexpr int PD = 5;
  bf16x8 Aq[PD + 1], ones;
#pragma unroll
  for (int i = 0; i < 8; ++i) ones[i] = (__bf16)1.0f;
  u32x4 pk[2], vv[2];
  auto rdq = [&](int m) { Aq[m % (PD + 1)] = lds_tr2(qp + m * 2048, qp + m * 2048 + 512); };
#pragma unroll
  for (int m = -2; m < 32; ++m) {
    if (m == -2) { lgkm0(); WAIT_VM(7); bar(); SB; rdq(0); rdq(1); SB; }
    if (m == 11) { lgkm0(); WAIT_VM(6); STAMP(4); bar(); SB; }
    if (m < 0) { ak = mfma32(xf[32 + m], Bf[(32 + m) % 3][0], ak); av = mfma32(xf[32 + m], Bf[(32 + m) % 3][1], av); SB; }
    else { aq = mfma32(Aq[m % (PD + 1)], xf[m], aq); SB; }
    if (m == -2) { rdq(2); SB; }
    if (m + PD < 32) { rdq(m + PD); SB; }
    if (m == 0) { vv[0] = __builtin_bit_cast(u32x4, pack8(av, 0)); vv[1] = __builtin_bit_cast(u32x4, pack8(av, 8)); PIN(vv[0]); PIN(vv[1]); SB; }
    if (m >= 1 && m < 9) {
      const int j = m - 1;
      unsigned p = pk_bf16(phi(ak[2 * j]), phi(ak[2 * j + 1]));
      PIN(p);
      pk[j >> 2][j & 3] = p;
      SB;
      if ((j & 3) == 3) {
        kv = mfma32(__builtin_bit_cast(bf16x8, pk[j >> 2]), __builtin_bit_cast(bf16x8, vv[j >> 2]), kv);
        z = mfma32(ones, __builtin_bit_cast(bf16x8, pk[j >> 2]), z);
        SB;
      }
      if (j == 1 || j == 4 || j == 6) { wo_issue(wo, 3 + j / 2 - (j == 1 ? 0 : 1)); SB; }
    }
    if (m == 10) {
      float sel[8];
#pragma unroll
      for (int i = 0; i < 8; ++i) { float a = kv[i], b = kv[8 + i]; PIN(a); PIN(b); sel[i] = dp < 16 ? a : b; }
      *(LAS f32x4*)exw = f32x4{sel[0], sel[1], sel[2], sel[3]};
      *(LAS f32x4*)((lptr)((unsigned)(size_t)exw ^ 16u)) = f32x4{sel[4], sel[5], sel[6], sel[7]};
      *(LAS float*)ezw = z[0];
      SB;
    }
    if (m == 11) {
      const LAS f32x4* ex = (const LAS f32x4*)exr;
      const u32x4 sum = __builtin_bit_cast(u32x4, (ex[0] + ex[272]) + (ex[544] + ex[816]));
      st128(wsr, kpo, u32x4{sum.x, tag.x, sum.y, tag.x});
      st128(wsr, kpo + 4352, u32x4{sum.z, tag.x, sum.w, tag.x});
      SB;
    }
    if (m == 13) { PIN(aq); gate_steps(7); SB; }
    if (m == 16 || m == 19) { wo_issue(wo, m == 16 ? 6 : 7); SB; }
  }
  PIN(aq);
  STAMP(5);
  {
    if (hp == 0) st32(wsr, h == 0 ? WS_G + cg * 8192 + (rb * 4 + rg) * 128 + lane * 4 : 0xfffffff0u, __float_as_uint(g0));
#pragma unroll
    for (int i = 0; i < 8; ++i) aq[i] = phi(aq[i]);
    st128(wsr, pqo, __builtin_bit_cast(u32x4, pack8(aq, 0)));
#pragma unroll
    for (int i = 8; i < 16; ++i) aq[i] = phi(aq[i]);
    st128(wsr, pqo + 1024, __builtin_bit_cast(u32x4, pack8(aq, 8)));
    STAMP(6);
    WAIT_VM(0);
  }
  STAMP(7);
  bar();
  STAMP(13);
  if (tid == 511) st64(wsr, WS_PF + (rb * 8 + cg) * 128, tag);
  return tag;
}

constexpr int SC_ROW = 2064;
constexpr int KC_CP = 4224, KC_CH = 2 * KC_CP;
constexpr int EX_BASE = 4 * KC_CH;
constexpr int U_BASE = 57 * SC_ROW, U_PAD = 224, U_CP = 4576, U_CH = 2 * U_CP;
constexpr int T_BASE = U_BASE + 4 * U_CH, T_CH = 1536;
static_assert(T_BASE + 4 * T_CH <= 163840 && U_BASE % 16 == 0, "lds");

DI f32x2 cexp_p(float l2, float rv) {
  const float m = __builtin_amdgcn_exp2f(l2), f = __builtin_amdgcn_fractf(rv);
  const f32x2 r = {m * __builtin_amdgcn_cosf(f), m * __builtin_amdgcn_sinf(f)};
  return r;
}
DI bf16x8 lds_2b64(lptr p) {
  const u32x2 a = *(const volatile LAS u32x2*)p, b = *(const volatile LAS u32x2*)(p + 8);
  const u32x4 v = {a.x, a.y, b.x, b.y};
  return __builtin_bit_cast(bf16x8, v);
}
DI unsigned perm(unsigned hi, unsigned lo, unsigned sel) { return __builtin_amdgcn_perm(hi, lo, sel); }
constexpr unsigned LL = 0x01000100, LH = 0x03020302, HL = 0x05040504, SWL = 0x01000302, SWH = 0x05040706, HL_LH = 0x03020504;
template <int E0, int E1, int E2, int E3>
DI void put4(unsigned base, f16x2 a, f16x2 b, f16x2 c, f16x2 d) {
  constexpr int O = E0 < 28 ? 0 : 28;
  static_assert((E1 < 28) == (E0 < 28) && (E2 < 28) == (E0 < 28) && (E3 < 28) == (E0 < 28), "one base");
  asm volatile("s_mov_b32 m0, %4\n\ts_nop 0\n\tds_write_addtid_b32 %0 offset:%5\n\tds_write_addtid_b32 %1 offset:%6\n\t"
               "ds_write_addtid_b32 %2 offset:%7\n\tds_write_addtid_b32 %3 offset:%8"
               :: "v"(a), "v"(b), "v"(c), "v"(d), "s"(base + O * SC_ROW), "i"((E0 - O) * SC_ROW), "i"((E1 - O) * SC_ROW),
                  "i"((E2 - O) * SC_ROW), "i"((E3 - O) * SC_ROW) : "memory");
}

DI u32x2 conv_role(lptr lds, const u16* __restrict__ x, const u16* __restrict__ lar, const u16* __restrict__ aim,
                   const u16* __restrict__ cre, const u16* __restrict__ cim, const u16* __restrict__ ldt,
                   const u16* __restrict__ dsk, char* ws, int ch0, int jrot, const Out& wo STAMP_PARM) {
  const int tid = threadIdx.x, lane = tid & 63;
  const int wv = __builtin_amdgcn_readfirstlane(tid >> 6), cl = wv & 3, half = wv >> 2;
  const int d = ch0 + cl;
  STAMP(0);
  const unsigned r_lar = ((const unsigned*)lar)[d * 64 + lane], r_aim = ((const unsigned*)aim)[d * 64 + lane];
  const unsigned r_cr = ((const unsigned*)cre)[d * 64 + lane], r_ci = ((const unsigned*)cim)[d * 64 + lane];
  const unsigned s_ldt = ((const CAS unsigned*)ldt)[d >> 1], s_dsk = ((const CAS unsigned*)dsk)[d >> 1];
  const int j = lane & 15, g = lane >> 4;
  const rsrc_t wsr = make_rsrc(ws, WS_USED);
  bar();
  const int cj = ch0 >> 2, cg = cj >> 4, rb = cj & 15;
  const unsigned kvo = wv == 4 && lane < 9 ? WS_KVZ + cj * 256 + lane * 16 : 0xffffffe0u;
  const int tt = (tid + jrot * 64) & 511;
  const char* xg = (const char*)x + ch0 * 2;
  const unsigned xoff = tt * 4096;
  u32x2 ur[4];
  ur[0] = *(const u32x2*)(xg + xoff); ur[1] = *(const u32x2*)(xg + xoff + 1024);
  u32x2 up;
  {
    const u32x4 zero = {0, 0, 0, 0};
    if (lane < 28) *(LAS u32x4*)(lds + U_BASE + cl * U_CH + half * U_CP + lane * 16) = zero;
  }
  const unsigned scb = (unsigned)(size_t)lds + cl * 512 + half * 256;
  const lptr fb = lds + cl * 512 + half * 256 + j * SC_ROW + g * 16;
  unsigned selA = lane < 32 ? LL : lane == 32 ? HL : SWH, selB = lane < 32 ? HL_LH : SWH, selC = lane < 32 ? LL : SWH, selE = lane < 32 ? LL : SWL;
  if (half == 0) { const bool odd = !(lane & 1); selA = odd ? LH : LL; selB = odd ? HL : LH; selC = odd ? HL : SWL; }
  PIN(selA); PIN(selB); PIN(selC); PIN(selE);
  const unsigned shl = half ? 0 : 16;
  const float f_ldt = (d & 1) ? bfhi(s_ldt) : bf2f(s_ldt), f_dsk = (d & 1) ? bfhi(s_dsk) : bf2f(s_dsk);
  const float dt = __int_as_float(__builtin_amdgcn_readfirstlane(__float_as_int(rbf(__expf(f_ldt)))));
  SB;
  const float l2 = -rbf(__builtin_amdgcn_exp2f(bfhi(r_lar << shl) * LOG2E)) * dt * LOG2E;
  const float rv = bfhi(r_aim << shl) * dt * 0.15915494309189535f;
  unsigned cm;
  {
    typedef __attribute__((ext_vector_type(2))) unsigned short u16x2;
    const u16x2 m2 = __builtin_elementwise_max(__builtin_bit_cast(u16x2, r_cr & 0x7fff7fffu), __builtin_bit_cast(u16x2, r_ci & 0x7fff7fffu));
    const unsigned m = __builtin_bit_cast(unsigned, m2);
    cm = umax(wave_umax(umax(m << 16, m)), 0x0da24260u);
  }
  const float cmax = __uint_as_float(cm), cinv = __builtin_amdgcn_rcpf(cmax);
  f32x4 kt[3];
  {
    f16x8 rf[4], t0[4];
    const f16x2 one = {(_Float16)1.f, (_Float16)0.f};
    const f32x2 E1 = cexp_p(l2, rv), E8 = cexp_p(l2 * 8.f, rv * 8.f), E128 = cexp_p(l2 * 128.f, rv * 128.f);
    f32x2 E2, E3, E4, E16, E24, E32, E64, E96, E256, E384, E512, H1, H2, H3, H4, dummy;
    const f32x2 H0 = {bfhi(r_cr << shl) * cinv, bfhi(r_ci << shl) * cinv};
    cmul2(E16, E8, E8, E256, E128, E128);
    cmul2(E32, E16, E16, E512, E256, E256);
    cmul2(E24, E16, E8, E2, E1, E1);
    cmul2(E64, E32, E32, H1, H0, E512);
    cmul2(E3, E2, E1, E4, E2, E2);
    cmul2(E96, E64, E32, H2, H1, E512);
    cmul2(E384, E256, E128, H3, H2, E512);
    {
      const f16x2 P1 = pk_f16c(E8), P2 = pk_f16c(E16), P3 = pk_f16c(E24), Q1 = pk_f16c(E32), Q2 = pk_f16c(E64), Q3 = pk_f16c(E96);
      put4<0, 1, 2, 3>(scb, one, P1, P2, P3);
      put4<4, 5, 6, 7>(scb, Q1, cmulh(Q1, P1), cmulh(Q1, P2), cmulh(Q1, P3));
      put4<8, 9, 10, 11>(scb, Q2, cmulh(Q2, P1), cmulh(Q2, P2), cmulh(Q2, P3));
      put4<12, 13, 14, 15>(scb, Q3, cmulh(Q3, P1), cmulh(Q3, P2), cmulh(Q3, P3));
    }
    SB;
    ur[2] = *(const u32x2*)(xg + xoff + 2048);
    SB;
    H4 = cmul(H3, E512); (void)dummy;
    const f16x2 e1 = pk_f16(E1), e2 = pk_f16(E2), e3 = pk_f16(E3), e4 = pk_f16(E4);
    const f16x2 e5 = cmulh(e4, e1), e6 = cmulh(e4, e2), e7 = cmulh(e4, e3);
    const f16x2 S1 = pk_f16(E128), S2 = pk_f16(E256), S3 = pk_f16(E384);
    const f16x2 h0 = pk_f16(H0), h1 = pk_f16(H1), h2 = pk_f16(H2), h3 = pk_f16(H3);
    {
      const f16x2 G1 = cmulh(h0, S1);
      put4<16, 17, 18, 19>(scb, h0, cmulh(h0, e1), cmulh(h0, e2), cmulh(h0, e3));
      put4<20, 21, 22, 23>(scb, cmulh(h0, e4), cmulh(h0, e5), cmulh(h0, e6), cmulh(h0, e7));
      put4<24, 25, 26, 27>(scb, G1, cmulh(G1, e1), cmulh(G1, e2), cmulh(G1, e3));
      put4<28, 29, 30, 31>(scb, cmulh(G1, e4), cmulh(G1, e5), cmulh(G1, e6), cmulh(G1, e7));
    }
#pragma unroll
    for (int s = 0; s < 4; ++s) { rf[s] = *(const LAS f16x8*)(fb + s * 64); t0[s] = *(const LAS f16x8*)(fb + 16 * SC_ROW + s * 64); }
    SB;
    ur[3] = *(const u32x2*)(xg + xoff + 3072);
    up = __builtin_amdgcn_raw_buffer_load_b64(make_rsrc((void*)x, 2048 * 1024), lane == 0 && tt > 0 ? ch0 * 2 + xoff - 1024 : 0xfffffff0u, 0, 0);
    SB;
    {
      const f16x2 G2 = cmulh(h0, S2), G3 = cmulh(h0, S3), G5 = cmulh(h1, S1), G6 = cmulh(h1, S2), G7 = cmulh(h1, S3);
      put4<32, 33, 34, 35>(scb, G2, cmulh(G2, e2), cmulh(G2, e4), cmulh(G2, e6));
      kt[0] = mfma16h(t0[0], rf[0], f32x4{0.f, 0.f, 0.f, 0.f});
      kt[0] = mfma16h(t0[1], rf[1], kt[0]);
      put4<36, 37, 38, 39>(scb, G3, cmulh(G3, e2), cmulh(G3, e4), cmulh(G3, e6));
      kt[0] = mfma16h(t0[2], rf[2], kt[0]);
      put4<40, 41, 42, 43>(scb, h1, cmulh(h1, e4), G5, cmulh(G5, e4));
      kt[0] = mfma16h(t0[3], rf[3], kt[0]);
      put4<44, 45, 46, 47>(scb, G6, cmulh(G6, e4), G7, cmulh(G7, e4));
    }
#pragma unroll
    for (int s = 0; s < 4; ++s) t0[s] = *(const LAS f16x8*)(fb + 32 * SC_ROW + s * 64);
    SB;
    put4<48, 49, 50, 51>(scb, h2, cmulh(h2, S1), cmulh(h2, S2), cmulh(h2, S3));
    put4<52, 53, 54, 55>(scb, h3, cmulh(h3, S1), cmulh(h3, S2), cmulh(h3, S3));
    *(LAS f16x2*)(lds + cl * 512 + half * 256 + lane * 4 + 56 * SC_ROW) = pk_f16(H4);
    f16x8 t2[4];
#pragma unroll
    for (int s = 0; s < 4; ++s) t2[s] = *(const LAS f16x8*)(fb + 48 * SC_ROW + s * 64);
    SB;
    kt[1] = f32x4{0.f, 0.f, 0.f, 0.f}; kt[2] = kt[1];
#pragma unroll
    for (int s = 0; s < 4; ++s) kt[1] = mfma16h(t0[s], rf[s], kt[1]);
#pragma unroll
    for (int s = 0; s < 4; ++s) kt[2] = mfma16h(t2[s], rf[s], kt[2]);
  }
  auto put_u = [&]() {
    const u32x2 pv = {(unsigned)__builtin_amdgcn_update_dpp((int)up.x, (int)ur[3].x, 0x138, 0xf, 0xf, false),
                      (unsigned)__builtin_amdgcn_update_dpp((int)up.y, (int)ur[3].y, 0x138, 0xf, 0xf, false)};
    const lptr ub = lds + U_BASE + (U_PAD + 4 * tt) * 2;
#pragma unroll
    for (int c = 0; c < 4; ++c) {
      const unsigned sel = (c & 1) ? 0x07060302 : 0x05040100;
      const unsigned p = pv[c >> 1], r0 = ur[0][c >> 1], r1 = ur[1][c >> 1], r2 = ur[2][c >> 1], r3 = ur[3][c >> 1];
      *(LAS u32x2*)(ub + c * U_CH) = u32x2{perm(r1, r0, sel), perm(r3, r2, sel)};
      *(LAS u32x2*)(ub + c * U_CH + U_CP) = u32x2{perm(r0, p, sel), perm(r2, r1, sel)};
    }
  };
  const lptr Tb = lds + T_BASE + cl * T_CH;
  {
    LAS f32x4* xw = (LAS f32x4*)(lds + cl * 512 + half * 256 + g * SC_ROW) + j;
    const LAS f32x4* xr = (const LAS f32x4*)(lds + cl * 512 + (half ^ 1) * 256 + g * SC_ROW) + j;
    if (half) xw[0] = kt[0]; else { xw[0] = kt[1]; xw[4 * SC_ROW / 16] = kt[2]; }
    const float ks = 2.f * dt * cmax;
    const lptr p1 = half ? Tb + (256 + 64 * g + (g < 2 ? 4 : 2) * j) * 2 : Tb + (128 * (g >> 1) + 4 * (g & 1) + 8 * j) * 2;
    lptr p1b = p1 + (g < 2 ? 4 : 64);
    PIN(p1b);
    lgkm0();
    STAMP(3);
    bar();
    if (half == 0) {
      f32x4 a0 = (kt[0] + xr[0]) * ks;
      if (lane == 0) a0[0] += f_dsk;
      *(LAS u32x2*)p1 = u32x2{pk_bf16(a0[0], a0[1]), pk_bf16(a0[2], a0[3])};
    } else {
      const f32x4 a1 = (kt[1] + xr[0]) * ks, a2 = (kt[2] + xr[4 * SC_ROW / 16]) * ks;
      *(LAS unsigned*)p1 = pk_bf16(a1[0], a1[1]);
      *(LAS unsigned*)p1b = pk_bf16(a1[2], a1[3]);
      const unsigned q0 = pk_bf16(a2[0], a2[1]), q1 = pk_bf16(a2[2], a2[3]);
      LAS u16* p2 = (LAS u16*)Tb + 512 + 64 * g + j;
      p2[0] = (u16)q0; p2[16] = (u16)(q0 >> 16); p2[32] = (u16)q1; p2[48] = (u16)(q1 >> 16);
    }
  }
  lgkm0();
  STAMP(4);
  bar();
  {
    const lptr kc = lds + cl * KC_CH + lane * 16;
    auto emit = [&](int Y0, unsigned P0, unsigned P1, unsigned P2, unsigned P3, unsigned P4) {
      *(LAS u32x4*)(kc + Y0 * 16) = u32x4{P0, P1, P2, P3};
      *(LAS u32x4*)(kc + KC_CP + Y0 * 16) = u32x4{P1, P2, P3, P4};
    };
    if (half == 0) {
      const LAS unsigned* p = (const LAS unsigned*)(Tb + ((575 - lane) >> 1) * 4);
      const unsigned D0 = p[32], D1 = p[33], F0 = p[0], F1 = p[1], Z = *(const LAS unsigned*)Tb;
      SB;
      const unsigned e1 = perm(D1, D0, selB), e0 = perm(D1, D0, selA), f1 = perm(F1, F0, selB), f0 = perm(F1, F0, selA);
      emit(0, e1, e1, perm(D1, D0, selC), e0, e0);
      emit(64, f1, f1, perm(F1, F0, selC), f0, f0);
      if (lane < 4) emit(256, lane == 0 ? perm(0, Z, SWL) : 0u, 0u, 0u, 0u, 0u);
    } else {
      const LAS unsigned* p = (const LAS unsigned*)(Tb + (255 - lane) * 4);
      const LAS unsigned* r = (const LAS unsigned*)(Tb + (lane < 32 ? 760 - 8 * lane : 1008 - 16 * lane));
      const unsigned F0 = p[0], F1 = p[1], D0 = r[0], D1 = r[1], D2 = r[2], D3 = r[3], D4 = r[4];
      SB;
      const unsigned e2 = perm(F1, F0, HL);
      emit(128, e2, e2, perm(F1, F0, LH), perm(F1, F0, SWL), perm(F1, F0, LL));
      emit(192, perm(D4, D2, selA), perm(lane < 32 ? D2 : D3, D1, selB), perm(D2, D1, selC), perm(D1, D0, selB), perm(D1, D0, selE));
    }
  }
  SB;
  put_u();
  const u32x2 tag = next_tag(lds, wv);
  lgkm0();
  STAMP(5);
  bar();
  const u32x4 wsl = mk_rsrc(ws, WS_USED);
  const int a_ = j;
  const lptr ab = lds + cl * KC_CH + ((a_ & 1) ? KC_CP + 4088 : 4096) - 8 * (a_ >> 1) + 16 * g - 3584 - 192 - half * 256;
  const lptr bb = lds + U_BASE + cl * U_CH + ((a_ & 1) ? 0 : U_CP) + (U_PAD + 32 * (a_ >> 1) + 8 * g) * 2 - 192 - half * 256;
  f32x4 acc[8];
#pragma unroll
  for (int i = 0; i < 8; ++i) acc[i] = f32x4{0.f, 0.f, 0.f, 0.f};
  LAS f32x4* exw = (LAS f32x4*)(lds + EX_BASE + wv * 4096) + lane;
  f32x4 keep[4];
  u32x4 k[4];
  constexpr unsigned DONE = 0xffffffe0u;
  const unsigned src = WS_KVP + (j * 8 + cg) * 8704;
  const unsigned ko = src + (rb >> 2) * 1024 + ((rb & 3) * 4 + cl) * 64 + ((g ^ (rb & 3)) << 4);
  const bool zl = wv == 4 && g == 0;
  unsigned ka[4] = {half ? ko : DONE, half ? ko + 4352 : DONE, zl ? src + 4096 + rb * 16 : DONE, zl ? src + 4352 + 4096 + rb * 16 : DONE};
  PIN(ka[0]); PIN(ka[1]); PIN(ka[2]); PIN(ka[3]);
  {
    u32x2 Al[2][8], Ah[2][8];
    bf16x8 B[2][8];
    auto rd = [&](int r, int k) {
      const int nb = r & 1, mu = k / 3, w = k % 3;
      if (w == 0) B[nb][mu] = lds_b128(bb + 512 * mu + 64 * (3 - r));
      if (w == 1) Al[nb][mu] = *(const volatile LAS u32x2*)(ab + 512 * (7 - mu) + 64 * (3 - r));
      if (w == 2) Ah[nb][mu] = *(const volatile LAS u32x2*)(ab + 512 * (7 - mu) + 64 * (3 - r) + 8);
    };
    auto AF = [&](int nb, int mu) { return __builtin_bit_cast(bf16x8, u32x4{Al[nb][mu].x, Al[nb][mu].y, Ah[nb][mu].x, Ah[nb][mu].y}); };
#pragma unroll
    for (int k = 0; k < 24; ++k) rd(0, k);
    SB;
#pragma unroll
    for (int r = 1; r < 4; ++r) {
      const int cb = (r - 1) & 1;
      int cnt = 0;

#pragma unroll
      for (int m = 0; m < 8; ++m)
#pragma unroll
        for (int n = 0; n <= m; ++n)
#pragma unroll
          for (int sw = 0; sw < 2; ++sw)
            if (m + n < 8 && (sw == 0 || n < m)) {
              const int mu = sw ? n : m, nu = sw ? m : n;
              acc[m + n] = mfma16(AF(cb, mu), B[cb][nu], acc[m + n]);
              SB;
              if (cnt % 3 != 2) rd(r, cnt - cnt / 3);
              else if (cnt == 14 || cnt == 29) wo_issue(wo, 2 * (r - 1) + (cnt == 29));
              else if ((cnt == 5 || cnt == 20) && r == 1) st128(wsr, WS_BT + ch0 * 4096 + (cnt == 5 ? 0 : 8192) + tid * 16, u32x4{EMPTY, EMPTY, EMPTY, EMPTY});
              else if (cnt == 2 && r == 1) st128(wsr, kvo, u32x4{EMPTY, EMPTY, EMPTY, EMPTY});
              else if ((cnt == 32 || cnt == 35) && r == 3) { k[(cnt - 32) / 3 * 2] = ld128c(wsl, ka[(cnt - 32) / 3 * 2]); k[(cnt - 32) / 3 * 2 + 1] = ld128c(wsl, ka[(cnt - 32) / 3 * 2 + 1]); }
              else if (cnt == 2 && r == 3) { WAIT_VM(3); st64(wsr, lane == 0 ? WS_FLAG + (cj * 8 + wv) * 8 : 0xfffffff0u, tag); }
              SB;
              ++cnt;
            }
    }
    auto tiles = [&](int t0) {
#pragma unroll
      for (int mu = 0; mu < 8; ++mu)
#pragma unroll
        for (int n = 0; n < 8; ++n) if (mu + n >= t0 && mu + n < t0 + 4) acc[mu + n] = mfma16(AF(1, mu), B[1][n], acc[mu + n]);
    };
    if (half) {
      tiles(0); SB; tiles(4);
#pragma unroll
      for (int i = 0; i < 4; ++i) { exw[64 * i] = acc[i]; keep[i] = acc[4 + i]; }
    } else {
      tiles(4); SB; tiles(0);
#pragma unroll
      for (int i = 0; i < 4; ++i) { exw[64 * i] = acc[4 + i]; keep[i] = acc[i]; }
    }
  }
  STAMP(6);
  {
    const LAS f32x4* exr = (const LAS f32x4*)(lds + EX_BASE + (wv ^ 4) * 4096) + lane;
    const unsigned bto = WS_BT + d * 4096 + half * 2048 + (g & 1) * 512 + (a_ >> 2) * 128 + (g >> 1) * 64 + (a_ & 3) * 16;
    u32x4 bt[2];
    lgkm0();
    STAMP(7);
    bar();
    f32x4 ex[4];
#pragma unroll
    for (int i = 0; i < 4; ++i) ex[i] = exr[64 * i];
    SB;
    STAMP(2);
    unsigned spin = 0;
    WAIT_VM(0);
    PIN(k[0]); PIN(k[1]); PIN(k[2]); PIN(k[3]);
    const lptr slice = lds + T_BASE;
    if (half) {
      unsigned val[8] = {0, 0, 0, 0, 0, 0, 0, 0};
#pragma unroll 1
      for (; spin < SPIN_CAP; ++spin) {
#pragma unroll
        for (int i = 0; i < 4; ++i) {
          const bool got = k[i].y == tag.x && k[i].w == tag.x && ka[i] != DONE;
          val[2 * i] = got ? k[i].x : val[2 * i]; val[2 * i + 1] = got ? k[i].z : val[2 * i + 1];
          ka[i] = got ? DONE : ka[i];
        }
        if (__all((ka[0] & ka[1] & ka[2] & ka[3]) == DONE)) break;
        asm volatile("buffer_load_dwordx4 %0, %4, %8, 0 offen sc1\n\tbuffer_load_dwordx4 %1, %5, %8, 0 offen sc1\n\t"
                     "buffer_load_dwordx4 %2, %6, %8, 0 offen sc1\n\tbuffer_load_dwordx4 %3, %7, %8, 0 offen sc1\n\ts_waitcnt vmcnt(0)"
                     : "=&v"(k[0]), "=&v"(k[1]), "=&v"(k[2]), "=&v"(k[3])
                     : "v"(ka[0]), "v"(ka[1]), "v"(ka[2]), "v"(ka[3]), "s"(wsl) : "memory");
      }
      auto rowsum = [&](unsigned u) { float v = __uint_as_float(u); v += dppf<0x111>(v); v += dppf<0x112>(v); v += dppf<0x114>(v); v += dppf<0x118>(v); return v; };
      const u32x2 kvs = {umin(pk_bf16(rowsum(val[0]), rowsum(val[1])), EMPTY - 1), umin(pk_bf16(rowsum(val[2]), rowsum(val[3])), EMPTY - 1)};
      if (j == 15) *(LAS u32x2*)(slice + (cl * 4 + g) * 8) = kvs;
      if (wv == 4) {
        const u32x2 zs = {umin(pk_bf16(rowsum(val[4]), rowsum(val[5])), EMPTY - 1), umin(pk_bf16(rowsum(val[6]), rowsum(val[7])), EMPTY - 1)};
        if (lane == 15) *(LAS u32x4*)(slice + 128) = u32x4{zs.x, zs.y, 0, 0};
      }
    }
    lgkm0();
    bar();
    PIN(ex[0]); PIN(ex[1]); PIN(ex[2]); PIN(ex[3]);
    if (wv == 4) st128(wsr, kvo, *(const LAS u32x4*)(slice + (lane < 9 ? lane : 0) * 16));
    STAMP(4);
#pragma unroll
    for (int i = 0; i < 4; i += 2) {
      const f32x4 v = keep[i] + ex[i], w = keep[i + 1] + ex[i + 1];
      const auto sx = __builtin_amdgcn_permlane16_swap(umin(pk_bf16(v[0], v[1]), EMPTY - 1), umin(pk_bf16(w[0], w[1]), EMPTY - 1), false, false);
      const auto sy = __builtin_amdgcn_permlane16_swap(umin(pk_bf16(v[2], v[3]), EMPTY - 1), umin(pk_bf16(w[2], w[3]), EMPTY - 1), false, false);
      bt[i >> 1] = u32x4{sx[0], sy[0], sx[1], sy[1]};
      PIN(bt[i >> 1]);
    }
    st128(wsr, bto, bt[0]); st128(wsr, bto + 1024, bt[1]);
    wo_issue(wo, 6); wo_issue(wo, 7);
    STAMP_VAL(3, st_[0] + spin);
    STAMP(13);
    return tag;
  }
}

DI void out_phase(lptr lds, char* ws, u16* __restrict__ out, int rt, int ct, u32x2 tag STAMP_PARM) {
  const int tid = threadIdx.x, lane = tid & 63;
  const int wv = __builtin_amdgcn_readfirstlane(tid >> 6), wr = wv & 1, hq = wv >> 1;
  const u32x4 wsl = mk_rsrc(ws, WS_USED);
  const int h = lane >> 5, t = lane & 31, trow = rt * 64 + sig(wr, t);
  const unsigned bo = WS_BT + (ct * 64 + wv * 8 + (lane >> 3)) * 4096 + rt * 128 + (((lane & 7) ^ (((lane >> 4) & 1) << 2)) << 4);
  const unsigned fo = lane < 8 ? WS_PF + ((rt >> 1) * 8 + lane) * 128 : lane < 24 ? WS_FLAG + 8 * ((ct * 16 + 2 * wv) * 8 + lane - 8)
                    : lane < 40 ? WS_FLAG + 8 * ((wv * 16 + lane - 24) * 8 + 4) : 0xfffffff0u;
  unsigned tries = 0;
  unsigned gp[4];
  const unsigned go = WS_G + h * 32768 + (rt * 2 + wr) * 128 + t * 4;
  bool b_arm;
#pragma unroll 1
  for (unsigned spin = 0; spin < SPIN_CAP; ++spin) {
    const u32x2 f = poll64(wsl, fo);
    const u64 bad = __ballot(f.x != tag.x || f.y != tag.y);
    ++tries;
    b_arm = (bad & 0xffff00ull) == 0;
    if ((bad & 0xffff0000ffull) == 0) break;
    __builtin_amdgcn_s_sleep(2);
  }
  STAMP(8);
  auto pq_piece = [&](int g) {
    dma16c(wsl, lane * 16, WS_PQF + rt * 65536 + (wv & 1) * 32768 + (wv >> 1) * 1024 + g * 4096, lds + L_PQ + (wv & 1) * 32768 + (wv >> 1) * 1024 + g * 4096);
  };
  {
    constexpr unsigned DONE = 0xffffffe0u;
    const unsigned ko = WS_KVZ + wv * 4096 + (lane >> 3) * 256 + (lane & 7) * 16;
    unsigned ka[3] = {ko, ko + 2048, lane < 16 ? WS_KVZ + wv * 4096 + lane * 256 + 128 : DONE};
    const lptr kl = lds + L_KVZ + wv * 2176 + (lane >> 5) * 544 + (lane & 31) * 16, kz = lds + L_KVZ + wv * 2176 + (lane >> 2) * 544 + 512 + (lane & 3) * 8;
    u32x4 k[3];
#pragma unroll
    for (int i = 0; i < 3; ++i) k[i] = ld128c(wsl, ka[i]);
    pq_piece(0); pq_piece(1);
    WAIT_VM(2);
    PIN(k[0]); PIN(k[1]); PIN(k[2]);
#pragma unroll 1
    for (unsigned spin = 0; spin < SPIN_CAP; ++spin) {
#pragma unroll
      for (int i = 0; i < 3; ++i) {
        if (umax4(0, k[i]) != EMPTY && ka[i] != DONE) {
          if (i < 2) *(LAS u32x4*)(kl + i * 1088) = k[i]; else *(LAS u32x2*)kz = u32x2{k[i].x, k[i].y};
          ka[i] = DONE;
        }
      }
      tries += 256;
      if (__all((ka[0] & ka[1] & ka[2]) == DONE)) break;
      __builtin_amdgcn_s_sleep(3);
      asm volatile("buffer_load_dwordx4 %0, %3, %6, 0 offen sc1\n\tbuffer_load_dwordx4 %1, %4, %6, 0 offen sc1\n\t"
                   "buffer_load_dwordx4 %2, %5, %6, 0 offen sc1\n\ts_waitcnt vmcnt(0)"
                   : "=&v"(k[0]), "=&v"(k[1]), "=&v"(k[2]) : "v"(ka[0]), "v"(ka[1]), "v"(ka[2]), "s"(wsl) : "memory");
    }
  }
  pq_piece(2); pq_piece(3);
  STAMP(9);
  STAMP_VAL(1, st_[0] + tries);
  const int gi = lane >> 4, blk = gi & 1, q = (lane & 15) >> 2, p4 = lane & 3;
  const lptr kvb = lds + L_KVZ + (t < 16 ? t : 16) * 32 + h * 16 + hq * 544;
  const lptr pqb = lds + L_PQ + wr * 32768 + lane * 16 + hq * 1024;
  lptr wob[2];
#pragma unroll
  for (int c = 0; c < 2; ++c)
    wob[c] = lds + L_WO + (4 * h + q) * 128 + (((2 * ((2 * c + blk + hq) & 3) + (p4 >> 1)) ^ ((q >> 1) << 2)) << 4) + (p4 & 1) * 8 + hq * 2048;
  const lptr bb = lds + L_BT + (hq * 16 + 4 * h + q) * 128 + (((4 * wr + 2 * blk + (p4 >> 1)) ^ ((q >> 1) << 2)) << 4) + (p4 & 1) * 8;
  char* dst = (char*)out + (unsigned)(trow * 1024 + ct * 128 + hq * 32 + h * 16);
  PIN(dst);
  float gt;
  u32x4 bchk;
  bool b_ok;
  f32x16 acc[2] = {}, nd[2];
  bf16x8 wf[2][2], kf[2], pf[2];
  lgkm0();
#pragma unroll
  for (int n = 0; n < 10; ++n) {
    const int a = n - 1, b = n - 2;
    if (n == 0) { WAIT_VM(2); bar(); STAMP(10); SB; }
    if (n == 2) { WAIT_VM(4); bar(); SB; }
    if (n == 4) { WAIT_VM(7); bar(); SB; }
    if (n == 6) { lgkm0(); WAIT_VM(5); bar(); SB; }
    if (a >= 0 && a < 8) { const f32x16 zero = {}; nd[a & 1] = mfma32(kf[a & 1], pf[a & 1], zero); SB; }
    if (n < 8) { kf[n & 1] = lds_b128(kvb + n * 2176); pf[n & 1] = lds_b128(pqb + n * 4096); SB; }
    if (n < 2) { pq_piece(2 * n + 4); SB; }
    if (n == 2) { dma16c(wsl, bo, 0, lds + L_BT + wv * 1024); SB; }
    if (n == 3) { gp[0] = ld32c(wsl, go); gp[1] = ld32c(wsl, go + 8192); SB; }
    if (b >= 0) {
      f32x16& v = nd[b & 1];
      PIN(v);
      const float r = __builtin_amdgcn_rcpf(v[8] + 1e-6f);
      float o[8];
#pragma unroll
      for (int e = 0; e < 8; ++e) { o[e] = v[e] * r; PIN(o[e]); }
      const u32x4 p = {pk_bf16(o[0], o[1]), pk_bf16(o[2], o[3]), pk_bf16(o[4], o[5]), pk_bf16(o[6], o[7])};
      acc[1] = mfma32(wf[b & 1][1], __builtin_bit_cast(bf16x8, p), acc[1]);
      acc[0] = mfma32(wf[b & 1][0], __builtin_bit_cast(bf16x8, p), acc[0]);
      SB;
    }
    if (a >= 0 && a < 8) {
#pragma unroll
      for (int c = 0; c < 2; ++c) wf[a & 1][c] = lds_tr2(wob[c] + a * 8192, wob[c] + a * 8192 + 1024);
      SB;
    }
    if (n < 2) { pq_piece(2 * n + 5); SB; }
    if (n == 3) { gp[2] = ld32c(wsl, go + 16384); gp[3] = ld32c(wsl, go + 24576); SB; }
    if (n == 8) {
      WAIT_VM(0);
      PIN(gp[0]); PIN(gp[1]); PIN(gp[2]); PIN(gp[3]);
      bchk = *(const volatile LAS u32x4*)(lds + L_BT + wv * 1024 + lane * 16);
      const unsigned gs = __float_as_uint((__uint_as_float(gp[0]) + __uint_as_float(gp[1])) + (__uint_as_float(gp[2]) + __uint_as_float(gp[3])));
      const auto gw2 = __builtin_amdgcn_permlane32_swap(gs, gs, false, false);
      gt = __builtin_amdgcn_rcpf(1.f + __builtin_amdgcn_exp2f(-(__uint_as_float(gw2[0]) + __uint_as_float(gw2[1])) * LOG2E));
      PIN(gt);
      SB;
    }
    if (n == 9) { b_ok = b_arm && __all(umax4(0, bchk) != EMPTY); SB; }
  }
  PIN(acc[0]); PIN(acc[1]);
  STAMP(11);
#pragma unroll 1
  for (unsigned spin = 0; spin < SPIN_CAP && !b_ok; ++spin) {
    __builtin_amdgcn_s_sleep(2);
    if (!b_arm) { const u32x2 f = poll64(wsl, fo); b_arm = (__ballot(f.x != tag.x || f.y != tag.y) & 0xffff00ull) == 0; }
    dma16c(wsl, bo, 0, lds + L_BT + wv * 1024);
    WAIT_VM(0);
    b_ok = b_arm && __all(umax4(0, *(const volatile LAS u32x4*)(lds + L_BT + wv * 1024 + lane * 16)) != EMPTY);
  }
  {
    const lptr xb = lds + L_PQ + wr * 32768 + lane * 16;
#pragma unroll
    for (int p = 3; p >= 1; --p) {
      const f32x16& v = acc[p >> 1];
      LAS f32x4* xw = (LAS f32x4*)(xb + (((hq + p) & 3) * 3 + p - 1) * 2048);
#pragma unroll
      for (int G = 0; G < 2; ++G) { const int i = 8 * (p & 1) + 4 * G; xw[64 * G] = f32x4{v[i], v[i + 1], v[i + 2], v[i + 3]}; }
    }
    const LAS f32x4* xr = (const LAS f32x4*)(xb + hq * 6144);
    lgkm0();
    bar();
    const u32x2 bv0 = __builtin_bit_cast(u32x2, lds_tr(bb)), bv1 = __builtin_bit_cast(u32x2, lds_tr(bb + 1024));
    u32x2 o[2];
#pragma unroll
    for (int G = 0; G < 2; ++G) {
      const unsigned b0 = G ? bv1.x : bv0.x, b1 = G ? bv1.y : bv0.y;
      const f32x4 bf = {bf2f(b0), bfhi(b0), bf2f(b1), bfhi(b1)};
      const f32x4 own = {acc[0][4 * G], acc[0][4 * G + 1], acc[0][4 * G + 2], acc[0][4 * G + 3]};
      const f32x4 r = (((own + xr[64 * G]) + (xr[64 * G + 128] + xr[64 * G + 256])) - bf) * gt + bf;
      o[G] = u32x2{pk_bf16(r[0], r[1]), pk_bf16(r[2], r[3])};
    }
    auto sx = __builtin_amdgcn_permlane32_swap(o[0].x, o[1].x, false, false);
    auto sy = __builtin_amdgcn_permlane32_swap(o[0].y, o[1].y, false, false);
    const u32x4 v = {sx[0], sy[0], sx[1], sy[1]};
    *(u32x4*)dst = v;
  }
  if (blockIdx.x == 0 && tid == 0) st64(make_rsrc(ws, WS_USED), WS_EPOCH, tag);
}

__global__ void __launch_bounds__(512, 2) fused(const u16* __restrict__ x, const u16* __restrict__ wqkv, const u16* __restrict__ gw,
                                                const u16* __restrict__ lar, const u16* __restrict__ aim, const u16* __restrict__ cre,
                                                const u16* __restrict__ cim, const u16* __restrict__ ldt, const u16* __restrict__ dsk,
                                                const u16* __restrict__ wo, char* ws, u16* __restrict__ out
) {
  STAMP_DECL;
  extern __shared__ __attribute__((aligned(16))) char lds_[];
  const lptr lds = (lptr)lds_;
  const int b = blockIdx.x, xcd = b & 7, idx = b >> 3, tid = threadIdx.x, lane = tid & 63;
  const int wv = __builtin_amdgcn_readfirstlane(tid >> 6);
  if (wv == 7) CODE_PREFETCH("fused", lds + L_DUMP, ((idx & 7) * 64 + lane) * 64);
  asm volatile("" :: "s"(x), "s"(wqkv), "s"(gw), "s"(lar), "s"(aim), "s"(cre), "s"(cim), "s"(ldt), "s"(dsk), "s"(wo), "s"(ws), "s"(out));
  if ((tid & 63) < 2) dma4(mk_rsrc(ws, WS_USED), WS_EPOCH + (tid & 63) * 4, lds + L_EPOCH + wv * 8);
  const int rt = (xcd >> 1) * 8 + (idx & 7), ct = (xcd & 1) * 4 + (idx >> 3);
  const Out o = {mk_rsrc(wo, 512 * 1024), (unsigned)(ct * 128 + (tid >> 3) * 1024 + (((tid & 7) ^ (((tid >> 4) & 1) << 2)) << 4)),
                 wv, lds};
  u32x2 tag;
  if (idx < 16) {
    tag = conv_role(lds, x, lar, aim, cre, cim, ldt, dsk, ws, xcd * 64 + idx * 4, idx, o STAMP_ARG);
  } else {
    const int rl = idx & 3, cl = (idx >> 2) & 3;
    tag = gemm_role(lds, x, wqkv, gw, ws, (xcd >> 1) * 4 + rl, (xcd & 1) * 4 + cl, 2 * ((rl + cl) & 3), o STAMP_ARG);
  }
  out_phase(lds, ws, out, rt, ct, tag STAMP_ARG);
  STAMP(12);
  STAMP_FLUSH;
  CODE_END("fused");
}

#define STARG(o)

extern "C" void kernel_launch(void* const* d_in, const int* in_sizes, int n_in, void* d_out, int out_size, void* d_ws,
                              size_t ws_size, hipStream_t stream) {
  const u16* x = (const u16*)d_in[0];
  const u16* wqkv = (const u16*)d_in[1];
  const u16* wo = (const u16*)d_in[2];
  const u16* lar = (const u16*)d_in[4];
  const u16* aim = (const u16*)d_in[5];
  const u16* cre = (const u16*)d_in[6];
  const u16* cim = (const u16*)d_in[7];
  const u16* ldt = (const u16*)d_in[8];
  const u16* dsk = (const u16*)d_in[9];
  const u16* gw = (const u16*)d_in[10];
  char* ws = (char*)d_ws;
  (void)hipFuncSetAttribute((const void*)fused, hipFuncAttributeMaxDynamicSharedMemorySize, 163840);
  fused<<<256, 512, 163840, stream>>>(x, wqkv, gw, lar, aim, cre, cim, ldt, dsk, wo, ws, (u16*)d_out STARG(0));
}
```

```cpp
#include <hip/hip_runtime.h>
#include <stdint.h>

typedef unsigned short u16;
typedef unsigned long long u64;
typedef __attribute__((ext_vector_type(8))) __bf16 bf16x8;
typedef __attribute__((ext_vector_type(2))) __bf16 bf16x2;
typedef __attribute__((ext_vector_type(8))) _Float16 f16x8;
typedef __attribute__((ext_vector_type(2))) _Float16 f16x2;
typedef __attribute__((ext_vector_type(4))) short s16x4;
typedef __attribute__((ext_vector_type(16))) float f32x16;
typedef __attribute__((ext_vector_type(4))) float f32x4;
typedef __attribute__((ext_vector_type(2))) float f32x2;
typedef __attribute__((ext_vector_type(4))) unsigned u32x4;
typedef __attribute__((ext_vector_type(2))) unsigned u32x2;
#define LAS __attribute__((address_space(3)))
#define CAS __attribute__((address_space(4)))
#define DI __device__ __forceinline__

constexpr unsigned WS_PQF = 0;
constexpr unsigned WS_BT  = 2u << 20;
constexpr unsigned WS_KVP = 4u << 20;
constexpr unsigned WS_KVZ = 6u << 20;
constexpr unsigned WS_G   = (6u << 20) + 65536;
constexpr unsigned WS_FLAG = (6u << 20) + 131072;
constexpr unsigned WS_EPOCH = WS_FLAG + 65536;
constexpr unsigned WS_PF = WS_FLAG + 131072;
constexpr unsigned WS_USED = 7u << 20;
constexpr unsigned SPIN_CAP = 200000;

constexpr float LOG2E = 1.4426950408889634f;

#define STAMP_DECL
#define STAMP_PARM
#define STAMP_ARG
#define STAMP(i)
#define STAMP_VAL(i, v)
#define STAMP_FLUSH
#define SB __builtin_amdgcn_sched_barrier(0)
#define PIN(x) asm volatile("" : "+v"(x))
#define PINS(x) asm volatile("" : "+s"(x))

typedef LAS char* lptr;
DI float bf2f(unsigned v) { return __uint_as_float(v << 16); }
DI float bfhi(unsigned v) { return __uint_as_float(v & 0xffff0000u); }
DI unsigned pk_bf16(float a, float b) { f32x2 v = {a, b}; return __builtin_bit_cast(unsigned, __builtin_convertvector(v, bf16x2)); }
DI float rbf(float a) { return __uint_as_float(pk_bf16(a, a) << 16); }
DI f16x2 pk_f16(f32x2 v) { return __builtin_convertvector(v, f16x2); }
DI f32x2 cmul(f32x2 a, f32x2 b) { f32x2 t = a.xx * b; f32x2 bs = {-b.y, b.x}; return __builtin_elementwise_fma(a.yy, bs, t); }
DI void cmul2(f32x2& r0, f32x2 a0, f32x2 b0, f32x2& r1, f32x2 a1, f32x2 b1) {
  f32x2 t0, t1;
  asm("v_pk_mul_f32 %0, %2, %3 op_sel_hi:[0,1]\n\tv_pk_mul_f32 %1, %4, %5 op_sel_hi:[0,1]\n\t"
      "v_pk_fma_f32 %0, %2, %3, %0 op_sel:[1,1,0] op_sel_hi:[1,0,1] neg_lo:[0,1,0]\n\t"
      "v_pk_fma_f32 %1, %4, %5, %1 op_sel:[1,1,0] op_sel_hi:[1,0,1] neg_lo:[0,1,0]"
      : "=&v"(t0), "=&v"(t1) : "v"(a0), "v"(b0), "v"(a1), "v"(b1));
  r0 = t0; r1 = t1;
}
DI f16x2 pk_f16c(f32x2 v) { const f32x2 c = {v.x, -v.y}; return __builtin_convertvector(c, f16x2); }
DI f16x2 cmulh(f16x2 a, f16x2 b) { f16x2 t = a.xx * b; f16x2 bs = {-b.y, b.x}; return __builtin_elementwise_fma(a.yy, bs, t); }
DI u32x4 mk_rsrc(const void* p, unsigned bytes) { const u64 a = (u64)p; return u32x4{(unsigned)a, (unsigned)(a >> 32), bytes, 0x00020000u}; }
DI void dma16(u32x4 rs, unsigned voff, unsigned soff, lptr l) {
  asm volatile("s_mov_b32 m0, %3\n\ts_nop 0\n\tbuffer_load_dwordx4 %0, %1, %2 offen lds"
               :: "v"(voff), "s"(rs), "s"(soff), "s"((unsigned)(size_t)l) : "memory");
}
DI void dma16c(u32x4 rs, unsigned voff, unsigned soff, lptr l) {
  asm volatile("s_mov_b32 m0, %3\n\ts_nop 0\n\tbuffer_load_dwordx4 %0, %1, %2 offen sc1 lds"
               :: "v"(voff), "s"(rs), "s"(soff), "s"((unsigned)(size_t)l) : "memory");
}
DI u32x4 ld128c(u32x4 rs, unsigned voff) { u32x4 v; asm volatile("buffer_load_dwordx4 %0, %1, %2, 0 offen sc1" : "=v"(v) : "v"(voff), "s"(rs) : "memory"); return v; }
DI u32x2 poll64(u32x4 rs, unsigned voff) {
  u32x2 v; asm volatile("buffer_load_dwordx2 %0, %1, %2, 0 offen sc1\n\ts_waitcnt vmcnt(0)" : "=&v"(v) : "v"(voff), "s"(rs) : "memory"); return v;
}
DI u32x2 ld64c(u32x4 rs, unsigned voff) { u32x2 v; asm volatile("buffer_load_dwordx2 %0, %1, %2, 0 offen sc1" : "=v"(v) : "v"(voff), "s"(rs) : "memory"); return v; }
DI unsigned ld32c(u32x4 rs, unsigned voff) { unsigned v; asm volatile("buffer_load_dword %0, %1, %2, 0 offen sc1" : "=v"(v) : "v"(voff), "s"(rs) : "memory"); return v; }
DI int sig(int w, int n) { return 32 * (n >> 4) + ((n >> 3) & 1) + 16 * w + 8 * ((n >> 2) & 1) + 2 * (n & 3); }
DI bf16x8 lds_b128(lptr p) { return *(const LAS bf16x8*)p; }
DI s16x4 lds_tr(lptr p) { return __builtin_amdgcn_ds_read_tr16_b64_v4i16((LAS s16x4*)p); }
DI bf16x8 lds_tr2(lptr p0, lptr p1) { s16x4 a = lds_tr(p0), b = lds_tr(p1); return __builtin_bit_cast(bf16x8, __builtin_shufflevector(a, b, 0, 1, 2, 3, 4, 5, 6, 7)); }
DI f32x16 mfma32(bf16x8 a, bf16x8 b, f32x16 c) { return __builtin_amdgcn_mfma_f32_32x32x16_bf16(a, b, c, 0, 0, 0); }
DI f32x4 mfma16(bf16x8 a, bf16x8 b, f32x4 c) { return __builtin_amdgcn_mfma_f32_16x16x32_bf16(a, b, c, 0, 0, 0); }
DI f32x4 mfma16h(f16x8 a, f16x8 b, f32x4 c) { return __builtin_amdgcn_mfma_f32_16x16x32_f16(a, b, c, 0, 0, 0); }
DI void bar() { asm volatile("" ::: "memory"); __builtin_amdgcn_s_barrier(); asm volatile("" ::: "memory"); }
DI void lgkm0() { asm volatile("s_waitcnt lgkmcnt(0)" ::: "memory"); }
#define WAIT_VM(n) asm volatile("s_waitcnt vmcnt(" #n ")" ::: "memory")
DI float phi(float tp) {
  const float e = __builtin_amdgcn_fmed3f(__builtin_amdgcn_exp2f(tp * LOG2E), 0.f, 1.f);
  const int ti = __float_as_int(tp);
  return __int_as_float(ti > 0x358637bd ? ti : 0x358637bd) + e;
}
DI bf16x8 pack8(const f32x16& a, int o) {
  u32x4 p = {pk_bf16(a[o], a[o + 1]), pk_bf16(a[o + 2], a[o + 3]), pk_bf16(a[o + 4], a[o + 5]), pk_bf16(a[o + 6], a[o + 7])};
  return __builtin_bit_cast(bf16x8, p);
}
template <int CTRL, int RM = 0xf> DI int dppi(int v) { return __builtin_amdgcn_update_dpp(0, v, CTRL, RM, 0xf, true); }
template <int CTRL, int RM = 0xf> DI float dppf(float v) { return __int_as_float(dppi<CTRL, RM>(__float_as_int(v))); }
DI unsigned umax(unsigned a, unsigned b) { return a > b ? a : b; }
DI unsigned umin(unsigned a, unsigned b) { return a < b ? a : b; }
DI unsigned umax4(unsigned m, u32x4 v) { return umax(umax(umax(m, v.x), v.y), umax(v.z, v.w)); }
DI unsigned wave_umax(unsigned v) {
  v = umax(v, dppi<0x111>(v)); v = umax(v, dppi<0x112>(v)); v = umax(v, dppi<0x114>(v)); v = umax(v, dppi<0x118>(v));
  v = umax(v, dppi<0x142, 0xa>(v)); v = umax(v, dppi<0x143, 0xc>(v));
  return __builtin_amdgcn_readlane(v, 63);
}
DI float wave_sum(float v) {
  v += dppf<0x111>(v); v += dppf<0x112>(v); v += dppf<0x114>(v); v += dppf<0x118>(v);
  v += dppf<0x142, 0xa>(v); v += dppf<0x143, 0xc>(v);
  return __int_as_float(__builtin_amdgcn_readlane(__float_as_int(v), 63));
}
DI void dma4(u32x4 rs, unsigned voff, lptr l) {
  asm volatile("s_mov_b32 m0, %2\n\ts_nop 0\n\tbuffer_load_dword %0, %1, 0 offen sc1 lds" :: "v"(voff), "s"(rs), "s"((unsigned)(size_t)l) : "memory");
}
#define CODE_PREFETCH(NAME, dump, off) do { u64 pc_; unsigned len_; \
  asm volatile("s_getpc_b64 %0\n.L" NAME "_beg:\n\ts_mov_b32 %1, .L" NAME "_end-.L" NAME "_beg" : "=s"(pc_), "=s"(len_)); \
  asm volatile("s_mov_b32 m0, %2\n\ts_nop 0\n\tbuffer_load_dword %0, %1, 0 offen lds" \
               :: "v"(off), "s"(mk_rsrc((const void*)pc_, len_)), "s"((unsigned)(size_t)(dump)) : "memory"); } while (0)
#define CODE_END(NAME) asm volatile(".L" NAME "_end:")
typedef __amdgpu_buffer_rsrc_t rsrc_t;
DI rsrc_t make_rsrc(void* p, unsigned bytes) { return __builtin_amdgcn_make_buffer_rsrc(p, 0, bytes, 0x00020000); }
constexpr int WT = 16;
DI void st128(rsrc_t r, unsigned off, u32x4 v) { __builtin_amdgcn_raw_buffer_store_b128(v, r, off, 0, WT); }
DI void st64(rsrc_t r, unsigned off, u32x2 v) { __builtin_amdgcn_raw_buffer_store_b64(v, r, off, 0, WT); }
DI void st32(rsrc_t r, unsigned off, unsigned v) { __builtin_amdgcn_raw_buffer_store_b32(v, r, off, 0, WT); }

constexpr int L_PQ = 0, L_WO = 66560, L_KVZ = L_WO + 65536, L_BT = L_KVZ + 17408;
struct Out { u32x4 wor; unsigned woo; int wv; lptr lds; };
constexpr unsigned EMPTY = 0xffffffffu;
DI void wo_issue(const Out& o, int gr) { dma16(o.wor, o.woo, gr * 65536, o.lds + L_WO + o.wv * 1024 + gr * 8192); }
constexpr int L_DUMP = 163840 - 512, L_EPOCH = 163840 - 256;
DI void epoch_load(lptr lds, char* ws, int wv) {
  if ((threadIdx.x & 63) < 2) dma4(mk_rsrc(ws, WS_USED), WS_EPOCH + (threadIdx.x & 63) * 4, lds + L_EPOCH + wv * 8);
}
DI u32x2 next_tag(lptr lds, int wv) {
  const u32x2 e = *(const volatile LAS u32x2*)(lds + L_EPOCH + wv * 8);
  const u64 t = (((u64)(unsigned)__builtin_amdgcn_readfirstlane(e.y) << 32) | (unsigned)__builtin_amdgcn_readfirstlane(e.x)) + 1;
  return u32x2{(unsigned)t, (unsigned)(t >> 32)};
}

constexpr int SLOT = 32768, NSLOT = 4;
constexpr int GA_EX = L_KVZ;
constexpr int GA_GW = GA_EX + 8 * 544 * 4;

DI u32x2 gemm_role(lptr lds, const u16* __restrict__ x, const u16* __restrict__ w, const u16* __restrict__ gw, char* ws,
                   int rb, int cg, int rot, const Out& wo STAMP_PARM) {
  const int tid = threadIdx.x, lane = tid & 63;
  const int wv = __builtin_amdgcn_readfirstlane(tid >> 6), rg = wv & 3, hp = wv >> 2;
  const u32x4 xr = mk_rsrc(x, 2048 * 1024), wr_ = mk_rsrc(w, 512 * 3072);
  const unsigned xrow = tid >> 3, xo = rb * 131072 + xrow * 1024 + (((tid & 7) ^ ((xrow >> 1) & 7)) << 4);
  const unsigned wrow = tid >> 4, wp = (tid & 15) ^ ((wrow & 3) << 2);
  const unsigned wo_ = cg * 128 + wrow * 3072 + 1024 + (wp >> 3) * 1024 + ((wp & 7) << 4);
  const unsigned qo = cg * 128 + xrow * 3072 + (((tid & 7) ^ (((xrow >> 1) & 1) << 2)) << 4);
  const lptr lw = lds + wv * 1024;
  auto issue = [&](int c, int i) {
    const lptr slot = lw + (c % NSLOT) * SLOT;
    if (c < 8) {
      const unsigned kc = (c + rot) & 7;
      if (i == 0) dma16(xr, xo, kc * 128, slot);
      if (i == 1) dma16(wr_, wo_, kc * (64 * 3072), slot + 16384);
      if (i == 2) dma16(xr, xo, kc * 128 + 65536, slot + 8192);
      if (i == 3) dma16(wr_, wo_, kc * (64 * 3072) + 32 * 3072, slot + 24576);
    } else {
      dma16(wr_, qo, (((c - 8) * 4 + i + rot) & 7) * (64 * 3072), slot + i * 8192);
    }
  };
  STAMP(0);
  const int gsel = hp == 0 ? ((cg - rot) & 7) : -1;
#pragma unroll
  for (int c = 0; c < 3; ++c) {
#pragma unroll
    for (int i = 0; i < 4; ++i) issue(c, i);
    if (c == 0 && hp == 0 && lane < 8) dma16(mk_rsrc(gw, 1024), lane * 16, cg * 128, lds + GA_GW + wv * 128);
    if (c < 2) bar();
  }
  epoch_load(lds, ws, wv);
  const rsrc_t wsr = make_rsrc(ws, WS_USED);
  const unsigned pqo = WS_PQF + (((rb * 4 + rg) * 32 + cg * 4 + 2 * hp) * 64 + lane) * 16;
  const int h = lane >> 5, row = (rg >> 1) * 64 + sig(rg & 1, lane & 31);
  const int gi = lane >> 4, blk = gi & 1, q = (lane & 15) >> 2, p4 = lane & 3;
  lptr ap[4], bp[2];
#pragma unroll
  for (int s = 0; s < 4; ++s) ap[s] = lds + row * 128 + (((2 * s + h) ^ ((row >> 1) & 7)) << 4);
#pragma unroll
  for (int ct = 0; ct < 2; ++ct) bp[ct] = lds + 16384 + (8 * h + q) * 256 + (((4 * (2 * ct + hp) + 2 * blk + (p4 >> 1)) ^ (q << 2)) << 4) + (p4 & 1) * 8;
  f32x16 ak, av;
  float g0 = 0.f;
#pragma unroll
  for (int i = 0; i < 16; ++i) { ak[i] = 1e-6f; av[i] = 0.f; }
  bf16x8 xf[32], Bf[3][2];
  u32x2 tag;
  auto gate_steps = [&](int c) {
    if (gsel == c) {
      const lptr gp = lds + GA_GW + wv * 128 + h * 16;
      f32x16 ag = {};
#pragma unroll
      for (int i = 0; i < 4; ++i) ag = mfma32(lds_b128(gp + i * 32), xf[4 * c + i], ag);
      g0 = ag[0];
    }
    PIN(g0);
  };
  lptr qp = lds + (8 * h + q) * 128 + (((4 * hp + 2 * blk + (p4 >> 1)) ^ ((q >> 1) << 2)) << 4) + (p4 & 1) * 8;
  const int dp = lane & 31, e_ = lane & 15;
  lptr exw = lds + GA_EX + rg * 4352 + (2 * hp + (dp >> 4)) * 1024 + e_ * 64 + (((2 * h) ^ (e_ >> 2)) << 4);
  lptr ezw = lds + GA_EX + rg * 4352 + 4096 + (2 * hp + (dp >> 4)) * 64 + (((e_ >> 2) & 1) * 8 + (e_ & 3) + 4 * (e_ >> 3)) * 4;
  lptr exr = lds + GA_EX + (tid < 272 ? tid : 0) * 16;
  unsigned kpo = tid < 272 ? WS_KVP + (rb * 8 + cg) * 8704 + tid * 16 : 0xffff0000u;
  PIN(qp); PIN(exw); PIN(ezw); PIN(exr); PIN(kpo);
  f32x16 aq;
  STAMP(1);
#pragma unroll
  for (int n = 0; n < 32; ++n) {
    const int c = n >> 2, s = n & 3, so = (c & 1) * SLOT;
    if (s == 0) {
      if (c > 0) {
        lgkm0();
        if ((c & 1) == 0) {
          const int dlt = (c == 4) ? -2 * SLOT : 2 * SLOT;
#pragma unroll
          for (int i = 0; i < 4; ++i) { ap[i] += dlt; PIN(ap[i]); }
#pragma unroll
          for (int i = 0; i < 2; ++i) { bp[i] += dlt; PIN(bp[i]); }
        }
      }
      if (c < 3) WAIT_VM(9); else WAIT_VM(8);
      bar();
      if (c == 0) STAMP(2);
      SB;
    }
    if (n >= 2) { ak = mfma32(xf[n - 2], Bf[(n - 2) % 3][0], ak); SB; }
    xf[n] = lds_b128(ap[s] + so); Bf[n % 3][0] = lds_tr2(bp[0] + so + s * 4096, bp[0] + so + s * 4096 + 1024); SB;
    if (n >= 2) { av = mfma32(xf[n - 2], Bf[(n - 2) % 3][1], av); SB; }
    Bf[n % 3][1] = lds_tr2(bp[1] + so + s * 4096, bp[1] + so + s * 4096 + 1024); SB;
    if (c + 3 < 10) { issue(c + 3, s); SB; } else if (s < 3) { wo_issue(wo, s); SB; }
    if (n == 14) { tag = next_tag(lds, wv); SB; }
    if (n == 30) {
#pragma unroll
      for (int i = 0; i < 16; ++i) aq[i] = 1e-6f;
      PIN(aq); SB;
    }
    if (n > 0 && s == 0) { gate_steps(c - 1); SB; }
  }
  STAMP(3);
  f32x16 kv = {}, z = {};
  constexpr int PD = 5;
  bf16x8 Aq[PD + 1], ones;
#pragma unroll
  for (int i = 0; i < 8; ++i) ones[i] = (__bf16)1.0f;
  u32x4 pk[2], vv[2];
  auto rdq = [&](int m) { Aq[m % (PD + 1)] = lds_tr2(qp + m * 2048, qp + m * 2048 + 512); };
#pragma unroll
  for (int m = -2; m < 32; ++m) {
    if (m == -2) { lgkm0(); WAIT_VM(7); bar(); SB; rdq(0); rdq(1); SB; }
    if (m == 11) { lgkm0(); WAIT_VM(6); STAMP(4); bar(); SB; }
    if (m < 0) { ak = mfma32(xf[32 + m], Bf[(32 + m) % 3][0], ak); av = mfma32(xf[32 + m], Bf[(32 + m) % 3][1], av); SB; }
    else { aq = mfma32(Aq[m % (PD + 1)], xf[m], aq); SB; }
    if (m == -2) { rdq(2); SB; }
    if (m + PD < 32) { rdq(m + PD); SB; }
    if (m == 0) { vv[0] = __builtin_bit_cast(u32x4, pack8(av, 0)); vv[1] = __builtin_bit_cast(u32x4, pack8(av, 8)); PIN(vv[0]); PIN(vv[1]); SB; }
    if (m >= 1 && m < 9) {
      const int j = m - 1;
      unsigned p = pk_bf16(phi(ak[2 * j]), phi(ak[2 * j + 1]));
      PIN(p);
      pk[j >> 2][j & 3] = p;
      SB;
      if ((j & 3) == 3) {
        kv = mfma32(__builtin_bit_cast(bf16x8, pk[j >> 2]), __builtin_bit_cast(bf16x8, vv[j >> 2]), kv);
        z = mfma32(ones, __builtin_bit_cast(bf16x8, pk[j >> 2]), z);
        SB;
      }
      if (j == 1 || j == 4 || j == 6) { wo_issue(wo, 3 + j / 2 - (j == 1 ? 0 : 1)); SB; }
    }
    if (m == 10) {
      float sel[8];
#pragma unroll
      for (int i = 0; i < 8; ++i) { float a = kv[i], b = kv[8 + i]; PIN(a); PIN(b); sel[i] = dp < 16 ? a : b; }
      *(LAS f32x4*)exw = f32x4{sel[0], sel[1], sel[2], sel[3]};
      *(LAS f32x4*)((lptr)((unsigned)(size_t)exw ^ 16u)) = f32x4{sel[4], sel[5], sel[6], sel[7]};
      *(LAS float*)ezw = z[0];
      SB;
    }
    if (m == 11) {
      const LAS f32x4* ex = (const LAS f32x4*)exr;
      const u32x4 sum = __builtin_bit_cast(u32x4, (ex[0] + ex[272]) + (ex[544] + ex[816]));
      st128(wsr, kpo, u32x4{sum.x, tag.x, sum.y, tag.x});
      st128(wsr, kpo + 4352, u32x4{sum.z, tag.x, sum.w, tag.x});
      SB;
    }
    if (m == 13) { PIN(aq); gate_steps(7); SB; }
    if (m == 16 || m == 19) { wo_issue(wo, m == 16 ? 6 : 7); SB; }
  }
  PIN(aq);
  STAMP(5);
  {
    if (hp == 0) st32(wsr, h == 0 ? WS_G + cg * 8192 + (rb * 4 + rg) * 128 + lane * 4 : 0xfffffff0u, __float_as_uint(g0));
#pragma unroll
    for (int i = 0; i < 8; ++i) aq[i] = phi(aq[i]);
    st128(wsr, pqo, __builtin_bit_cast(u32x4, pack8(aq, 0)));
#pragma unroll
    for (int i = 8; i < 16; ++i) aq[i] = phi(aq[i]);
    st128(wsr, pqo + 1024, __builtin_bit_cast(u32x4, pack8(aq, 8)));
    STAMP(6);
    WAIT_VM(0);
  }
  STAMP(7);
  bar();
  STAMP(13);
  if (tid == 511) st64(wsr, WS_PF + (rb * 8 + cg) * 128, tag);
  return tag;
}

constexpr int SC_ROW = 2064;
constexpr int KC_CP = 4224, KC_CH = 2 * KC_CP;
constexpr int EX_BASE = 4 * KC_CH;
constexpr int U_BASE = 57 * SC_ROW, U_PAD = 224, U_CP = 4576, U_CH = 2 * U_CP;
constexpr int T_BASE = U_BASE + 4 * U_CH, T_CH = 1536;
static_assert(T_BASE + 4 * T_CH <= 163840 && U_BASE % 16 == 0, "lds");

DI f32x2 cexp_p(float l2, float rv) {
  const float m = __builtin_amdgcn_exp2f(l2), f = __builtin_amdgcn_fractf(rv);
  const f32x2 r = {m * __builtin_amdgcn_cosf(f), m * __builtin_amdgcn_sinf(f)};
  return r;
}
DI bf16x8 lds_2b64(lptr p) {
  const u32x2 a = *(const volatile LAS u32x2*)p, b = *(const volatile LAS u32x2*)(p + 8);
  const u32x4 v = {a.x, a.y, b.x, b.y};
  return __builtin_bit_cast(bf16x8, v);
}
DI unsigned perm(unsigned hi, unsigned lo, unsigned sel) { return __builtin_amdgcn_perm(hi, lo, sel); }
constexpr unsigned LL = 0x01000100, LH = 0x03020302, HL = 0x05040504, SWL = 0x01000302, SWH = 0x05040706, HL_LH = 0x03020504;
template <int E0, int E1, int E2, int E3>
DI void put4(unsigned base, f16x2 a, f16x2 b, f16x2 c, f16x2 d) {
  constexpr int O = E0 < 28 ? 0 : 28;
  static_assert((E1 < 28) == (E0 < 28) && (E2 < 28) == (E0 < 28) && (E3 < 28) == (E0 < 28), "one base");
  asm volatile("s_mov_b32 m0, %4\n\ts_nop 0\n\tds_write_addtid_b32 %0 offset:%5\n\tds_write_addtid_b32 %1 offset:%6\n\t"
               "ds_write_addtid_b32 %2 offset:%7\n\tds_write_addtid_b32 %3 offset:%8"
               :: "v"(a), "v"(b), "v"(c), "v"(d), "s"(base + O * SC_ROW), "i"((E0 - O) * SC_ROW), "i"((E1 - O) * SC_ROW),
                  "i"((E2 - O) * SC_ROW), "i"((E3 - O) * SC_ROW) : "memory");
}

DI u32x2 conv_role(lptr lds, const u16* __restrict__ x, const u16* __restrict__ lar, const u16* __restrict__ aim,
                   const u16* __restrict__ cre, const u16* __restrict__ cim, const u16* __restrict__ ldt,
                   const u16* __restrict__ dsk, char* ws, int ch0, int jrot, const Out& wo STAMP_PARM) {
  const int tid = threadIdx.x, lane = tid & 63;
  const int wv = __builtin_amdgcn_readfirstlane(tid >> 6), cl = wv & 3, half = wv >> 2;
  const int d = ch0 + cl;
  STAMP(0);
  const unsigned r_lar = ((const unsigned*)lar)[d * 64 + lane], r_aim = ((const unsigned*)aim)[d * 64 + lane];
  const unsigned r_cr = ((const unsigned*)cre)[d * 64 + lane], r_ci = ((const unsigned*)cim)[d * 64 + lane];
  const unsigned s_ldt = ((const CAS unsigned*)ldt)[d >> 1], s_dsk = ((const CAS unsigned*)dsk)[d >> 1];
  const int j = lane & 15, g = lane >> 4;
  const rsrc_t wsr = make_rsrc(ws, WS_USED);
  bar();
  const int cj = ch0 >> 2, cg = cj >> 4, rb = cj & 15;
  const unsigned kvo = wv == 4 && lane < 9 ? WS_KVZ + cj * 256 + lane * 16 : 0xffffffe0u;
  const int tt = (tid + jrot * 64) & 511;
  const char* xg = (const char*)x + ch0 * 2;
  const unsigned xoff = tt * 4096;
  u32x2 ur[4];
  ur[0] = *(const u32x2*)(xg + xoff); ur[1] = *(const u32x2*)(xg + xoff + 1024);
  u32x2 up;
  {
    const u32x4 zero = {0, 0, 0, 0};
    if (lane < 28) *(LAS u32x4*)(lds + U_BASE + cl * U_CH + half * U_CP + lane * 16) = zero;
  }
  const unsigned scb = (unsigned)(size_t)lds + cl * 512 + half * 256;
  const lptr fb = lds + cl * 512 + half * 256 + j * SC_ROW + g * 16;
  unsigned selA = lane < 32 ? LL : lane == 32 ? HL : SWH, selB = lane < 32 ? HL_LH : SWH, selC = lane < 32 ? LL : SWH, selE = lane < 32 ? LL : SWL;
  if (half == 0) { const bool odd = !(lane & 1); selA = odd ? LH : LL; selB = odd ? HL : LH; selC = odd ? HL : SWL; }
  PIN(selA); PIN(selB); PIN(selC); PIN(selE);
  const unsigned shl = half ? 0 : 16;
  const float f_ldt = (d & 1) ? bfhi(s_ldt) : bf2f(s_ldt), f_dsk = (d & 1) ? bfhi(s_dsk) : bf2f(s_dsk);
  const float dt = __int_as_float(__builtin_amdgcn_readfirstlane(__float_as_int(rbf(__expf(f_ldt)))));
  SB;
  const float l2 = -rbf(__builtin_amdgcn_exp2f(bfhi(r_lar << shl) * LOG2E)) * dt * LOG2E;
  const float rv = bfhi(r_aim << shl) * dt * 0.15915494309189535f;
  unsigned cm;
  {
    typedef __attribute__((ext_vector_type(2))) unsigned short u16x2;
    const u16x2 m2 = __builtin_elementwise_max(__builtin_bit_cast(u16x2, r_cr & 0x7fff7fffu), __builtin_bit_cast(u16x2, r_ci & 0x7fff7fffu));
    const unsigned m = __builtin_bit_cast(unsigned, m2);
    cm = umax(wave_umax(umax(m << 16, m)), 0x0da24260u);
  }
  const float cmax = __uint_as_float(cm), cinv = __builtin_amdgcn_rcpf(cmax);
  f32x4 kt[3];
  {
    f16x8 rf[4], t0[4];
    const f16x2 one = {(_Float16)1.f, (_Float16)0.f};
    const f32x2 E1 = cexp_p(l2, rv), E8 = cexp_p(l2 * 8.f, rv * 8.f), E128 = cexp_p(l2 * 128.f, rv * 128.f);
    f32x2 E2, E3, E4, E16, E24, E32, E64, E96, E256, E384, E512, H1, H2, H3, H4, dummy;
    const f32x2 H0 = {bfhi(r_cr << shl) * cinv, bfhi(r_ci << shl) * cinv};
    cmul2(E16, E8, E8, E256, E128, E128);
    cmul2(E32, E16, E16, E512, E256, E256);
    cmul2(E24, E16, E8, E2, E1, E1);
    cmul2(E64, E32, E32, H1, H0, E512);
    cmul2(E3, E2, E1, E4, E2, E2);
    cmul2(E96, E64, E32, H2, H1, E512);
    cmul2(E384, E256, E128, H3, H2, E512);
    {
      const f16x2 P1 = pk_f16c(E8), P2 = pk_f16c(E16), P3 = pk_f16c(E24), Q1 = pk_f16c(E32), Q2 = pk_f16c(E64), Q3 = pk_f16c(E96);
      put4<0, 1, 2, 3>(scb, one, P1, P2, P3);
      put4<4, 5, 6, 7>(scb, Q1, cmulh(Q1, P1), cmulh(Q1, P2), cmulh(Q1, P3));
      put4<8, 9, 10, 11>(scb, Q2, cmulh(Q2, P1), cmulh(Q2, P2), cmulh(Q2, P3));
      put4<12, 13, 14, 15>(scb, Q3, cmulh(Q3, P1), cmulh(Q3, P2), cmulh(Q3, P3));
    }
    SB;
    ur[2] = *(const u32x2*)(xg + xoff + 2048);
    SB;
    H4 = cmul(H3, E512); (void)dummy;
    const f16x2 e1 = pk_f16(E1), e2 = pk_f16(E2), e3 = pk_f16(E3), e4 = pk_f16(E4);
    const f16x2 e5 = cmulh(e4, e1), e6 = cmulh(e4, e2), e7 = cmulh(e4, e3);
    const f16x2 S1 = pk_f16(E128), S2 = pk_f16(E256), S3 = pk_f16(E384);
    const f16x2 h0 = pk_f16(H0), h1 = pk_f16(H1), h2 = pk_f16(H2), h3 = pk_f16(H3);
    {
      const f16x2 G1 = cmulh(h0, S1);
      put4<16, 17, 18, 19>(scb, h0, cmulh(h0, e1), cmulh(h0, e2), cmulh(h0, e3));
      put4<20, 21, 22, 23>(scb, cmulh(h0, e4), cmulh(h0, e5), cmulh(h0, e6), cmulh(h0, e7));
      put4<24, 25, 26, 27>(scb, G1, cmulh(G1, e1), cmulh(G1, e2), cmulh(G1, e3));
      put4<28, 29, 30, 31>(scb, cmulh(G1, e4), cmulh(G1, e5), cmulh(G1, e6), cmulh(G1, e7));
    }
#pragma unroll
    for (int s = 0; s < 4; ++s) { rf[s] = *(const LAS f16x8*)(fb + s * 64); t0[s] = *(const LAS f16x8*)(fb + 16 * SC_ROW + s * 64); }
    SB;
    ur[3] = *(const u32x2*)(xg + xoff + 3072);
    up = __builtin_amdgcn_raw_buffer_load_b64(make_rsrc((void*)x, 2048 * 1024), lane == 0 && tt > 0 ? ch0 * 2 + xoff - 1024 : 0xfffffff0u, 0, 0);
    epoch_load(lds, ws, wv);
    SB;
    {
      const f16x2 G2 = cmulh(h0, S2), G3 = cmulh(h0, S3), G5 = cmulh(h1, S1), G6 = cmulh(h1, S2), G7 = cmulh(h1, S3);
      put4<32, 33, 34, 35>(scb, G2, cmulh(G2, e2), cmulh(G2, e4), cmulh(G2, e6));
      kt[0] = mfma16h(t0[0], rf[0], f32x4{0.f, 0.f, 0.f, 0.f});
      kt[0] = mfma16h(t0[1], rf[1], kt[0]);
      put4<36, 37, 38, 39>(scb, G3, cmulh(G3, e2), cmulh(G3, e4), cmulh(G3, e6));
      kt[0] = mfma16h(t0[2], rf[2], kt[0]);
      put4<40, 41, 42, 43>(scb, h1, cmulh(h1, e4), G5, cmulh(G5, e4));
      kt[0] = mfma16h(t0[3], rf[3], kt[0]);
      put4<44, 45, 46, 47>(scb, G6, cmulh(G6, e4), G7, cmulh(G7, e4));
    }
#pragma unroll
    for (int s = 0; s < 4; ++s) t0[s] = *(const LAS f16x8*)(fb + 32 * SC_ROW + s * 64);
    SB;
    put4<48, 49, 50, 51>(scb, h2, cmulh(h2, S1), cmulh(h2, S2), cmulh(h2, S3));
    put4<52, 53, 54, 55>(scb, h3, cmulh(h3, S1), cmulh(h3, S2), cmulh(h3, S3));
    *(LAS f16x2*)(lds + cl * 512 + half * 256 + lane * 4 + 56 * SC_ROW) = pk_f16(H4);
    f16x8 t2[4];
#pragma unroll
    for (int s = 0; s < 4; ++s) t2[s] = *(const LAS f16x8*)(fb + 48 * SC_ROW + s * 64);
    SB;
    kt[1] = f32x4{0.f, 0.f, 0.f, 0.f}; kt[2] = kt[1];
#pragma unroll
    for (int s = 0; s < 4; ++s) kt[1] = mfma16h(t0[s], rf[s], kt[1]);
#pragma unroll
    for (int s = 0; s < 4; ++s) kt[2] = mfma16h(t2[s], rf[s], kt[2]);
  }
  auto put_u = [&]() {
    const u32x2 pv = {(unsigned)__builtin_amdgcn_update_dpp((int)up.x, (int)ur[3].x, 0x138, 0xf, 0xf, false),
                      (unsigned)__builtin_amdgcn_update_dpp((int)up.y, (int)ur[3].y, 0x138, 0xf, 0xf, false)};
    const lptr ub = lds + U_BASE + (U_PAD + 4 * tt) * 2;
#pragma unroll
    for (int c = 0; c < 4; ++c) {
      const unsigned sel = (c & 1) ? 0x07060302 : 0x05040100;
      const unsigned p = pv[c >> 1], r0 = ur[0][c >> 1], r1 = ur[1][c >> 1], r2 = ur[2][c >> 1], r3 = ur[3][c >> 1];
      *(LAS u32x2*)(ub + c * U_CH) = u32x2{perm(r1, r0, sel), perm(r3, r2, sel)};
      *(LAS u32x2*)(ub + c * U_CH + U_CP) = u32x2{perm(r0, p, sel), perm(r2, r1, sel)};
    }
  };
  const lptr Tb = lds + T_BASE + cl * T_CH;
  {
    LAS f32x4* xw = (LAS f32x4*)(lds + cl * 512 + half * 256 + g * SC_ROW) + j;
    const LAS f32x4* xr = (const LAS f32x4*)(lds + cl * 512 + (half ^ 1) * 256 + g * SC_ROW) + j;
    if (half) xw[0] = kt[0]; else { xw[0] = kt[1]; xw[4 * SC_ROW / 16] = kt[2]; }
    const float ks = 2.f * dt * cmax;
    const lptr p1 = half ? Tb + (256 + 64 * g + (g < 2 ? 4 : 2) * j) * 2 : Tb + (128 * (g >> 1) + 4 * (g & 1) + 8 * j) * 2;
    lptr p1b = p1 + (g < 2 ? 4 : 64);
    PIN(p1b);
    lgkm0();
    STAMP(3);
    bar();
    if (half == 0) {
      f32x4 a0 = (kt[0] + xr[0]) * ks;
      if (lane == 0) a0[0] += f_dsk;
      *(LAS u32x2*)p1 = u32x2{pk_bf16(a0[0], a0[1]), pk_bf16(a0[2], a0[3])};
    } else {
      const f32x4 a1 = (kt[1] + xr[0]) * ks, a2 = (kt[2] + xr[4 * SC_ROW / 16]) * ks;
      *(LAS unsigned*)p1 = pk_bf16(a1[0], a1[1]);
      *(LAS unsigned*)p1b = pk_bf16(a1[2], a1[3]);
      const unsigned q0 = pk_bf16(a2[0], a2[1]), q1 = pk_bf16(a2[2], a2[3]);
      LAS u16* p2 = (LAS u16*)Tb + 512 + 64 * g + j;
      p2[0] = (u16)q0; p2[16] = (u16)(q0 >> 16); p2[32] = (u16)q1; p2[48] = (u16)(q1 >> 16);
    }
  }
  lgkm0();
  STAMP(4);
  bar();
  {
    const lptr kc = lds + cl * KC_CH + lane * 16;
    auto emit = [&](int Y0, unsigned P0, unsigned P1, unsigned P2, unsigned P3, unsigned P4) {
      *(LAS u32x4*)(kc + Y0 * 16) = u32x4{P0, P1, P2, P3};
      *(LAS u32x4*)(kc + KC_CP + Y0 * 16) = u32x4{P1, P2, P3, P4};
    };
    if (half == 0) {
      const LAS unsigned* p = (const LAS unsigned*)(Tb + ((575 - lane) >> 1) * 4);
      const unsigned D0 = p[32], D1 = p[33], F0 = p[0], F1 = p[1], Z = *(const LAS unsigned*)Tb;
      SB;
      const unsigned e1 = perm(D1, D0, selB), e0 = perm(D1, D0, selA), f1 = perm(F1, F0, selB), f0 = perm(F1, F0, selA);
      emit(0, e1, e1, perm(D1, D0, selC), e0, e0);
      emit(64, f1, f1, perm(F1, F0, selC), f0, f0);
      if (lane < 4) emit(256, lane == 0 ? perm(0, Z, SWL) : 0u, 0u, 0u, 0u, 0u);
    } else {
      const LAS unsigned* p = (const LAS unsigned*)(Tb + (255 - lane) * 4);
      const LAS unsigned* r = (const LAS unsigned*)(Tb + (lane < 32 ? 760 - 8 * lane : 1008 - 16 * lane));
      const unsigned F0 = p[0], F1 = p[1], D0 = r[0], D1 = r[1], D2 = r[2], D3 = r[3], D4 = r[4];
      SB;
      const unsigned e2 = perm(F1, F0, HL);
      emit(128, e2, e2, perm(F1, F0, LH), perm(F1, F0, SWL), perm(F1, F0, LL));
      emit(192, perm(D4, D2, selA), perm(lane < 32 ? D2 : D3, D1, selB), perm(D2, D1, selC), perm(D1, D0, selB), perm(D1, D0, selE));
    }
  }
  SB;
  put_u();
  WAIT_VM(0);
  const u32x2 tag = next_tag(lds, wv);
  lgkm0();
  STAMP(5);
  bar();
  const u32x4 wsl = mk_rsrc(ws, WS_USED);
  const int a_ = j;
  const lptr ab = lds + cl * KC_CH + ((a_ & 1) ? KC_CP + 4088 : 4096) - 8 * (a_ >> 1) + 16 * g - 3584 - 192 - half * 256;
  const lptr bb = lds + U_BASE + cl * U_CH + ((a_ & 1) ? 0 : U_CP) + (U_PAD + 32 * (a_ >> 1) + 8 * g) * 2 - 192 - half * 256;
  f32x4 acc[8];
#pragma unroll
  for (int i = 0; i < 8; ++i) acc[i] = f32x4{0.f, 0.f, 0.f, 0.f};
  LAS f32x4* exw = (LAS f32x4*)(lds + EX_BASE + wv * 4096) + lane;
  f32x4 keep[4];
  u32x4 k[3];
  constexpr unsigned DONE = 0xffffffe0u;
  const unsigned src = WS_KVP + (j * 8 + cg) * 8704;
  const unsigned ko = src + (rb >> 2) * 1024 + ((rb & 3) * 4 + cl) * 64 + ((g ^ (rb & 3)) << 4);
  const bool zl = wv == 4 && g < 2;
  unsigned ka[3] = {half ? ko : DONE, half ? ko + 4352 : DONE, zl ? src + g * 4352 + 4096 + rb * 16 : DONE};
  unsigned tagz = zl ? tag.x : 0u;
  lptr slw = lds + T_BASE + (j == 15 ? (cl * 4 + g) * 8 : 256), zsw = lds + T_BASE + (j == 15 && zl ? 128 + g * 4 : 256), slr = lds + T_BASE + (lane < 9 ? lane : 0) * 16;
  PIN(ka[0]); PIN(ka[1]); PIN(ka[2]); PIN(tagz); PIN(slw); PIN(zsw); PIN(slr);
  {
    u32x2 Al[2][8], Ah[2][8];
    bf16x8 B[2][8];
    auto rd = [&](int r, int k) {
      const int nb = r & 1, mu = k / 3, w = k % 3;
      if (w == 0) B[nb][mu] = lds_b128(bb + 512 * mu + 64 * (3 - r));
      if (w == 1) Al[nb][mu] = *(const volatile LAS u32x2*)(ab + 512 * (7 - mu) + 64 * (3 - r));
      if (w == 2) Ah[nb][mu] = *(const volatile LAS u32x2*)(ab + 512 * (7 - mu) + 64 * (3 - r) + 8);
    };
    auto AF = [&](int nb, int mu) { return __builtin_bit_cast(bf16x8, u32x4{Al[nb][mu].x, Al[nb][mu].y, Ah[nb][mu].x, Ah[nb][mu].y}); };
#pragma unroll
    for (int k = 0; k < 24; ++k) rd(0, k);
    SB;
#pragma unroll
    for (int r = 1; r < 4; ++r) {
      const int cb = (r - 1) & 1;
      int cnt = 0;

#pragma unroll
      for (int m = 0; m < 8; ++m)
#pragma unroll
        for (int n = 0; n <= m; ++n)
#pragma unroll
          for (int sw = 0; sw < 2; ++sw)
            if (m + n < 8 && (sw == 0 || n < m)) {
              const int mu = sw ? n : m, nu = sw ? m : n;
              acc[m + n] = mfma16(AF(cb, mu), B[cb][nu], acc[m + n]);
              SB;
              if (cnt % 3 != 2) rd(r, cnt - cnt / 3);
              else if (cnt == 14 || cnt == 29) wo_issue(wo, 2 * (r - 1) + (cnt == 29));
              else if ((cnt == 5 || cnt == 20) && r == 1) st128(wsr, WS_BT + ch0 * 4096 + (cnt == 5 ? 0 : 8192) + tid * 16, u32x4{EMPTY, EMPTY, EMPTY, EMPTY});
              else if (cnt == 2 && r == 1) st128(wsr, kvo, u32x4{EMPTY, EMPTY, EMPTY, EMPTY});
              else if ((cnt == 26 || cnt == 32 || cnt == 35) && r == 3) k[cnt == 26 ? 2 : cnt == 35] = ld128c(wsl, ka[cnt == 26 ? 2 : cnt == 35]);
              else if (cnt == 2 && r == 3) { WAIT_VM(3); st64(wsr, lane == 0 ? WS_FLAG + (cj * 8 + wv) * 8 : 0xfffffff0u, tag); }
              SB;
              ++cnt;
            }
    }
    auto tiles = [&](int t0) {
#pragma unroll
      for (int mu = 0; mu < 8; ++mu)
#pragma unroll
        for (int n = 0; n < 8; ++n) if (mu + n >= t0 && mu + n < t0 + 4) acc[mu + n] = mfma16(AF(1, mu), B[1][n], acc[mu + n]);
    };
    if (half) {
      tiles(0); SB; tiles(4);
#pragma unroll
      for (int i = 0; i < 4; ++i) { exw[64 * i] = acc[i]; keep[i] = acc[4 + i]; }
    } else {
      tiles(4); SB; tiles(0);
#pragma unroll
      for (int i = 0; i < 4; ++i) { exw[64 * i] = acc[4 + i]; keep[i] = acc[i]; }
    }
  }
  STAMP(6);
  {
    const LAS f32x4* exr = (const LAS f32x4*)(lds + EX_BASE + (wv ^ 4) * 4096) + lane;
    const unsigned bto = WS_BT + d * 4096 + half * 2048 + (g & 1) * 512 + (a_ >> 2) * 128 + (g >> 1) * 64 + (a_ & 3) * 16;
    u32x4 bt[2];
    lgkm0();
    STAMP(7);
    bar();
    f32x4 ex[4];
#pragma unroll
    for (int i = 0; i < 4; ++i) ex[i] = exr[64 * i];
    SB;
    STAMP(2);
    unsigned spin = 0;
    WAIT_VM(0);
    PIN(k[0]); PIN(k[1]); PIN(k[2]);
    if (half) {
#pragma unroll 1
      for (; spin < SPIN_CAP; ++spin) {
        const unsigned x = ((k[0].y ^ tag.x) | (k[0].w ^ tag.x) | (k[1].y ^ tag.x)) | ((k[1].w ^ tag.x) | (k[2].y ^ tagz) | (k[2].w ^ tagz));
        if (__all(x == 0)) break;
        asm volatile("buffer_load_dwordx4 %0, %3, %6, 0 offen sc1\n\tbuffer_load_dwordx4 %1, %4, %6, 0 offen sc1\n\t"
                     "buffer_load_dwordx4 %2, %5, %6, 0 offen sc1\n\ts_waitcnt vmcnt(0)"
                     : "=&v"(k[0]), "=&v"(k[1]), "=&v"(k[2]) : "v"(ka[0]), "v"(ka[1]), "v"(ka[2]), "s"(wsl) : "memory");
      }
      float v[6] = {__uint_as_float(k[0].x), __uint_as_float(k[0].z), __uint_as_float(k[1].x), __uint_as_float(k[1].z),
                    __uint_as_float(k[2].x), __uint_as_float(k[2].z)};
#pragma unroll
      for (int i = 0; i < 6; ++i) { v[i] += dppf<0x111>(v[i]); PIN(v[i]); }
#pragma unroll
      for (int i = 0; i < 6; ++i) { v[i] += dppf<0x112>(v[i]); PIN(v[i]); }
#pragma unroll
      for (int i = 0; i < 6; ++i) { v[i] += dppf<0x114>(v[i]); PIN(v[i]); }
#pragma unroll
      for (int i = 0; i < 6; ++i) { v[i] += dppf<0x118>(v[i]); PIN(v[i]); }
      *(LAS u32x2*)slw = u32x2{umin(pk_bf16(v[0], v[1]), EMPTY - 1), umin(pk_bf16(v[2], v[3]), EMPTY - 1)};
      *(LAS unsigned*)zsw = umin(pk_bf16(v[4], v[5]), EMPTY - 1);
    }
    lgkm0();
    bar();
    PIN(ex[0]); PIN(ex[1]); PIN(ex[2]); PIN(ex[3]);
    if (wv == 4) st128(wsr, kvo, *(const LAS u32x4*)slr);
    STAMP(4);
#pragma unroll
    for (int i = 0; i < 4; i += 2) {
      const f32x4 v = keep[i] + ex[i], w = keep[i + 1] + ex[i + 1];
      const auto sx = __builtin_amdgcn_permlane16_swap(umin(pk_bf16(v[0], v[1]), EMPTY - 1), umin(pk_bf16(w[0], w[1]), EMPTY - 1), false, false);
      const auto sy = __builtin_amdgcn_permlane16_swap(umin(pk_bf16(v[2], v[3]), EMPTY - 1), umin(pk_bf16(w[2], w[3]), EMPTY - 1), false, false);
      bt[i >> 1] = u32x4{sx[0], sy[0], sx[1], sy[1]};
      PIN(bt[i >> 1]);
    }
    st128(wsr, bto, bt[0]); st128(wsr, bto + 1024, bt[1]);
    wo_issue(wo, 6); wo_issue(wo, 7);
    STAMP_VAL(3, st_[0] + spin);
    STAMP(13);
    return tag;
  }
}

DI void out_phase(lptr lds, char* ws, u16* __restrict__ out, int rt, int ct, u32x2 tag STAMP_PARM) {
  const int tid = threadIdx.x, lane = tid & 63;
  const int wv = __builtin_amdgcn_readfirstlane(tid >> 6), wr = wv & 1, hq = wv >> 1;
  const u32x4 wsl = mk_rsrc(ws, WS_USED);
  const int h = lane >> 5, t = lane & 31, trow = rt * 64 + sig(wr, t);
  const unsigned bo = WS_BT + (ct * 64 + wv * 8 + (lane >> 3)) * 4096 + rt * 128 + (((lane & 7) ^ (((lane >> 4) & 1) << 2)) << 4);
  const unsigned fo = lane < 8 ? WS_PF + ((rt >> 1) * 8 + lane) * 128 : lane < 24 ? WS_FLAG + 8 * ((ct * 16 + 2 * wv) * 8 + lane - 8)
                    : lane < 40 ? WS_FLAG + 8 * ((wv * 16 + lane - 24) * 8 + 4) : 0xfffffff0u;
  unsigned tries = 0;
  unsigned gp[4];
  const unsigned go = WS_G + h * 32768 + (rt * 2 + wr) * 128 + t * 4;
  bool b_arm;
#pragma unroll 1
  for (unsigned spin = 0; spin < SPIN_CAP; ++spin) {
    const u32x2 f = poll64(wsl, fo);
    const u64 bad = __ballot(f.x != tag.x || f.y != tag.y);
    ++tries;
    b_arm = (bad & 0xffff00ull) == 0;
    if ((bad & 0xffff0000ffull) == 0) break;
    __builtin_amdgcn_s_sleep(2);
  }
  STAMP(8);
  auto pq_piece = [&](int g) {
    dma16c(wsl, lane * 16, WS_PQF + rt * 65536 + (wv & 1) * 32768 + (wv >> 1) * 1024 + g * 4096, lds + L_PQ + (wv & 1) * 32768 + (wv >> 1) * 1024 + g * 4096);
  };
  {
    constexpr unsigned DONE = 0xffffffe0u;
    const unsigned ko = WS_KVZ + wv * 4096 + (lane >> 3) * 256 + (lane & 7) * 16;
    unsigned ka[3] = {ko, ko + 2048, lane < 16 ? WS_KVZ + wv * 4096 + lane * 256 + 128 : DONE};
    const lptr kl = lds + L_KVZ + wv * 2176 + (lane >> 5) * 544 + (lane & 31) * 16, kz = lane < 16 ? lds + L_KVZ + wv * 2176 + (lane >> 2) * 544 + 512 + (lane & 3) * 8 : lds + L_DUMP;
    u32x4 k[3];
#pragma unroll
    for (int i = 0; i < 3; ++i) k[i] = ld128c(wsl, ka[i]);
    pq_piece(0); pq_piece(1);
    WAIT_VM(2);
    PIN(k[0]); PIN(k[1]); PIN(k[2]);
    if (__all(umax4(umax4(umax(k[2].x, k[2].y), k[0]), k[1]) != EMPTY)) {
      *(LAS u32x4*)kl = k[0]; *(LAS u32x4*)(kl + 1088) = k[1]; *(LAS u32x2*)kz = u32x2{k[2].x, k[2].y};
      tries += 256;
    } else {
#pragma unroll 1
      for (unsigned spin = 0; spin < SPIN_CAP; ++spin) {
#pragma unroll
        for (int i = 0; i < 3; ++i) {
          if ((i < 2 ? umax4(0, k[i]) : umax(k[i].x, k[i].y)) != EMPTY && ka[i] != DONE) {
            if (i < 2) *(LAS u32x4*)(kl + i * 1088) = k[i]; else *(LAS u32x2*)kz = u32x2{k[i].x, k[i].y};
            ka[i] = DONE;
          }
        }
        tries += 256;
        if (__all((ka[0] & ka[1] & ka[2]) == DONE)) break;
        __builtin_amdgcn_s_sleep(3);
        asm volatile("buffer_load_dwordx4 %0, %3, %6, 0 offen sc1\n\tbuffer_load_dwordx4 %1, %4, %6, 0 offen sc1\n\t"
                     "buffer_load_dwordx4 %2, %5, %6, 0 offen sc1\n\ts_waitcnt vmcnt(0)"
                     : "=&v"(k[0]), "=&v"(k[1]), "=&v"(k[2]) : "v"(ka[0]), "v"(ka[1]), "v"(ka[2]), "s"(wsl) : "memory");
      }
    }
  }
  pq_piece(2); pq_piece(3);
  STAMP(9);
  STAMP_VAL(1, st_[0] + tries);
  const int gi = lane >> 4, blk = gi & 1, q = (lane & 15) >> 2, p4 = lane & 3;
  const lptr kvb = lds + L_KVZ + (t < 16 ? t : 16) * 32 + h * 16 + hq * 544;
  const lptr pqb = lds + L_PQ + wr * 32768 + lane * 16 + hq * 1024;
  lptr wob[2];
#pragma unroll
  for (int c = 0; c < 2; ++c)
    wob[c] = lds + L_WO + (4 * h + q) * 128 + (((2 * ((2 * c + blk + hq) & 3) + (p4 >> 1)) ^ ((q >> 1) << 2)) << 4) + (p4 & 1) * 8 + hq * 2048;
  lptr bb = lds + L_BT + (hq * 16 + 4 * h + q) * 128 + (((4 * wr + 2 * blk + (p4 >> 1)) ^ ((q >> 1) << 2)) << 4) + (p4 & 1) * 8;
  unsigned dst = trow * 1024 + ct * 128 + hq * 32 + h * 16;
  PIN(dst); PIN(bb);
  float gt;
  u32x4 bchk;
  bool b_ok;
  f32x16 acc[2] = {}, nd[2];
  bf16x8 wf[2][2], kf[2], pf[2];
  lgkm0();
#pragma unroll
  for (int n = 0; n < 10; ++n) {
    const int a = n - 1, b = n - 2;
    if (n == 0) { WAIT_VM(2); bar(); STAMP(10); SB; }
    if (n == 2) { WAIT_VM(4); bar(); SB; }
    if (n == 4) { WAIT_VM(7); bar(); SB; }
    if (n == 6) { lgkm0(); WAIT_VM(5); bar(); SB; }
    if (a >= 0 && a < 8) { const f32x16 zero = {}; nd[a & 1] = mfma32(kf[a & 1], pf[a & 1], zero); SB; }
    if (n < 8) { kf[n & 1] = lds_b128(kvb + n * 2176); pf[n & 1] = lds_b128(pqb + n * 4096); SB; }
    if (n < 2) { pq_piece(2 * n + 4); SB; }
    if (n == 2) { dma16c(wsl, bo, 0, lds + L_BT + wv * 1024); SB; }
    if (n == 3) { gp[0] = ld32c(wsl, go); gp[1] = ld32c(wsl, go + 8192); SB; }
    if (b >= 0) {
      f32x16& v = nd[b & 1];
      PIN(v);
      const float r = __builtin_amdgcn_rcpf(v[8] + 1e-6f);
      float o[8];
#pragma unroll
      for (int e = 0; e < 8; ++e) { o[e] = v[e] * r; PIN(o[e]); }
      const u32x4 p = {pk_bf16(o[0], o[1]), pk_bf16(o[2], o[3]), pk_bf16(o[4], o[5]), pk_bf16(o[6], o[7])};
      acc[1] = mfma32(wf[b & 1][1], __builtin_bit_cast(bf16x8, p), acc[1]);
      acc[0] = mfma32(wf[b & 1][0], __builtin_bit_cast(bf16x8, p), acc[0]);
      SB;
    }
    if (a >= 0 && a < 8) {
#pragma unroll
      for (int c = 0; c < 2; ++c) wf[a & 1][c] = lds_tr2(wob[c] + a * 8192, wob[c] + a * 8192 + 1024);
      SB;
    }
    if (n < 2) { pq_piece(2 * n + 5); SB; }
    if (n == 3) { gp[2] = ld32c(wsl, go + 16384); gp[3] = ld32c(wsl, go + 24576); SB; }
    if (n == 8) {
      WAIT_VM(0);
      PIN(gp[0]); PIN(gp[1]); PIN(gp[2]); PIN(gp[3]);
      bchk = *(const volatile LAS u32x4*)(lds + L_BT + wv * 1024 + lane * 16);
      const unsigned gs = __float_as_uint((__uint_as_float(gp[0]) + __uint_as_float(gp[1])) + (__uint_as_float(gp[2]) + __uint_as_float(gp[3])));
      const auto gw2 = __builtin_amdgcn_permlane32_swap(gs, gs, false, false);
      gt = __builtin_amdgcn_rcpf(1.f + __builtin_amdgcn_exp2f(-(__uint_as_float(gw2[0]) + __uint_as_float(gw2[1])) * LOG2E));
      PIN(gt);
      SB;
    }
    if (n == 9) { b_ok = b_arm && __all(umax4(0, bchk) != EMPTY); SB; }
  }
  PIN(acc[0]); PIN(acc[1]);
  STAMP(11);
#pragma unroll 1
  for (unsigned spin = 0; spin < SPIN_CAP && !b_ok; ++spin) {
    __builtin_amdgcn_s_sleep(2);
    if (!b_arm) { const u32x2 f = poll64(wsl, fo); b_arm = (__ballot(f.x != tag.x || f.y != tag.y) & 0xffff00ull) == 0; }
    dma16c(wsl, bo, 0, lds + L_BT + wv * 1024);
    WAIT_VM(0);
    b_ok = b_arm && __all(umax4(0, *(const volatile LAS u32x4*)(lds + L_BT + wv * 1024 + lane * 16)) != EMPTY);
  }
  {
    const lptr xb = lds + L_PQ + wr * 32768 + lane * 16;
#pragma unroll
    for (int p = 3; p >= 1; --p) {
      const f32x16& v = acc[p >> 1];
      LAS f32x4* xw = (LAS f32x4*)(xb + (((hq + p) & 3) * 3 + p - 1) * 2048);
#pragma unroll
      for (int G = 0; G < 2; ++G) { const int i = 8 * (p & 1) + 4 * G; xw[64 * G] = f32x4{v[i], v[i + 1], v[i + 2], v[i + 3]}; }
    }
    const LAS f32x4* xr = (const LAS f32x4*)(xb + hq * 6144);
    lgkm0();
    bar();
    const u32x2 bv0 = __builtin_bit_cast(u32x2, lds_tr(bb)), bv1 = __builtin_bit_cast(u32x2, lds_tr(bb + 1024));
    u32x2 o[2];
#pragma unroll
    for (int G = 0; G < 2; ++G) {
      const unsigned b0 = G ? bv1.x : bv0.x, b1 = G ? bv1.y : bv0.y;
      const f32x4 bf = {bf2f(b0), bfhi(b0), bf2f(b1), bfhi(b1)};
      const f32x4 own = {acc[0][4 * G], acc[0][4 * G + 1], acc[0][4 * G + 2], acc[0][4 * G + 3]};
      const f32x4 r = (((own + xr[64 * G]) + (xr[64 * G + 128] + xr[64 * G + 256])) - bf) * gt + bf;
      o[G] = u32x2{pk_bf16(r[0], r[1]), pk_bf16(r[2], r[3])};
    }
    auto sx = __builtin_amdgcn_permlane32_swap(o[0].x, o[1].x, false, false);
    auto sy = __builtin_amdgcn_permlane32_swap(o[0].y, o[1].y, false, false);
    const u32x4 v = {sx[0], sy[0], sx[1], sy[1]};
    __builtin_amdgcn_raw_buffer_store_b128(v, make_rsrc(out, 2048 * 1024), dst, 0, 0);
  }
  if (blockIdx.x == 0 && tid == 0) st64(make_rsrc(ws, WS_USED), WS_EPOCH, tag);
}

__global__ void __launch_bounds__(512, 2) fused(const u16* __restrict__ x, const u16* __restrict__ wqkv, const u16* __restrict__ gw,
                                                const u16* __restrict__ lar, const u16* __restrict__ aim, const u16* __restrict__ cre,
                                                const u16* __restrict__ cim, const u16* __restrict__ ldt, const u16* __restrict__ dsk,
                                                const u16* __restrict__ wo, char* ws, u16* __restrict__ out
) {
  STAMP_DECL;
  extern __shared__ __attribute__((aligned(16))) char lds_[];
  const lptr lds = (lptr)lds_;
  const int b = blockIdx.x, xcd = b & 7, idx = b >> 3, tid = threadIdx.x, lane = tid & 63;
  const int wv = __builtin_amdgcn_readfirstlane(tid >> 6);
  if (wv == 7 && idx < 16) CODE_PREFETCH("fused", lds + L_DUMP, ((idx & 7) * 64 + lane) * 64);
  asm volatile("" :: "s"(x), "s"(wqkv), "s"(gw), "s"(lar), "s"(aim), "s"(cre), "s"(cim), "s"(ldt), "s"(dsk), "s"(wo), "s"(ws), "s"(out));
  const int rt = (xcd >> 1) * 8 + (idx & 7), ct = (xcd & 1) * 4 + (idx >> 3);
  const Out o = {mk_rsrc(wo, 512 * 1024), (unsigned)(ct * 128 + (tid >> 3) * 1024 + (((tid & 7) ^ (((tid >> 4) & 1) << 2)) << 4)),
                 wv, lds};
  u32x2 tag;
  if (idx < 16) {
    tag = conv_role(lds, x, lar, aim, cre, cim, ldt, dsk, ws, xcd * 64 + idx * 4, idx, o STAMP_ARG);
  } else {
    const int rl = idx & 3, cl = (idx >> 2) & 3;
    tag = gemm_role(lds, x, wqkv, gw, ws, (xcd >> 1) * 4 + rl, (xcd & 1) * 4 + cl, 2 * ((rl + cl) & 3), o STAMP_ARG);
  }
  out_phase(lds, ws, out, rt, ct, tag STAMP_ARG);
  STAMP(12);
  STAMP_FLUSH;
  CODE_END("fused");
}

#define STARG(o)

extern "C" void kernel_launch(void* const* d_in, const int* in_sizes, int n_in, void* d_out, int out_size, void* d_ws,
                              size_t ws_size, hipStream_t stream) {
  const u16* x = (const u16*)d_in[0];
  const u16* wqkv = (const u16*)d_in[1];
  const u16* wo = (const u16*)d_in[2];
  const u16* lar = (const u16*)d_in[4];
  const u16* aim = (const u16*)d_in[5];
  const u16* cre = (const u16*)d_in[6];
  const u16* cim = (const u16*)d_in[7];
  const u16* ldt = (const u16*)d_in[8];
  const u16* dsk = (const u16*)d_in[9];
  const u16* gw = (const u16*)d_in[10];
  char* ws = (char*)d_ws;
  (void)hipFuncSetAttribute((const void*)fused, hipFuncAttributeMaxDynamicSharedMemorySize, 163840);
  fused<<<256, 512, 163840, stream>>>(x, wqkv, gw, lar, aim, cre, cim, ldt, dsk, wo, ws, (u16*)d_out STARG(0));
}
```

```cpp
#include <hip/hip_runtime.h>
#include <stdint.h>

typedef unsigned short u16;
typedef unsigned long long u64;
typedef __attribute__((ext_vector_type(8))) __bf16 bf16x8;
typedef __attribute__((ext_vector_type(2))) __bf16 bf16x2;
typedef __attribute__((ext_vector_type(8))) _Float16 f16x8;
typedef __attribute__((ext_vector_type(2))) _Float16 f16x2;
typedef __attribute__((ext_vector_type(4))) short s16x4;
typedef __attribute__((ext_vector_type(16))) float f32x16;
typedef __attribute__((ext_vector_type(4))) float f32x4;
typedef __attribute__((ext_vector_type(2))) float f32x2;
typedef __attribute__((ext_vector_type(4))) unsigned u32x4;
typedef __attribute__((ext_vector_type(2))) unsigned u32x2;
#define LAS __attribute__((address_space(3)))
#define CAS __attribute__((address_space(4)))
#define DI __device__ __forceinline__

constexpr unsigned WS_PQF = 0;
constexpr unsigned WS_BT  = 2u << 20;
constexpr unsigned WS_KVP = 4u << 20;
constexpr unsigned WS_KVZ = 6u << 20;
constexpr unsigned WS_G   = (6u << 20) + 65536;
constexpr unsigned WS_FLAG = (6u << 20) + 131072;
constexpr unsigned WS_EPOCH = WS_FLAG + 65536;
constexpr unsigned WS_PF = WS_FLAG + 131072;
constexpr unsigned WS_USED = 7u << 20;
constexpr unsigned SPIN_CAP = 200000;

constexpr float LOG2E = 1.4426950408889634f;

#define SB __builtin_amdgcn_sched_barrier(0)
#define PIN(x) asm volatile("" : "+v"(x))
#define PINS(x) asm volatile("" : "+s"(x))

typedef LAS char* lptr;
DI float bf2f(unsigned v) { return __uint_as_float(v << 16); }
DI float bfhi(unsigned v) { return __uint_as_float(v & 0xffff0000u); }
DI unsigned pk_bf16(float a, float b) { f32x2 v = {a, b}; return __builtin_bit_cast(unsigned, __builtin_convertvector(v, bf16x2)); }
DI float rbf(float a) { return __uint_as_float(pk_bf16(a, a) << 16); }
DI f16x2 pk_f16(f32x2 v) { return __builtin_convertvector(v, f16x2); }
DI f32x2 cmul(f32x2 a, f32x2 b) { f32x2 t = a.xx * b; f32x2 bs = {-b.y, b.x}; return __builtin_elementwise_fma(a.yy, bs, t); }
DI void cmul2(f32x2& r0, f32x2 a0, f32x2 b0, f32x2& r1, f32x2 a1, f32x2 b1) {
  f32x2 t0, t1;
  asm("v_pk_mul_f32 %0, %2, %3 op_sel_hi:[0,1]\n\tv_pk_mul_f32 %1, %4, %5 op_sel_hi:[0,1]\n\t"
      "v_pk_fma_f32 %0, %2, %3, %0 op_sel:[1,1,0] op_sel_hi:[1,0,1] neg_lo:[0,1,0]\n\t"
      "v_pk_fma_f32 %1, %4, %5, %1 op_sel:[1,1,0] op_sel_hi:[1,0,1] neg_lo:[0,1,0]"
      : "=&v"(t0), "=&v"(t1) : "v"(a0), "v"(b0), "v"(a1), "v"(b1));
  r0 = t0; r1 = t1;
}
DI f16x2 pk_f16c(f32x2 v) { const f32x2 c = {v.x, -v.y}; return __builtin_convertvector(c, f16x2); }
DI f16x2 cmulh(f16x2 a, f16x2 b) { f16x2 t = a.xx * b; f16x2 bs = {-b.y, b.x}; return __builtin_elementwise_fma(a.yy, bs, t); }
DI u32x4 mk_rsrc(const void* p, unsigned bytes) { const u64 a = (u64)p; return u32x4{(unsigned)a, (unsigned)(a >> 32), bytes, 0x00020000u}; }
DI void dma16(u32x4 rs, unsigned voff, unsigned soff, lptr l) {
  asm volatile("s_mov_b32 m0, %3\n\ts_nop 0\n\tbuffer_load_dwordx4 %0, %1, %2 offen lds"
               :: "v"(voff), "s"(rs), "s"(soff), "s"((unsigned)(size_t)l) : "memory");
}
DI void dma16c(u32x4 rs, unsigned voff, unsigned soff, lptr l) {
  asm volatile("s_mov_b32 m0, %3\n\ts_nop 0\n\tbuffer_load_dwordx4 %0, %1, %2 offen sc1 lds"
               :: "v"(voff), "s"(rs), "s"(soff), "s"((unsigned)(size_t)l) : "memory");
}
DI u32x4 ld128c(u32x4 rs, unsigned voff) { u32x4 v; asm volatile("buffer_load_dwordx4 %0, %1, %2, 0 offen sc1" : "=v"(v) : "v"(voff), "s"(rs) : "memory"); return v; }
DI u32x2 poll64(u32x4 rs, unsigned voff) {
  u32x2 v; asm volatile("buffer_load_dwordx2 %0, %1, %2, 0 offen sc1\n\ts_waitcnt vmcnt(0)" : "=&v"(v) : "v"(voff), "s"(rs) : "memory"); return v;
}
DI unsigned ld32c(u32x4 rs, unsigned voff) { unsigned v; asm volatile("buffer_load_dword %0, %1, %2, 0 offen sc1" : "=v"(v) : "v"(voff), "s"(rs) : "memory"); return v; }
DI int sig(int w, int n) { return 32 * (n >> 4) + ((n >> 3) & 1) + 16 * w + 8 * ((n >> 2) & 1) + 2 * (n & 3); }
DI bf16x8 lds_b128(lptr p) { return *(const LAS bf16x8*)p; }
DI s16x4 lds_tr(lptr p) { return __builtin_amdgcn_ds_read_tr16_b64_v4i16((LAS s16x4*)p); }
DI bf16x8 lds_tr2(lptr p0, lptr p1) { s16x4 a = lds_tr(p0), b = lds_tr(p1); return __builtin_bit_cast(bf16x8, __builtin_shufflevector(a, b, 0, 1, 2, 3, 4, 5, 6, 7)); }
DI f32x16 mfma32(bf16x8 a, bf16x8 b, f32x16 c) { return __builtin_amdgcn_mfma_f32_32x32x16_bf16(a, b, c, 0, 0, 0); }
DI f32x4 mfma16(bf16x8 a, bf16x8 b, f32x4 c) { return __builtin_amdgcn_mfma_f32_16x16x32_bf16(a, b, c, 0, 0, 0); }
DI f32x4 mfma16h(f16x8 a, f16x8 b, f32x4 c) { return __builtin_amdgcn_mfma_f32_16x16x32_f16(a, b, c, 0, 0, 0); }
DI void bar() { asm volatile("" ::: "memory"); __builtin_amdgcn_s_barrier(); asm volatile("" ::: "memory"); }
DI void lgkm0() { asm volatile("s_waitcnt lgkmcnt(0)" ::: "memory"); }
#define WAIT_VM(n) asm volatile("s_waitcnt vmcnt(" #n ")" ::: "memory")
DI float phi(float tp) {
  const float e = __builtin_amdgcn_fmed3f(__builtin_amdgcn_exp2f(tp * LOG2E), 0.f, 1.f);
  const int ti = __float_as_int(tp);
  return __int_as_float(ti > 0x358637bd ? ti : 0x358637bd) + e;
}
DI bf16x8 pack8(const f32x16& a, int o) {
  u32x4 p = {pk_bf16(a[o], a[o + 1]), pk_bf16(a[o + 2], a[o + 3]), pk_bf16(a[o + 4], a[o + 5]), pk_bf16(a[o + 6], a[o + 7])};
  return __builtin_bit_cast(bf16x8, p);
}
template <int CTRL, int RM = 0xf> DI int dppi(int v) { return __builtin_amdgcn_update_dpp(0, v, CTRL, RM, 0xf, true); }
template <int CTRL, int RM = 0xf> DI float dppf(float v) { return __int_as_float(dppi<CTRL, RM>(__float_as_int(v))); }
DI unsigned umax(unsigned a, unsigned b) { return a > b ? a : b; }
DI unsigned umin(unsigned a, unsigned b) { return a < b ? a : b; }
DI unsigned umax4(unsigned m, u32x4 v) { return umax(umax(umax(m, v.x), v.y), umax(v.z, v.w)); }
DI unsigned wave_umax(unsigned v) {
  v = umax(v, dppi<0x111>(v)); v = umax(v, dppi<0x112>(v)); v = umax(v, dppi<0x114>(v)); v = umax(v, dppi<0x118>(v));
  v = umax(v, dppi<0x142, 0xa>(v)); v = umax(v, dppi<0x143, 0xc>(v));
  return __builtin_amdgcn_readlane(v, 63);
}
DI void dma4(u32x4 rs, unsigned voff, lptr l) {
  asm volatile("s_mov_b32 m0, %2\n\ts_nop 0\n\tbuffer_load_dword %0, %1, 0 offen sc1 lds" :: "v"(voff), "s"(rs), "s"((unsigned)(size_t)l) : "memory");
}
#define CODE_PREFETCH(NAME, dump, off) do { u64 pc_; unsigned len_; \
  asm volatile("s_getpc_b64 %0\n.L" NAME "_beg:\n\ts_mov_b32 %1, .L" NAME "_end-.L" NAME "_beg" : "=s"(pc_), "=s"(len_)); \
  asm volatile("s_mov_b32 m0, %2\n\ts_nop 0\n\tbuffer_load_dword %0, %1, 0 offen lds" \
               :: "v"(off), "s"(mk_rsrc((const void*)pc_, len_)), "s"((unsigned)(size_t)(dump)) : "memory"); } while (0)
#define CODE_END(NAME) asm volatile(".L" NAME "_end:")
typedef __amdgpu_buffer_rsrc_t rsrc_t;
DI rsrc_t make_rsrc(void* p, unsigned bytes) { return __builtin_amdgcn_make_buffer_rsrc(p, 0, bytes, 0x00020000); }
constexpr int WT = 16;
DI void st128(rsrc_t r, unsigned off, u32x4 v) { __builtin_amdgcn_raw_buffer_store_b128(v, r, off, 0, WT); }
DI void st64(rsrc_t r, unsigned off, u32x2 v) { __builtin_amdgcn_raw_buffer_store_b64(v, r, off, 0, WT); }
DI void st32(rsrc_t r, unsigned off, unsigned v) { __builtin_amdgcn_raw_buffer_store_b32(v, r, off, 0, WT); }

constexpr int L_PQ = 0, L_WO = 66560, L_KVZ = L_WO + 65536, L_BT = L_KVZ + 17408;
struct Out { u32x4 wor; unsigned woo; int wv; lptr lds; };
constexpr unsigned EMPTY = 0xffffffffu;
DI void wo_issue(const Out& o, int gr) { dma16(o.wor, o.woo, gr * 65536, o.lds + L_WO + o.wv * 1024 + gr * 8192); }
constexpr int L_DUMP = 163840 - 512, L_EPOCH = 163840 - 256;
DI void epoch_load(lptr lds, char* ws, int wv) {
  if ((threadIdx.x & 63) < 2) dma4(mk_rsrc(ws, WS_USED), WS_EPOCH + (threadIdx.x & 63) * 4, lds + L_EPOCH + wv * 8);
}
DI u32x2 next_tag(lptr lds, int wv) {
  const u32x2 e = *(const volatile LAS u32x2*)(lds + L_EPOCH + wv * 8);
  const u64 t = (((u64)(unsigned)__builtin_amdgcn_readfirstlane(e.y) << 32) | (unsigned)__builtin_amdgcn_readfirstlane(e.x)) + 1;
  return u32x2{(unsigned)t, (unsigned)(t >> 32)};
}

constexpr int SLOT = 32768, NSLOT = 4;
constexpr int GA_EX = L_KVZ;
constexpr int GA_GW = GA_EX + 8 * 544 * 4;

DI u32x2 gemm_role(lptr lds, const u16* __restrict__ x, const u16* __restrict__ w, const u16* __restrict__ gw, char* ws,
                   int rb, int cg, int rot, const Out& wo) {
  const int tid = threadIdx.x, lane = tid & 63;
  const int wv = __builtin_amdgcn_readfirstlane(tid >> 6), rg = wv & 3, hp = wv >> 2;
  const u32x4 xr = mk_rsrc(x, 2048 * 1024), wr_ = mk_rsrc(w, 512 * 3072);
  const unsigned xrow = tid >> 3, xo = rb * 131072 + xrow * 1024 + (((tid & 7) ^ ((xrow >> 1) & 7)) << 4);
  const unsigned wrow = tid >> 4, wp = (tid & 15) ^ ((wrow & 3) << 2);
  const unsigned wo_ = cg * 128 + wrow * 3072 + 1024 + (wp >> 3) * 1024 + ((wp & 7) << 4);
  const unsigned qo = cg * 128 + xrow * 3072 + (((tid & 7) ^ (((xrow >> 1) & 1) << 2)) << 4);
  const lptr lw = lds + wv * 1024;
  auto issue = [&](int c, int i) {
    const lptr slot = lw + (c % NSLOT) * SLOT;
    if (c < 8) {
      const unsigned kc = (c + rot) & 7;
      if (i == 0) dma16(xr, xo, kc * 128, slot);
      if (i == 1) dma16(wr_, wo_, kc * (64 * 3072), slot + 16384);
      if (i == 2) dma16(xr, xo, kc * 128 + 65536, slot + 8192);
      if (i == 3) dma16(wr_, wo_, kc * (64 * 3072) + 32 * 3072, slot + 24576);
    } else {
      dma16(wr_, qo, (((c - 8) * 4 + i + rot) & 7) * (64 * 3072), slot + i * 8192);
    }
  };
  const int gsel = hp == 0 ? ((cg - rot) & 7) : -1;
#pragma unroll
  for (int c = 0; c < 3; ++c) {
#pragma unroll
    for (int i = 0; i < 4; ++i) issue(c, i);
    if (c == 0 && hp == 0 && lane < 8) dma16(mk_rsrc(gw, 1024), lane * 16, cg * 128, lds + GA_GW + wv * 128);
  }
  epoch_load(lds, ws, wv);
  const rsrc_t wsr = make_rsrc(ws, WS_USED);
  const unsigned pqo = WS_PQF + (((rb * 4 + rg) * 32 + cg * 4 + 2 * hp) * 64 + lane) * 16;
  const int h = lane >> 5, row = (rg >> 1) * 64 + sig(rg & 1, lane & 31);
  const int gi = lane >> 4, blk = gi & 1, q = (lane & 15) >> 2, p4 = lane & 3;
  lptr ap[4], bp[2];
#pragma unroll
  for (int s = 0; s < 4; ++s) ap[s] = lds + row * 128 + (((2 * s + h) ^ ((row >> 1) & 7)) << 4);
#pragma unroll
  for (int ct = 0; ct < 2; ++ct) bp[ct] = lds + 16384 + (8 * h + q) * 256 + (((4 * (2 * ct + hp) + 2 * blk + (p4 >> 1)) ^ (q << 2)) << 4) + (p4 & 1) * 8;
  f32x16 ak, av;
  float g0 = 0.f;
#pragma unroll
  for (int i = 0; i < 16; ++i) { ak[i] = 1e-6f; av[i] = 0.f; }
  bf16x8 xf[32], Bf[3][2];
  u32x2 tag;
  auto gate_steps = [&](int c) {
    if (gsel == c) {
      const lptr gp = lds + GA_GW + wv * 128 + h * 16;
      f32x16 ag = {};
#pragma unroll
      for (int i = 0; i < 4; ++i) ag = mfma32(lds_b128(gp + i * 32), xf[4 * c + i], ag);
      g0 = ag[0];
    }
    PIN(g0);
  };
  lptr qp = lds + (8 * h + q) * 128 + (((4 * hp + 2 * blk + (p4 >> 1)) ^ ((q >> 1) << 2)) << 4) + (p4 & 1) * 8;
  const int dp = lane & 31, e_ = lane & 15;
  lptr exw = lds + GA_EX + rg * 4352 + (2 * hp + (dp >> 4)) * 1024 + e_ * 64 + (((2 * h) ^ (e_ >> 2)) << 4);
  lptr ezw = lds + GA_EX + rg * 4352 + 4096 + (2 * hp + (dp >> 4)) * 64 + (((e_ >> 2) & 1) * 8 + (e_ & 3) + 4 * (e_ >> 3)) * 4;
  lptr exr = lds + GA_EX + (tid < 272 ? tid : 0) * 16;
  unsigned kpo = tid < 272 ? WS_KVP + (rb * 8 + cg) * 8704 + tid * 16 : 0xffff0000u;
  PIN(qp); PIN(exw); PIN(ezw); PIN(exr); PIN(kpo);
  f32x16 aq;
#pragma unroll
  for (int n = 0; n < 32; ++n) {
    const int c = n >> 2, s = n & 3, so = (c & 1) * SLOT;
    if (s == 0) {
      if (c > 0) {
        lgkm0();
        if ((c & 1) == 0) {
          const int dlt = (c == 4) ? -2 * SLOT : 2 * SLOT;
#pragma unroll
          for (int i = 0; i < 4; ++i) { ap[i] += dlt; PIN(ap[i]); }
#pragma unroll
          for (int i = 0; i < 2; ++i) { bp[i] += dlt; PIN(bp[i]); }
        }
      }
      if (c < 3) WAIT_VM(9); else WAIT_VM(8);
      bar();
      SB;
    }
    if (n >= 2) { ak = mfma32(xf[n - 2], Bf[(n - 2) % 3][0], ak); SB; }
    xf[n] = lds_b128(ap[s] + so); Bf[n % 3][0] = lds_tr2(bp[0] + so + s * 4096, bp[0] + so + s * 4096 + 1024); SB;
    if (n >= 2) { av = mfma32(xf[n - 2], Bf[(n - 2) % 3][1], av); SB; }
    Bf[n % 3][1] = lds_tr2(bp[1] + so + s * 4096, bp[1] + so + s * 4096 + 1024); SB;
    if (c + 3 < 10) { issue(c + 3, s); SB; } else if (s < 3) { wo_issue(wo, s); SB; }
    if (n == 14) { tag = next_tag(lds, wv); SB; }
    if (n == 30) {
#pragma unroll
      for (int i = 0; i < 16; ++i) aq[i] = 1e-6f;
      PIN(aq); SB;
    }
    if (n > 0 && s == 0) { gate_steps(c - 1); SB; }
  }
  f32x16 kv = {}, z = {};
  constexpr int PD = 5;
  bf16x8 Aq[PD + 1], ones;
#pragma unroll
  for (int i = 0; i < 8; ++i) ones[i] = (__bf16)1.0f;
  u32x4 pk[2], vv[2];
  auto rdq = [&](int m) { Aq[m % (PD + 1)] = lds_tr2(qp + m * 2048, qp + m * 2048 + 512); };
#pragma unroll
  for (int m = -2; m < 32; ++m) {
    if (m == -2) { lgkm0(); WAIT_VM(7); bar(); SB; rdq(0); rdq(1); SB; }
    if (m == 11) { lgkm0(); WAIT_VM(6); bar(); SB; }
    if (m < 0) { ak = mfma32(xf[32 + m], Bf[(32 + m) % 3][0], ak); av = mfma32(xf[32 + m], Bf[(32 + m) % 3][1], av); SB; }
    else { aq = mfma32(Aq[m % (PD + 1)], xf[m], aq); SB; }
    if (m == -2) { rdq(2); SB; }
    if (m + PD < 32) { rdq(m + PD); SB; }
    if (m == 0) { vv[0] = __builtin_bit_cast(u32x4, pack8(av, 0)); vv[1] = __builtin_bit_cast(u32x4, pack8(av, 8)); PIN(vv[0]); PIN(vv[1]); SB; }
    if (m >= 1 && m < 9) {
      const int j = m - 1;
      unsigned p = pk_bf16(phi(ak[2 * j]), phi(ak[2 * j + 1]));
      PIN(p);
      pk[j >> 2][j & 3] = p;
      SB;
      if ((j & 3) == 3) {
        kv = mfma32(__builtin_bit_cast(bf16x8, pk[j >> 2]), __builtin_bit_cast(bf16x8, vv[j >> 2]), kv);
        z = mfma32(ones, __builtin_bit_cast(bf16x8, pk[j >> 2]), z);
        SB;
      }
      if (j == 1 || j == 4 || j == 6) { wo_issue(wo, 3 + j / 2 - (j == 1 ? 0 : 1)); SB; }
    }
    if (m == 10) {
      float sel[8];
#pragma unroll
      for (int i = 0; i < 8; ++i) { float a = kv[i], b = kv[8 + i]; PIN(a); PIN(b); sel[i] = dp < 16 ? a : b; }
      *(LAS f32x4*)exw = f32x4{sel[0], sel[1], sel[2], sel[3]};
      *(LAS f32x4*)((lptr)((unsigned)(size_t)exw ^ 16u)) = f32x4{sel[4], sel[5], sel[6], sel[7]};
      *(LAS float*)ezw = z[0];
      SB;
    }
    if (m == 11) {
      const LAS f32x4* ex = (const LAS f32x4*)exr;
      const u32x4 sum = __builtin_bit_cast(u32x4, (ex[0] + ex[272]) + (ex[544] + ex[816]));
      st128(wsr, kpo, u32x4{sum.x, tag.x, sum.y, tag.x});
      st128(wsr, kpo + 4352, u32x4{sum.z, tag.x, sum.w, tag.x});
      SB;
    }
    if (m == 13) { PIN(aq); gate_steps(7); SB; }
    if (m == 16 || m == 19) { wo_issue(wo, m == 16 ? 6 : 7); SB; }
  }
  PIN(aq);
  {
    if (hp == 0) st32(wsr, h == 0 ? WS_G + cg * 8192 + (rb * 4 + rg) * 128 + lane * 4 : 0xfffffff0u, __float_as_uint(g0));
#pragma unroll
    for (int i = 0; i < 8; ++i) aq[i] = phi(aq[i]);
    st128(wsr, pqo, __builtin_bit_cast(u32x4, pack8(aq, 0)));
#pragma unroll
    for (int i = 8; i < 16; ++i) aq[i] = phi(aq[i]);
    st128(wsr, pqo + 1024, __builtin_bit_cast(u32x4, pack8(aq, 8)));
    WAIT_VM(0);
  }
  bar();
  if (tid == 511) st64(wsr, WS_PF + (rb * 8 + cg) * 128, tag);
  return tag;
}

constexpr int SC_ROW = 2064;
constexpr int KC_CP = 4224, KC_CH = 2 * KC_CP;
constexpr int EX_BASE = 4 * KC_CH;
constexpr int U_BASE = 57 * SC_ROW, U_PAD = 224, U_CP = 4576, U_CH = 2 * U_CP;
constexpr int T_BASE = U_BASE + 4 * U_CH, T_CH = 1536;
static_assert(T_BASE + 4 * T_CH <= 163840 && U_BASE % 16 == 0, "lds");

DI f32x2 cexp_p(float l2, float rv) {
  const float m = __builtin_amdgcn_exp2f(l2), f = __builtin_amdgcn_fractf(rv);
  const f32x2 r = {m * __builtin_amdgcn_cosf(f), m * __builtin_amdgcn_sinf(f)};
  return r;
}
DI bf16x8 lds_2b64(lptr p) {
  const u32x2 a = *(const volatile LAS u32x2*)p, b = *(const volatile LAS u32x2*)(p + 8);
  const u32x4 v = {a.x, a.y, b.x, b.y};
  return __builtin_bit_cast(bf16x8, v);
}
DI unsigned perm(unsigned hi, unsigned lo, unsigned sel) { return __builtin_amdgcn_perm(hi, lo, sel); }
constexpr unsigned LL = 0x01000100, LH = 0x03020302, HL = 0x05040504, SWL = 0x01000302, SWH = 0x05040706, HL_LH = 0x03020504;
template <int E0, int E1, int E2, int E3>
DI void put4(unsigned base, f16x2 a, f16x2 b, f16x2 c, f16x2 d) {
  constexpr int O = E0 < 28 ? 0 : 28;
  static_assert((E1 < 28) == (E0 < 28) && (E2 < 28) == (E0 < 28) && (E3 < 28) == (E0 < 28), "one base");
  asm volatile("s_mov_b32 m0, %4\n\ts_nop 0\n\tds_write_addtid_b32 %0 offset:%5\n\tds_write_addtid_b32 %1 offset:%6\n\t"
               "ds_write_addtid_b32 %2 offset:%7\n\tds_write_addtid_b32 %3 offset:%8"
               :: "v"(a), "v"(b), "v"(c), "v"(d), "s"(base + O * SC_ROW), "i"((E0 - O) * SC_ROW), "i"((E1 - O) * SC_ROW),
                  "i"((E2 - O) * SC_ROW), "i"((E3 - O) * SC_ROW) : "memory");
}

DI u32x2 conv_role(lptr lds, const u16* __restrict__ x, const u16* __restrict__ lar, const u16* __restrict__ aim,
                   const u16* __restrict__ cre, const u16* __restrict__ cim, const u16* __restrict__ ldt,
                   const u16* __restrict__ dsk, char* ws, int ch0, int jrot, const Out& wo) {
  const int tid = threadIdx.x, lane = tid & 63;
  const int wv = __builtin_amdgcn_readfirstlane(tid >> 6), cl = wv & 3, half = wv >> 2;
  const int d = ch0 + cl;
  const unsigned r_lar = ((const unsigned*)lar)[d * 64 + lane], r_aim = ((const unsigned*)aim)[d * 64 + lane];
  const unsigned r_cr = ((const unsigned*)cre)[d * 64 + lane], r_ci = ((const unsigned*)cim)[d * 64 + lane];
  const unsigned s_ldt = ((const CAS unsigned*)ldt)[d >> 1], s_dsk = ((const CAS unsigned*)dsk)[d >> 1];
  const int j = lane & 15, g = lane >> 4;
  const rsrc_t wsr = make_rsrc(ws, WS_USED);
  bar();
  const int cj = ch0 >> 2, cg = cj >> 4, rb = cj & 15;
  const unsigned kvo = wv == 4 && lane < 9 ? WS_KVZ + cj * 256 + lane * 16 : 0xffffffe0u;
  const int tt = (tid + jrot * 64) & 511;
  const char* xg = (const char*)x + ch0 * 2;
  const unsigned xoff = tt * 4096;
  u32x2 ur[4];
  ur[0] = *(const u32x2*)(xg + xoff); ur[1] = *(const u32x2*)(xg + xoff + 1024);
  u32x2 up;
  {
    const u32x4 zero = {0, 0, 0, 0};
    if (lane < 28) *(LAS u32x4*)(lds + U_BASE + cl * U_CH + half * U_CP + lane * 16) = zero;
  }
  const unsigned scb = (unsigned)(size_t)lds + cl * 512 + half * 256;
  const lptr fb = lds + cl * 512 + half * 256 + j * SC_ROW + g * 16;
  unsigned selA = lane < 32 ? LL : lane == 32 ? HL : SWH, selB = lane < 32 ? HL_LH : SWH, selC = lane < 32 ? LL : SWH, selE = lane < 32 ? LL : SWL;
  if (half == 0) { const bool odd = !(lane & 1); selA = odd ? LH : LL; selB = odd ? HL : LH; selC = odd ? HL : SWL; }
  PIN(selA); PIN(selB); PIN(selC); PIN(selE);
  const unsigned shl = half ? 0 : 16;
  const float f_ldt = (d & 1) ? bfhi(s_ldt) : bf2f(s_ldt), f_dsk = (d & 1) ? bfhi(s_dsk) : bf2f(s_dsk);
  const float dt = __int_as_float(__builtin_amdgcn_readfirstlane(__float_as_int(rbf(__expf(f_ldt)))));
  SB;
  const float l2 = -rbf(__builtin_amdgcn_exp2f(bfhi(r_lar << shl) * LOG2E)) * dt * LOG2E;
  const float rv = bfhi(r_aim << shl) * dt * 0.15915494309189535f;
  unsigned cm;
  {
    typedef __attribute__((ext_vector_type(2))) unsigned short u16x2;
    const u16x2 m2 = __builtin_elementwise_max(__builtin_bit_cast(u16x2, r_cr & 0x7fff7fffu), __builtin_bit_cast(u16x2, r_ci & 0x7fff7fffu));
    const unsigned m = __builtin_bit_cast(unsigned, m2);
    cm = umax(wave_umax(umax(m << 16, m)), 0x0da24260u);
  }
  const float cmax = __uint_as_float(cm), cinv = __builtin_amdgcn_rcpf(cmax);
  f32x4 kt[3];
  {
    f16x8 rf[4], t0[4];
    const f16x2 one = {(_Float16)1.f, (_Float16)0.f};
    const f32x2 E1 = cexp_p(l2, rv), E8 = cexp_p(l2 * 8.f, rv * 8.f), E128 = cexp_p(l2 * 128.f, rv * 128.f);
    f32x2 E2, E3, E4, E16, E24, E32, E64, E96, E256, E384, E512, H1, H2, H3, H4, dummy;
    const f32x2 H0 = {bfhi(r_cr << shl) * cinv, bfhi(r_ci << shl) * cinv};
    cmul2(E16, E8, E8, E256, E128, E128);
    cmul2(E32, E16, E16, E512, E256, E256);
    cmul2(E24, E16, E8, E2, E1, E1);
    cmul2(E64, E32, E32, H1, H0, E512);
    cmul2(E3, E2, E1, E4, E2, E2);
    cmul2(E96, E64, E32, H2, H1, E512);
    cmul2(E384, E256, E128, H3, H2, E512);
    {
      const f16x2 P1 = pk_f16c(E8), P2 = pk_f16c(E16), P3 = pk_f16c(E24), Q1 = pk_f16c(E32), Q2 = pk_f16c(E64), Q3 = pk_f16c(E96);
      put4<0, 1, 2, 3>(scb, one, P1, P2, P3);
      put4<4, 5, 6, 7>(scb, Q1, cmulh(Q1, P1), cmulh(Q1, P2), cmulh(Q1, P3));
      put4<8, 9, 10, 11>(scb, Q2, cmulh(Q2, P1), cmulh(Q2, P2), cmulh(Q2, P3));
      put4<12, 13, 14, 15>(scb, Q3, cmulh(Q3, P1), cmulh(Q3, P2), cmulh(Q3, P3));
    }
    SB;
    ur[2] = *(const u32x2*)(xg + xoff + 2048);
    SB;
    H4 = cmul(H3, E512); (void)dummy;
    const f16x2 e1 = pk_f16(E1), e2 = pk_f16(E2), e3 = pk_f16(E3), e4 = pk_f16(E4);
    const f16x2 e5 = cmulh(e4, e1), e6 = cmulh(e4, e2), e7 = cmulh(e4, e3);
    const f16x2 S1 = pk_f16(E128), S2 = pk_f16(E256), S3 = pk_f16(E384);
    const f16x2 h0 = pk_f16(H0), h1 = pk_f16(H1), h2 = pk_f16(H2), h3 = pk_f16(H3);
    {
      const f16x2 G1 = cmulh(h0, S1);
      put4<16, 17, 18, 19>(scb, h0, cmulh(h0, e1), cmulh(h0, e2), cmulh(h0, e3));
      put4<20, 21, 22, 23>(scb, cmulh(h0, e4), cmulh(h0, e5), cmulh(h0, e6), cmulh(h0, e7));
      put4<24, 25, 26, 27>(scb, G1, cmulh(G1, e1), cmulh(G1, e2), cmulh(G1, e3));
      put4<28, 29, 30, 31>(scb, cmulh(G1, e4), cmulh(G1, e5), cmulh(G1, e6), cmulh(G1, e7));
    }
#pragma unroll
    for (int s = 0; s < 4; ++s) { rf[s] = *(const LAS f16x8*)(fb + s * 64); t0[s] = *(const LAS f16x8*)(fb + 16 * SC_ROW + s * 64); }
    SB;
    ur[3] = *(const u32x2*)(xg + xoff + 3072);
    up = __builtin_amdgcn_raw_buffer_load_b64(make_rsrc((void*)x, 2048 * 1024), lane == 0 && tt > 0 ? ch0 * 2 + xoff - 1024 : 0xfffffff0u, 0, 0);
    epoch_load(lds, ws, wv);
    SB;
    {
      const f16x2 G2 = cmulh(h0, S2), G3 = cmulh(h0, S3), G5 = cmulh(h1, S1), G6 = cmulh(h1, S2), G7 = cmulh(h1, S3);
      put4<32, 33, 34, 35>(scb, G2, cmulh(G2, e2), cmulh(G2, e4), cmulh(G2, e6));
      kt[0] = mfma16h(t0[0], rf[0], f32x4{0.f, 0.f, 0.f, 0.f});
      kt[0] = mfma16h(t0[1], rf[1], kt[0]);
      put4<36, 37, 38, 39>(scb, G3, cmulh(G3, e2), cmulh(G3, e4), cmulh(G3, e6));
      kt[0] = mfma16h(t0[2], rf[2], kt[0]);
      put4<40, 41, 42, 43>(scb, h1, cmulh(h1, e4), G5, cmulh(G5, e4));
      kt[0] = mfma16h(t0[3], rf[3], kt[0]);
      put4<44, 45, 46, 47>(scb, G6, cmulh(G6, e4), G7, cmulh(G7, e4));
    }
#pragma unroll
    for (int s = 0; s < 4; ++s) t0[s] = *(const LAS f16x8*)(fb + 32 * SC_ROW + s * 64);
    SB;
    put4<48, 49, 50, 51>(scb, h2, cmulh(h2, S1), cmulh(h2, S2), cmulh(h2, S3));
    put4<52, 53, 54, 55>(scb, h3, cmulh(h3, S1), cmulh(h3, S2), cmulh(h3, S3));
    *(LAS f16x2*)(lds + cl * 512 + half * 256 + lane * 4 + 56 * SC_ROW) = pk_f16(H4);
    f16x8 t2[4];
#pragma unroll
    for (int s = 0; s < 4; ++s) t2[s] = *(const LAS f16x8*)(fb + 48 * SC_ROW + s * 64);
    SB;
    kt[1] = f32x4{0.f, 0.f, 0.f, 0.f}; kt[2] = kt[1];
#pragma unroll
    for (int s = 0; s < 4; ++s) kt[1] = mfma16h(t0[s], rf[s], kt[1]);
#pragma unroll
    for (int s = 0; s < 4; ++s) kt[2] = mfma16h(t2[s], rf[s], kt[2]);
  }
  auto put_u = [&]() {
    const u32x2 pv = {(unsigned)__builtin_amdgcn_update_dpp((int)up.x, (int)ur[3].x, 0x138, 0xf, 0xf, false),
                      (unsigned)__builtin_amdgcn_update_dpp((int)up.y, (int)ur[3].y, 0x138, 0xf, 0xf, false)};
    const lptr ub = lds + U_BASE + (U_PAD + 4 * tt) * 2;
#pragma unroll
    for (int c = 0; c < 4; ++c) {
      const unsigned sel = (c & 1) ? 0x07060302 : 0x05040100;
      const unsigned p = pv[c >> 1], r0 = ur[0][c >> 1], r1 = ur[1][c >> 1], r2 = ur[2][c >> 1], r3 = ur[3][c >> 1];
      *(LAS u32x2*)(ub + c * U_CH) = u32x2{perm(r1, r0, sel), perm(r3, r2, sel)};
      *(LAS u32x2*)(ub + c * U_CH + U_CP) = u32x2{perm(r0, p, sel), perm(r2, r1, sel)};
    }
  };
  const lptr Tb = lds + T_BASE + cl * T_CH;
  {
    LAS f32x4* xw = (LAS f32x4*)(lds + cl * 512 + half * 256 + g * SC_ROW) + j;
    const LAS f32x4* xr = (const LAS f32x4*)(lds + cl * 512 + (half ^ 1) * 256 + g * SC_ROW) + j;
    if (half) xw[0] = kt[0]; else { xw[0] = kt[1]; xw[4 * SC_ROW / 16] = kt[2]; }
    const float ks = 2.f * dt * cmax;
    const lptr p1 = half ? Tb + (256 + 64 * g + (g < 2 ? 4 : 2) * j) * 2 : Tb + (128 * (g >> 1) + 4 * (g & 1) + 8 * j) * 2;
    lptr p1b = p1 + (g < 2 ? 4 : 64);
    PIN(p1b);
    lgkm0();
    bar();
    if (half == 0) {
      f32x4 a0 = (kt[0] + xr[0]) * ks;
      if (lane == 0) a0[0] += f_dsk;
      *(LAS u32x2*)p1 = u32x2{pk_bf16(a0[0], a0[1]), pk_bf16(a0[2], a0[3])};
    } else {
      const f32x4 a1 = (kt[1] + xr[0]) * ks, a2 = (kt[2] + xr[4 * SC_ROW / 16]) * ks;
      *(LAS unsigned*)p1 = pk_bf16(a1[0], a1[1]);
      *(LAS unsigned*)p1b = pk_bf16(a1[2], a1[3]);
      const unsigned q0 = pk_bf16(a2[0], a2[1]), q1 = pk_bf16(a2[2], a2[3]);
      LAS u16* p2 = (LAS u16*)Tb + 512 + 64 * g + j;
      p2[0] = (u16)q0; p2[16] = (u16)(q0 >> 16); p2[32] = (u16)q1; p2[48] = (u16)(q1 >> 16);
    }
  }
  lgkm0();
  bar();
  {
    const lptr kc = lds + cl * KC_CH + lane * 16;
    auto emit = [&](int Y0, unsigned P0, unsigned P1, unsigned P2, unsigned P3, unsigned P4) {
      *(LAS u32x4*)(kc + Y0 * 16) = u32x4{P0, P1, P2, P3};
      *(LAS u32x4*)(kc + KC_CP + Y0 * 16) = u32x4{P1, P2, P3, P4};
    };
    if (half == 0) {
      const LAS unsigned* p = (const LAS unsigned*)(Tb + ((575 - lane) >> 1) * 4);
      const unsigned D0 = p[32], D1 = p[33], F0 = p[0], F1 = p[1], Z = *(const LAS unsigned*)Tb;
      SB;
      const unsigned e1 = perm(D1, D0, selB), e0 = perm(D1, D0, selA), f1 = perm(F1, F0, selB), f0 = perm(F1, F0, selA);
      emit(0, e1, e1, perm(D1, D0, selC), e0, e0);
      emit(64, f1, f1, perm(F1, F0, selC), f0, f0);
      if (lane < 4) emit(256, lane == 0 ? perm(0, Z, SWL) : 0u, 0u, 0u, 0u, 0u);
    } else {
      const LAS unsigned* p = (const LAS unsigned*)(Tb + (255 - lane) * 4);
      const LAS unsigned* r = (const LAS unsigned*)(Tb + (lane < 32 ? 760 - 8 * lane : 1008 - 16 * lane));
      const unsigned F0 = p[0], F1 = p[1], D0 = r[0], D1 = r[1], D2 = r[2], D3 = r[3], D4 = r[4];
      SB;
      const unsigned e2 = perm(F1, F0, HL);
      emit(128, e2, e2, perm(F1, F0, LH), perm(F1, F0, SWL), perm(F1, F0, LL));
      emit(192, perm(D4, D2, selA), perm(lane < 32 ? D2 : D3, D1, selB), perm(D2, D1, selC), perm(D1, D0, selB), perm(D1, D0, selE));
    }
  }
  SB;
  put_u();
  WAIT_VM(0);
  const u32x2 tag = next_tag(lds, wv);
  lgkm0();
  bar();
  const u32x4 wsl = mk_rsrc(ws, WS_USED);
  const int a_ = j;
  const lptr ab = lds + cl * KC_CH + ((a_ & 1) ? KC_CP + 4088 : 4096) - 8 * (a_ >> 1) + 16 * g - 3584 - 192 - half * 256;
  const lptr bb = lds + U_BASE + cl * U_CH + ((a_ & 1) ? 0 : U_CP) + (U_PAD + 32 * (a_ >> 1) + 8 * g) * 2 - 192 - half * 256;
  f32x4 acc[8];
#pragma unroll
  for (int i = 0; i < 8; ++i) acc[i] = f32x4{0.f, 0.f, 0.f, 0.f};
  LAS f32x4* exw = (LAS f32x4*)(lds + EX_BASE + wv * 4096) + lane;
  f32x4 keep[4];
  u32x4 k[3];
  constexpr unsigned DONE = 0xffffffe0u;
  const unsigned src = WS_KVP + (j * 8 + cg) * 8704;
  const unsigned ko = src + (rb >> 2) * 1024 + ((rb & 3) * 4 + cl) * 64 + ((g ^ (rb & 3)) << 4);
  const bool zl = wv == 4 && g < 2;
  unsigned ka[3] = {half ? ko : DONE, half ? ko + 4352 : DONE, zl ? src + g * 4352 + 4096 + rb * 16 : DONE};
  unsigned tagz = zl ? tag.x : 0u;
  lptr slw = lds + T_BASE + (j == 15 ? (cl * 4 + g) * 8 : 256), zsw = lds + T_BASE + (j == 15 && zl ? 128 + g * 4 : 256), slr = lds + T_BASE + (lane < 9 ? lane : 0) * 16;
  PIN(ka[0]); PIN(ka[1]); PIN(ka[2]); PIN(tagz); PIN(slw); PIN(zsw); PIN(slr);
  {
    u32x2 Al[2][8], Ah[2][8];
    bf16x8 B[2][8];
    auto rd = [&](int r, int k) {
      const int nb = r & 1, mu = k / 3, w = k % 3;
      if (w == 0) B[nb][mu] = lds_b128(bb + 512 * mu + 64 * (3 - r));
      if (w == 1) Al[nb][mu] = *(const volatile LAS u32x2*)(ab + 512 * (7 - mu) + 64 * (3 - r));
      if (w == 2) Ah[nb][mu] = *(const volatile LAS u32x2*)(ab + 512 * (7 - mu) + 64 * (3 - r) + 8);
    };
    auto AF = [&](int nb, int mu) { return __builtin_bit_cast(bf16x8, u32x4{Al[nb][mu].x, Al[nb][mu].y, Ah[nb][mu].x, Ah[nb][mu].y}); };
#pragma unroll
    for (int k = 0; k < 24; ++k) rd(0, k);
    SB;
#pragma unroll
    for (int r = 1; r < 4; ++r) {
      const int cb = (r - 1) & 1;
      int cnt = 0;

#pragma unroll
      for (int m = 0; m < 8; ++m)
#pragma unroll
        for (int n = 0; n <= m; ++n)
#pragma unroll
          for (int sw = 0; sw < 2; ++sw)
            if (m + n < 8 && (sw == 0 || n < m)) {
              const int mu = sw ? n : m, nu = sw ? m : n;
              acc[m + n] = mfma16(AF(cb, mu), B[cb][nu], acc[m + n]);
              SB;
              if (cnt % 3 != 2) rd(r, cnt - cnt / 3);
              else if (cnt == 14 || cnt == 29) wo_issue(wo, 2 * (r - 1) + (cnt == 29));
              else if ((cnt == 5 || cnt == 20) && r == 1) st128(wsr, WS_BT + ch0 * 4096 + (cnt == 5 ? 0 : 8192) + tid * 16, u32x4{EMPTY, EMPTY, EMPTY, EMPTY});
              else if (cnt == 2 && r == 1) st128(wsr, kvo, u32x4{EMPTY, EMPTY, EMPTY, EMPTY});
              else if ((cnt == 26 || cnt == 32 || cnt == 35) && r == 3) k[cnt == 26 ? 2 : cnt == 35] = ld128c(wsl, ka[cnt == 26 ? 2 : cnt == 35]);
              else if (cnt == 2 && r == 3) { WAIT_VM(3); st64(wsr, lane == 0 ? WS_FLAG + (cj * 8 + wv) * 8 : 0xfffffff0u, tag); }
              SB;
              ++cnt;
            }
    }
    auto tiles = [&](int t0) {
#pragma unroll
      for (int mu = 0; mu < 8; ++mu)
#pragma unroll
        for (int n = 0; n < 8; ++n) if (mu + n >= t0 && mu + n < t0 + 4) acc[mu + n] = mfma16(AF(1, mu), B[1][n], acc[mu + n]);
    };
    if (half) {
      tiles(0); SB; tiles(4);
#pragma unroll
      for (int i = 0; i < 4; ++i) { exw[64 * i] = acc[i]; keep[i] = acc[4 + i]; }
    } else {
      tiles(4); SB; tiles(0);
#pragma unroll
      for (int i = 0; i < 4; ++i) { exw[64 * i] = acc[4 + i]; keep[i] = acc[i]; }
    }
  }
  {
    const LAS f32x4* exr = (const LAS f32x4*)(lds + EX_BASE + (wv ^ 4) * 4096) + lane;
    const unsigned bto = WS_BT + d * 4096 + half * 2048 + (g & 1) * 512 + (a_ >> 2) * 128 + (g >> 1) * 64 + (a_ & 3) * 16;
    u32x4 bt[2];
    lgkm0();
    bar();
    f32x4 ex[4];
#pragma unroll
    for (int i = 0; i < 4; ++i) ex[i] = exr[64 * i];
    SB;
    WAIT_VM(0);
    PIN(k[0]); PIN(k[1]); PIN(k[2]);
    if (half) {
#pragma unroll 1
      for (unsigned spin = 0; spin < SPIN_CAP; ++spin) {
        const unsigned x = ((k[0].y ^ tag.x) | (k[0].w ^ tag.x) | (k[1].y ^ tag.x)) | ((k[1].w ^ tag.x) | (k[2].y ^ tagz) | (k[2].w ^ tagz));
        if (__all(x == 0)) break;
        asm volatile("buffer_load_dwordx4 %0, %3, %6, 0 offen sc1\n\tbuffer_load_dwordx4 %1, %4, %6, 0 offen sc1\n\t"
                     "buffer_load_dwordx4 %2, %5, %6, 0 offen sc1\n\ts_waitcnt vmcnt(0)"
                     : "=&v"(k[0]), "=&v"(k[1]), "=&v"(k[2]) : "v"(ka[0]), "v"(ka[1]), "v"(ka[2]), "s"(wsl) : "memory");
      }
      float v[6] = {__uint_as_float(k[0].x), __uint_as_float(k[0].z), __uint_as_float(k[1].x), __uint_as_float(k[1].z),
                    __uint_as_float(k[2].x), __uint_as_float(k[2].z)};
#pragma unroll
      for (int i = 0; i < 6; ++i) { v[i] += dppf<0x111>(v[i]); PIN(v[i]); }
#pragma unroll
      for (int i = 0; i < 6; ++i) { v[i] += dppf<0x112>(v[i]); PIN(v[i]); }
#pragma unroll
      for (int i = 0; i < 6; ++i) { v[i] += dppf<0x114>(v[i]); PIN(v[i]); }
#pragma unroll
      for (int i = 0; i < 6; ++i) { v[i] += dppf<0x118>(v[i]); PIN(v[i]); }
      *(LAS u32x2*)slw = u32x2{umin(pk_bf16(v[0], v[1]), EMPTY - 1), umin(pk_bf16(v[2], v[3]), EMPTY - 1)};
      *(LAS unsigned*)zsw = umin(pk_bf16(v[4], v[5]), EMPTY - 1);
    }
    lgkm0();
    bar();
    PIN(ex[0]); PIN(ex[1]); PIN(ex[2]); PIN(ex[3]);
    if (wv == 4) st128(wsr, kvo, *(const LAS u32x4*)slr);
#pragma unroll
    for (int i = 0; i < 4; i += 2) {
      const f32x4 v = keep[i] + ex[i], w = keep[i + 1] + ex[i + 1];
      const auto sx = __builtin_amdgcn_permlane16_swap(umin(pk_bf16(v[0], v[1]), EMPTY - 1), umin(pk_bf16(w[0], w[1]), EMPTY - 1), false, false);
      const auto sy = __builtin_amdgcn_permlane16_swap(umin(pk_bf16(v[2], v[3]), EMPTY - 1), umin(pk_bf16(w[2], w[3]), EMPTY - 1), false, false);
      bt[i >> 1] = u32x4{sx[0], sy[0], sx[1], sy[1]};
      PIN(bt[i >> 1]);
    }
    st128(wsr, bto, bt[0]); st128(wsr, bto + 1024, bt[1]);
    wo_issue(wo, 6); wo_issue(wo, 7);
    return tag;
  }
}

DI void out_phase(lptr lds, char* ws, u16* __restrict__ out, int rt, int ct, u32x2 tag) {
  const int tid = threadIdx.x, lane = tid & 63;
  const int wv = __builtin_amdgcn_readfirstlane(tid >> 6), wr = wv & 1, hq = wv >> 1;
  const u32x4 wsl = mk_rsrc(ws, WS_USED);
  const int h = lane >> 5, t = lane & 31, trow = rt * 64 + sig(wr, t);
  const unsigned bo = WS_BT + (ct * 64 + wv * 8 + (lane >> 3)) * 4096 + rt * 128 + (((lane & 7) ^ (((lane >> 4) & 1) << 2)) << 4);
  const unsigned fo = lane < 8 ? WS_PF + ((rt >> 1) * 8 + lane) * 128 : lane < 24 ? WS_FLAG + 8 * ((ct * 16 + 2 * wv) * 8 + lane - 8)
                    : lane < 40 ? WS_FLAG + 8 * ((wv * 16 + lane - 24) * 8 + 4) : 0xfffffff0u;
  unsigned gp[4];
  const unsigned go = WS_G + h * 32768 + (rt * 2 + wr) * 128 + t * 4;
  bool b_arm;
#pragma unroll 1
  for (unsigned spin = 0; spin < SPIN_CAP; ++spin) {
    const u32x2 f = poll64(wsl, fo);
    const u64 bad = __ballot(f.x != tag.x || f.y != tag.y);
    b_arm = (bad & 0xffff00ull) == 0;
    if ((bad & 0xffff0000ffull) == 0) break;
    __builtin_amdgcn_s_sleep(2);
  }
  auto pq_piece = [&](int g) {
    dma16c(wsl, lane * 16, WS_PQF + rt * 65536 + (wv & 1) * 32768 + (wv >> 1) * 1024 + g * 4096, lds + L_PQ + (wv & 1) * 32768 + (wv >> 1) * 1024 + g * 4096);
  };
  {
    constexpr unsigned DONE = 0xffffffe0u;
    const unsigned ko = WS_KVZ + wv * 4096 + (lane >> 3) * 256 + (lane & 7) * 16;
    unsigned ka[3] = {ko, ko + 2048, lane < 16 ? WS_KVZ + wv * 4096 + lane * 256 + 128 : DONE};
    const lptr kl = lds + L_KVZ + wv * 2176 + (lane >> 5) * 544 + (lane & 31) * 16, kz = lane < 16 ? lds + L_KVZ + wv * 2176 + (lane >> 2) * 544 + 512 + (lane & 3) * 8 : lds + L_DUMP;
    u32x4 k[3];
#pragma unroll
    for (int i = 0; i < 3; ++i) k[i] = ld128c(wsl, ka[i]);
    pq_piece(0); pq_piece(1);
    WAIT_VM(2);
    PIN(k[0]); PIN(k[1]); PIN(k[2]);
    if (__all(umax4(umax4(umax(k[2].x, k[2].y), k[0]), k[1]) != EMPTY)) {
      *(LAS u32x4*)kl = k[0]; *(LAS u32x4*)(kl + 1088) = k[1]; *(LAS u32x2*)kz = u32x2{k[2].x, k[2].y};
    } else {
#pragma unroll 1
      for (unsigned spin = 0; spin < SPIN_CAP; ++spin) {
#pragma unroll
        for (int i = 0; i < 3; ++i) {
          if ((i < 2 ? umax4(0, k[i]) : umax(k[i].x, k[i].y)) != EMPTY && ka[i] != DONE) {
            if (i < 2) *(LAS u32x4*)(kl + i * 1088) = k[i]; else *(LAS u32x2*)kz = u32x2{k[i].x, k[i].y};
            ka[i] = DONE;
          }
        }
        if (__all((ka[0] & ka[1] & ka[2]) == DONE)) break;
        __builtin_amdgcn_s_sleep(3);
        asm volatile("buffer_load_dwordx4 %0, %3, %6, 0 offen sc1\n\tbuffer_load_dwordx4 %1, %4, %6, 0 offen sc1\n\t"
                     "buffer_load_dwordx4 %2, %5, %6, 0 offen sc1\n\ts_waitcnt vmcnt(0)"
                     : "=&v"(k[0]), "=&v"(k[1]), "=&v"(k[2]) : "v"(ka[0]), "v"(ka[1]), "v"(ka[2]), "s"(wsl) : "memory");
      }
    }
  }
  pq_piece(2); pq_piece(3);
  const int gi = lane >> 4, blk = gi & 1, q = (lane & 15) >> 2, p4 = lane & 3;
  const lptr kvb = lds + L_KVZ + (t < 16 ? t : 16) * 32 + h * 16 + hq * 544;
  const lptr pqb = lds + L_PQ + wr * 32768 + lane * 16 + hq * 1024;
  lptr wob[2];
#pragma unroll
  for (int c = 0; c < 2; ++c)
    wob[c] = lds + L_WO + (4 * h + q) * 128 + (((2 * ((2 * c + blk + hq) & 3) + (p4 >> 1)) ^ ((q >> 1) << 2)) << 4) + (p4 & 1) * 8 + hq * 2048;
  lptr bb = lds + L_BT + (hq * 16 + 4 * h + q) * 128 + (((4 * wr + 2 * blk + (p4 >> 1)) ^ ((q >> 1) << 2)) << 4) + (p4 & 1) * 8;
  unsigned dst = trow * 1024 + ct * 128 + hq * 32 + h * 16;
  PIN(dst); PIN(bb);
  float gt;
  u32x4 bchk;
  bool b_ok;
  f32x16 acc[2] = {}, nd[2];
  bf16x8 wf[2][2], kf[2], pf[2];
  lgkm0();
#pragma unroll
  for (int n = 0; n < 10; ++n) {
    const int a = n - 1, b = n - 2;
    if (n == 0) { WAIT_VM(2); bar(); SB; }
    if (n == 2) { WAIT_VM(4); bar(); SB; }
    if (n == 4) { WAIT_VM(7); bar(); SB; }
    if (n == 6) { lgkm0(); WAIT_VM(5); bar(); SB; }
    if (a >= 0 && a < 8) { const f32x16 zero = {}; nd[a & 1] = mfma32(kf[a & 1], pf[a & 1], zero); SB; }
    if (n < 8) { kf[n & 1] = lds_b128(kvb + n * 2176); pf[n & 1] = lds_b128(pqb + n * 4096); SB; }
    if (n < 2) { pq_piece(2 * n + 4); SB; }
    if (n == 2) { dma16c(wsl, bo, 0, lds + L_BT + wv * 1024); SB; }
    if (n == 3) { gp[0] = ld32c(wsl, go); gp[1] = ld32c(wsl, go + 8192); SB; }
    if (b >= 0) {
      f32x16& v = nd[b & 1];
      PIN(v);
      const float r = __builtin_amdgcn_rcpf(v[8] + 1e-6f);
      float o[8];
#pragma unroll
      for (int e = 0; e < 8; ++e) { o[e] = v[e] * r; PIN(o[e]); }
      const u32x4 p = {pk_bf16(o[0], o[1]), pk_bf16(o[2], o[3]), pk_bf16(o[4], o[5]), pk_bf16(o[6], o[7])};
      acc[1] = mfma32(wf[b & 1][1], __builtin_bit_cast(bf16x8, p), acc[1]);
      acc[0] = mfma32(wf[b & 1][0], __builtin_bit_cast(bf16x8, p), acc[0]);
      SB;
    }
    if (a >= 0 && a < 8) {
#pragma unroll
      for (int c = 0; c < 2; ++c) wf[a & 1][c] = lds_tr2(wob[c] + a * 8192, wob[c] + a * 8192 + 1024);
      SB;
    }
    if (n < 2) { pq_piece(2 * n + 5); SB; }
    if (n == 3) { gp[2] = ld32c(wsl, go + 16384); gp[3] = ld32c(wsl, go + 24576); SB; }
    if (n == 8) {
      WAIT_VM(0);
      PIN(gp[0]); PIN(gp[1]); PIN(gp[2]); PIN(gp[3]);
      bchk = *(const volatile LAS u32x4*)(lds + L_BT + wv * 1024 + lane * 16);
      const unsigned gs = __float_as_uint((__uint_as_float(gp[0]) + __uint_as_float(gp[1])) + (__uint_as_float(gp[2]) + __uint_as_float(gp[3])));
      const auto gw2 = __builtin_amdgcn_permlane32_swap(gs, gs, false, false);
      gt = __builtin_amdgcn_rcpf(1.f + __builtin_amdgcn_exp2f(-(__uint_as_float(gw2[0]) + __uint_as_float(gw2[1])) * LOG2E));
      PIN(gt);
      SB;
    }
    if (n == 9) { b_ok = b_arm && __all(umax4(0, bchk) != EMPTY); SB; }
  }
  PIN(acc[0]); PIN(acc[1]);
#pragma unroll 1
  for (unsigned spin = 0; spin < SPIN_CAP && !b_ok; ++spin) {
    __builtin_amdgcn_s_sleep(2);
    if (!b_arm) { const u32x2 f = poll64(wsl, fo); b_arm = (__ballot(f.x != tag.x || f.y != tag.y) & 0xffff00ull) == 0; }
    dma16c(wsl, bo, 0, lds + L_BT + wv * 1024);
    WAIT_VM(0);
    b_ok = b_arm && __all(umax4(0, *(const volatile LAS u32x4*)(lds + L_BT + wv * 1024 + lane * 16)) != EMPTY);
  }
  {
    const lptr xb = lds + L_PQ + wr * 32768 + lane * 16;
#pragma unroll
    for (int p = 3; p >= 1; --p) {
      const f32x16& v = acc[p >> 1];
      LAS f32x4* xw = (LAS f32x4*)(xb + (((hq + p) & 3) * 3 + p - 1) * 2048);
#pragma unroll
      for (int G = 0; G < 2; ++G) { const int i = 8 * (p & 1) + 4 * G; xw[64 * G] = f32x4{v[i], v[i + 1], v[i + 2], v[i + 3]}; }
    }
    const LAS f32x4* xr = (const LAS f32x4*)(xb + hq * 6144);
    lgkm0();
    bar();
    const u32x2 bv0 = __builtin_bit_cast(u32x2, lds_tr(bb)), bv1 = __builtin_bit_cast(u32x2, lds_tr(bb + 1024));
    u32x2 o[2];
#pragma unroll
    for (int G = 0; G < 2; ++G) {
      const unsigned b0 = G ? bv1.x : bv0.x, b1 = G ? bv1.y : bv0.y;
      const f32x4 bf = {bf2f(b0), bfhi(b0), bf2f(b1), bfhi(b1)};
      const f32x4 own = {acc[0][4 * G], acc[0][4 * G + 1], acc[0][4 * G + 2], acc[0][4 * G + 3]};
      const f32x4 r = (((own + xr[64 * G]) + (xr[64 * G + 128] + xr[64 * G + 256])) - bf) * gt + bf;
      o[G] = u32x2{pk_bf16(r[0], r[1]), pk_bf16(r[2], r[3])};
    }
    auto sx = __builtin_amdgcn_permlane32_swap(o[0].x, o[1].x, false, false);
    auto sy = __builtin_amdgcn_permlane32_swap(o[0].y, o[1].y, false, false);
    const u32x4 v = {sx[0], sy[0], sx[1], sy[1]};
    __builtin_amdgcn_raw_buffer_store_b128(v, make_rsrc(out, 2048 * 1024), dst, 0, 0);
  }
  if (blockIdx.x == 0 && tid == 0) st64(make_rsrc(ws, WS_USED), WS_EPOCH, tag);
}

__global__ void __launch_bounds__(512, 2) fused(const u16* __restrict__ x, const u16* __restrict__ wqkv, const u16* __restrict__ gw,
                                                const u16* __restrict__ lar, const u16* __restrict__ aim, const u16* __restrict__ cre,
                                                const u16* __restrict__ cim, const u16* __restrict__ ldt, const u16* __restrict__ dsk,
                                                const u16* __restrict__ wo, char* ws, u16* __restrict__ out
) {
  extern __shared__ __attribute__((aligned(16))) char lds_[];
  const lptr lds = (lptr)lds_;
  const int b = blockIdx.x, xcd = b & 7, idx = b >> 3, tid = threadIdx.x, lane = tid & 63;
  const int wv = __builtin_amdgcn_readfirstlane(tid >> 6);
  if (wv == 7 && idx < 16) CODE_PREFETCH("fused", lds + L_DUMP, ((idx & 7) * 64 + lane) * 64);
  asm volatile("" :: "s"(x), "s"(wqkv), "s"(gw), "s"(lar), "s"(aim), "s"(cre), "s"(cim), "s"(ldt), "s"(dsk), "s"(wo), "s"(ws), "s"(out));
  const int rt = (xcd >> 1) * 8 + (idx & 7), ct = (xcd & 1) * 4 + (idx >> 3);
  const Out o = {mk_rsrc(wo, 512 * 1024), (unsigned)(ct * 128 + (tid >> 3) * 1024 + (((tid & 7) ^ (((tid >> 4) & 1) << 2)) << 4)),
                 wv, lds};
  u32x2 tag;
  if (idx < 16) {
    tag = conv_role(lds, x, lar, aim, cre, cim, ldt, dsk, ws, xcd * 64 + idx * 4, idx, o);
  } else {
    const int rl = idx & 3, cl = (idx >> 2) & 3;
    tag = gemm_role(lds, x, wqkv, gw, ws, (xcd >> 1) * 4 + rl, (xcd & 1) * 4 + cl, 2 * ((rl + cl) & 3), o);
  }
  out_phase(lds, ws, out, rt, ct, tag);
  CODE_END("fused");
}

extern "C" void kernel_launch(void* const* d_in, const int* in_sizes, int n_in, void* d_out, int out_size, void* d_ws,
                              size_t ws_size, hipStream_t stream) {
  const u16* x = (const u16*)d_in[0];
  const u16* wqkv = (const u16*)d_in[1];
  const u16* wo = (const u16*)d_in[2];
  const u16* lar = (const u16*)d_in[4];
  const u16* aim = (const u16*)d_in[5];
  const u16* cre = (const u16*)d_in[6];
  const u16* cim = (const u16*)d_in[7];
  const u16* ldt = (const u16*)d_in[8];
  const u16* dsk = (const u16*)d_in[9];
  const u16* gw = (const u16*)d_in[10];
  char* ws = (char*)d_ws;
  (void)hipFuncSetAttribute((const void*)fused, hipFuncAttributeMaxDynamicSharedMemorySize, 163840);
  fused<<<256, 512, 163840, stream>>>(x, wqkv, gw, lar, aim, cre, cim, ldt, dsk, wo, ws, (u16*)d_out);
}
```
